```python
import torch

def _softmax(x, axis=-1):
    return torch.softmax(x, dim=axis)

def _gelu(x):
    return torch.nn.functional.gelu(x, approximate='tanh')

def _silu(x):
    return torch.nn.functional.silu(x)

def _sigmoid(x):
    return torch.sigmoid(x)

def _layernorm(x, axis=-1, eps=1e-05):
    m = x.mean(dim=axis, keepdim=True)
    v = x.var(dim=axis, keepdim=True, unbiased=False)
    return (x - m) / torch.sqrt(v + eps)

def _scatter_add(tgt, idx, src, axis=0):
    return tgt.clone().index_add_(axis, idx, src)

def _segment_sum(src, seg, n):
    out = torch.zeros((n,) + src.shape[1:], dtype=src.dtype)
    return out.index_add_(0, seg, src)

def _pad(x, pw):
    flat = tuple((v for pair in reversed(pw) for v in pair))
    return torch.nn.functional.pad(x, flat)
kernel_name = 'adaLN_cond__sliding_block_cached__glu_gated_reglu__prompt'

def setup_inputs():

    _w = lambda a: (lambda t: t.to(torch.bfloat16) if t.is_floating_point() else t)(torch.as_tensor(a))

    def _A_setup():
        B, Sq, D = (1, 2048, 576)
        Hq, Hkv, Sk, Dh = (32, 16, 2048, 128)
        assert Hq % Hkv == 0, (Hq, Hkv)
        return {'x': _w((lambda *s: torch.randn(*s, dtype=torch.bfloat16) * 0.05)(B, Sq, D)), 'k_cache': _w((lambda *s: torch.randn(*s, dtype=torch.bfloat16) * 1.6)(B, Hkv, Sk, Dh)), 'v_cache': _w((lambda *s: torch.randn(*s, dtype=torch.bfloat16) * 0.8)(B, Hkv, Sk, Dh)), 'wq': _w((lambda *s: torch.randn(*s, dtype=torch.bfloat16) * 1.6)(D, Hq * Dh) * D ** (-0.5)), 'wo': _w((lambda *s: torch.randn(*s, dtype=torch.bfloat16) * 0.8)(Hq * Dh, D) * (Hq * Dh) ** (-0.5))}
    _A_in = _A_setup()

    def _B_setup():
        B, S, D, F = (1, 2048, 576, 4608)
        return {'x': _w((lambda *s: torch.randn(*s, dtype=torch.bfloat16) * 0.05)(B, S, D)), 'W_gate': _w((lambda *s: torch.randn(*s, dtype=torch.bfloat16) * 0.4)(D, F) * (2.0 / D) ** 0.5), 'W_up': _w((lambda *s: torch.randn(*s, dtype=torch.bfloat16) * 0.4)(D, F) * (2.0 / D) ** 0.5), 'W_down': _w((lambda *s: torch.randn(*s, dtype=torch.bfloat16) * 0.4)(F, D) * (1.0 / F) ** 0.5)}
    _B_in = _B_setup()
    return {'A_x': _A_in['x'], 'A_k_cache': _A_in['k_cache'], 'A_v_cache': _A_in['v_cache'], 'A_wq': _A_in['wq'], 'A_wo': _A_in['wo'], 'B_x': _B_in['x'], 'B_W_gate': _B_in['W_gate'], 'B_W_up': _B_in['W_up'], 'B_W_down': _B_in['W_down'], 'c': _w((lambda *s: torch.randn(*s, dtype=torch.bfloat16) * 0.2)(1, 576) * 8.0), 'w_mod': _w((lambda *s: torch.randn(*s, dtype=torch.bfloat16) * 0.2)(576, 6 * 576) * 576 ** (-0.5) * 8.0)}

def reference(A_x, A_k_cache, A_v_cache, A_wq, A_wo, B_x, B_W_gate, B_W_up, B_W_down, c, w_mod):

    def _A_ref(x, k_cache, v_cache, wq, wo):
        B, Sq, D = x.shape
        Hq, Dh = (32, 128)
        Hkv, Sk = (k_cache.shape[1], k_cache.shape[2])
        W = min(4096, max(256, Sk // 4))
        rep = Hq // Hkv
        q = (lambda x, *d: x.permute(*d))((x @ wq).reshape(B, Sq, Hq, Dh), 0, 2, 1, 3)
        k = (lambda x, n, axis: torch.repeat_interleave(x, n, dim=axis))(k_cache[:, :, Sk - W:, :], rep, axis=1)
        v = (lambda x, n, axis: torch.repeat_interleave(x, n, dim=axis))(v_cache[:, :, Sk - W:, :], rep, axis=1)
        scores = torch.einsum('bhqd,bhkd->bhqk', q, k) * Dh ** (-0.5)
        p = _softmax(scores, axis=-1)
        o = torch.einsum('bhqk,bhkd->bhqd', p, v)
        return (lambda x, *d: x.permute(*d))(o, 0, 2, 1, 3).reshape(B, Sq, Hq * Dh) @ wo

    def _B_ref(x, W_gate, W_up, W_down):
        gate = torch.einsum('bsd,df->bsf', x, W_gate)
        up = torch.einsum('bsd,df->bsf', x, W_up)
        gate = gate * (lambda x, dt: x.to(dt))(gate > 0, gate.dtype)
        h = gate * up
        return torch.einsum('bsf,fd->bsd', h, W_down)
    x = A_x
    mod = (_silu(c) @ w_mod).reshape(c.shape[0], 6, 1, -1)
    sa, ca, ga, sb, cb, gb = (mod[:, 0].clone(), mod[:, 1].clone(), mod[:, 2].clone(), mod[:, 3].clone(), mod[:, 4].clone(), mod[:, 5].clone())
    h = _layernorm(x, axis=-1) * (1 + ca) + sa
    x = x + ga * _A_ref(h, A_k_cache, A_v_cache, A_wq, A_wo)
    h = _layernorm(x, axis=-1) * (1 + cb) + sb
    x = x + gb * _B_ref(h, B_W_gate, B_W_up, B_W_down)
    return x
```

```cpp
#include <hip/hip_runtime.h>
#include <hip/hip_bf16.h>
typedef unsigned short u16;
__device__ __forceinline__ float b2f(u16 x){ return __uint_as_float(((unsigned)x)<<16); }
__device__ __forceinline__ u16 f2b(float f){ __hip_bfloat16 h = __float2bfloat16(f); return *reinterpret_cast<u16*>(&h); }
constexpr int S=2048, D=576, HQ=32, HKV=16, SK=2048, DH=128, W=512, F=4608;

__global__ void k_mod(const u16* c, const u16* wmod, float* mod){
  int n = blockIdx.x*blockDim.x+threadIdx.x; if(n>=6*D) return;
  float acc=0; for(int k=0;k<D;k++){ float cv=b2f(c[k]); float s=cv/(1.f+__expf(-cv)); s=b2f(f2b(s)); acc+=s*b2f(wmod[k*6*D+n]); }
  mod[n]=b2f(f2b(acc));
}
__global__ void k_ln(const float* x, const float* mod, int so, int co, u16* h){
  int row=blockIdx.x; __shared__ float red[256]; int t=threadIdx.x;
  float s=0; for(int i=t;i<D;i+=256) s+=x[row*D+i]; red[t]=s; __syncthreads();
  for(int o=128;o>0;o>>=1){ if(t<o) red[t]+=red[t+o]; __syncthreads(); }
  float m=red[0]/D; __syncthreads();
  s=0; for(int i=t;i<D;i+=256){ float d=x[row*D+i]-m; s+=d*d;} red[t]=s; __syncthreads();
  for(int o=128;o>0;o>>=1){ if(t<o) red[t]+=red[t+o]; __syncthreads(); }
  float r=rsqrtf(red[0]/D+1e-5f);
  for(int i=t;i<D;i+=256) h[row*D+i]=f2b((x[row*D+i]-m)*r*(1.f+mod[co*D+i])+mod[so*D+i]);
}
__global__ void k_cvt(const u16* x, float* y, int n){ int i=blockIdx.x*blockDim.x+threadIdx.x; if(i<n) y[i]=b2f(x[i]); }
__global__ void k_gemm(const u16* A, const u16* B, float* C, int M, int N, int K){
  __shared__ float As[64][17], Bs[16][65];
  int tx=threadIdx.x&15, ty=threadIdx.x>>4; int m0=blockIdx.y*64, n0=blockIdx.x*64;
  float acc[4][4]={};
  for(int k0=0;k0<K;k0+=16){
    for(int i=threadIdx.x;i<64*16;i+=256){ int r=i>>4,c=i&15; As[r][c]=b2f(A[(long)(m0+r)*K+k0+c]); }
    for(int i=threadIdx.x;i<64*16;i+=256){ int r=i>>6,c=i&63; Bs[r][c]=b2f(B[(long)(k0+r)*N+n0+c]); }
    __syncthreads();
    for(int k=0;k<16;k++){ float a[4],b[4]; for(int i=0;i<4;i++){a[i]=As[ty*4+i][k]; b[i]=Bs[k][tx*4+i];}
      for(int i=0;i<4;i++)for(int j=0;j<4;j++)acc[i][j]+=a[i]*b[j]; }
    __syncthreads();
  }
  for(int i=0;i<4;i++)for(int j=0;j<4;j++) C[(long)(m0+ty*4+i)*N+n0+tx*4+j]=acc[i][j];
}
__global__ void k_attn(const float* Q, const u16* Kc, const u16* Vc, u16* O){
  int row=blockIdx.x, h=blockIdx.y, kv=h/2, t=threadIdx.x;
  __shared__ float q[DH], p[W], red[256];
  if(t<DH) q[t]=b2f(f2b(Q[(long)row*HQ*DH+h*DH+t])); __syncthreads();
  const u16* Kb=Kc+((long)kv*SK+SK-W)*DH; const u16* Vb=Vc+((long)kv*SK+SK-W)*DH;
  float mx=-1e30f;
  for(int j=t;j<W;j+=256){ float s=0; for(int d=0;d<DH;d++) s+=q[d]*b2f(Kb[j*DH+d]); s*=0.08838834764f; p[j]=s; mx=fmaxf(mx,s);}
  red[t]=mx; __syncthreads(); for(int o=128;o>0;o>>=1){ if(t<o) red[t]=fmaxf(red[t],red[t+o]); __syncthreads(); }
  mx=red[0]; __syncthreads();
  float sm=0; for(int j=t;j<W;j+=256){ float e=__expf(p[j]-mx); p[j]=e; sm+=e; }
  red[t]=sm; __syncthreads(); for(int o=128;o>0;o>>=1){ if(t<o) red[t]+=red[t+o]; __syncthreads(); }
  sm=red[0];
  if(t<DH){ float a=0; for(int j=0;j<W;j++) a+=p[j]*b2f(Vb[j*DH+t]); O[(long)row*HQ*DH+h*DH+t]=f2b(a/sm); }
}
__global__ void k_res(float* x, const float* y, const float* mod, int go, u16* out, int n){ int i=blockIdx.x*blockDim.x+threadIdx.x; if(i<n){ float v=x[i]+mod[go*D+i%D]*y[i]; x[i]=v; if(out) out[i]=f2b(v);} }
__global__ void k_glu(const float* g, const float* u, u16* h, int n){ int i=blockIdx.x*blockDim.x+threadIdx.x; if(i<n) h[i]=f2b(fmaxf(g[i],0.f)*u[i]); }

extern "C" void kernel_launch(void* const* d_in, const int* in_sizes, int n_in, void* d_out, int out_size, void* d_ws, size_t ws_size, hipStream_t stream){
  const u16 *x=(const u16*)d_in[0], *kc=(const u16*)d_in[1], *vc=(const u16*)d_in[2], *wq=(const u16*)d_in[3], *wo=(const u16*)d_in[4];
  const u16 *wg=(const u16*)d_in[6], *wu=(const u16*)d_in[7], *wd=(const u16*)d_in[8], *c=(const u16*)d_in[9], *wm=(const u16*)d_in[10];
  char* ws=(char*)d_ws;
  float* mod=(float*)ws; ws+=16384;
  float* xf=(float*)ws; ws+=S*D*4;
  u16* h=(u16*)ws; ws+=S*D*2;
  float* q=(float*)ws; ws+=(size_t)S*4608*4;
  float* u=(float*)ws; ws+=(size_t)S*4608*4;
  u16* o=(u16*)ws; ws+=(size_t)S*4608*2;
  float* y=(float*)ws; ws+=S*D*4;
  k_mod<<<(6*D+63)/64,64,0,stream>>>(c,wm,mod);
  k_cvt<<<S*D/256,256,0,stream>>>(x,xf,S*D);
  k_ln<<<S,256,0,stream>>>(xf,mod,0,1,h);
  k_gemm<<<dim3(4096/64,S/64),256,0,stream>>>(h,wq,q,S,4096,D);
  k_attn<<<dim3(S,HQ),256,0,stream>>>(q,kc,vc,o);
  k_gemm<<<dim3(D/64,S/64),256,0,stream>>>(o,wo,y,S,D,4096);
  k_res<<<S*D/256,256,0,stream>>>(xf,y,mod,2,nullptr,S*D);
  k_ln<<<S,256,0,stream>>>(xf,mod,3,4,h);
  k_gemm<<<dim3(F/64,S/64),256,0,stream>>>(h,wg,q,S,F,D);
  k_gemm<<<dim3(F/64,S/64),256,0,stream>>>(h,wu,u,S,F,D);
  k_glu<<<S*F/256,256,0,stream>>>(q,u,o,S*F);
  k_gemm<<<dim3(D/64,S/64),256,0,stream>>>(o,wd,y,S,D,F);
  k_res<<<S*D/256,256,0,stream>>>(xf,y,mod,5,(u16*)d_out,S*D);
}
```

```cpp
#include <hip/hip_runtime.h>
#include <hip/hip_bf16.h>
constexpr int S_ = 2048, D_ = 576;

#define ASM_CLOB "v1","v2","v3","v4","v5","v6","v7","v8","v9","v10","v11","v12","v13","v14","v15","v16","v17","v18","v19","v20","v21","v22","v23","v24","v25","v26","v27","v28","v29","v30","v31","v32", \
  "v33","v34","v35","v36","v37","v38","v39","v40","v41","v42","v43","v44","v45","v46","v47","v48","v49","v50","v51","v52","v53","v54","v55","v56","v57","v58","v59","v60","v61","v62","v63","v64", \
  "v65","v66","v67","v68","v69","v70","v71","v72","v73","v74","v75","v76","v77","v78","v79","v80","v81","v82","v83","v84","v85","v86","v87","v88","v89","v90","v91","v92","v93","v94","v95","v96", \
  "v97","v98","v99","v100","v101","v102","v103","v104","v105","v106","v107","v108","v109","v110","v111","v112","v113","v114","v115","v116","v117","v118","v119","v120","v121","v122","v123","v124","v125","v126","v127","v128", \
  "v129","v130","v131","v132","v133","v134","v135","v136","v137","v138","v139","v140","v141","v142","v143","v144","v145","v146","v147","v148","v149","v150","v151","v152","v153","v154","v155","v156","v157","v158","v159","v160", \
  "v161","v162","v163","v164","v165","v166","v167","v168","v169","v170","v171","v172","v173","v174","v175","v176","v177","v178","v179","v180","v181","v182","v183","v184","v185","v186","v187","v188","v189","v190","v191","v192", \
  "v193","v194","v195","v196","v197","v198","v199","v200","v201","v202","v203","v204","v205","v206","v207","v208","v209","v210","v211","v212","v213","v214","v215","v216","v217","v218","v219","v220","v221","v222","v223","v224", \
  "v225","v226","v227","v228","v229","v230","v231","v232","v233","v234","v235","v236","v237","v238","v239","v240","v241","v242","v243","v244","v245","v246","v247","v248","v249","v250","v251","v252","v253","v254","v255","a0", \
  "a1","a2","a3","a4","a5","a6","a7","a8","a9","a10","a11","a12","a13","a14","a15","a16","a17","a18","a19","a20","a21","a22","a23","a24","a25","a26","a27","a28","a29","a30","a31","a32", \
  "a33","a34","a35","a36","a37","a38","a39","a40","a41","a42","a43","a44","a45","a46","a47","a48","a49","a50","a51","a52","a53","a54","a55","a56","a57","a58","a59","a60","a61","a62","a63","a64", \
  "a65","a66","a67","a68","a69","a70","a71","a72","a73","a74","a75","a76","a77","a78","a79","a80","a81","a82","a83","a84","a85","a86","a87","a88","a89","a90","a91","a92","a93","a94","a95","a96", \
  "a97","a98","a99","a100","a101","a102","a103","a104","a105","a106","a107","a108","a109","a110","a111","a112","a113","a114","a115","a116","a117","a118","a119","a120","a121","a122","a123","a124","a125","a126","a127","a128", \
  "a129","a130","a131","a132","a133","a134","a135","a136","a137","a138","a139","a140","a141","a142","a143","a144","a145","a146","a147","a148","a149","a150","a151","a152","a153","a154","a155","a156","a157","a158","a159","a160", \
  "a161","a162","a163","a164","a165","a166","a167","a168","a169","a170","a171","a172","a173","a174","a175","a176","a177","a178","a179","a180","a181","a182","a183","a184","a185","a186","a187","a188","a189","a190","a191","a192", \
  "a193","a194","a195","a196","a197","a198","a199","a200","a201","a202","a203","a204","a205","a206","a207","a208","a209","a210","a211","a212","a213","a214","a215","a216","a217","a218","a219","a220","a221","a222","a223","a224", \
  "a225","a226","a227","a228","a229","a230","a231","a232","a233","a234","a235","a236","a237","a238","a239","a240","a241","a242","a243","a244","a245","a246","a247","a248","a249","a250","a251","a252","a253","a254","a255","s4", \
  "s5","s6","s7","s8","s9","s10","s11","s12","s13","s14","s15","s16","s17","s18","s19","s20","s21","s22","s23","s24","s25","s26","s27","s28","s29","s30","s31","s34","s35","s36","s37","s38", \
  "s39","s40","s41","s42","s43","s44","s45","s46","s47","s48","s49","s50","s51","s52","s53","s54","s55","s56","s57","s58","s59","s60","s61","s62","s63","s64","s65","s66","s67","s68","s69","s70", \
  "s71","s72","s73","s74","s75","s76","s77","s78","s79","s80","s81","s82","s83","s84","s85","s86","s87","s88","s89","s90","s91","s92","s93","s94","s95","s96","s97","s98","s99","vcc","scc","memory"

__global__ __launch_bounds__(256, 1) void kmain(const void* xh, const void* modv, const void* wq, const void* kc, const void* vc, const void* wo,
                                                void* p1, void* dbg, const void* wg, const void* wu, const void* wd, const void* x,
                                                void* h2, void* p2, void* out, void* flags, const void* c, const void* wmod, void* modp) {
  __shared__ __attribute__((aligned(16))) char lds[163840];
  asm volatile(R"ASM(
s_load_dwordx2 s[12:13], %0, 0x78
s_load_dwordx2 s[24:25], %0, 0x88
s_load_dwordx2 s[28:29], %0, 0x80
s_load_dwordx2 s[16:17], %0, 0x58
s_load_dwordx2 s[36:37], %0, 0x10
s_load_dwordx2 s[20:21], %0, 0x0
s_load_dwordx2 s[96:97], %0, 0x90
s_load_dwordx2 s[64:65], %0, 0x0
s_load_dwordx2 s[40:41], %0, 0x18
s_load_dwordx2 s[44:45], %0, 0x20
s_load_dwordx2 s[48:49], %0, 0x28
s_load_dwordx2 s[68:69], %0, 0x30
s_load_dwordx2 s[76:77], %0, 0x60
v_and_b32 v1, 63, %1
v_lshrrev_b32 v13, 6, %1
s_nop 0
v_readfirstlane_b32 s52, v13
v_lshlrev_b32 v2, 4, v1
s_and_b32 s56, %2, 7
s_lshr_b32 s57, %2, 3
s_getpc_b64 s[72:73]
.Lcode0:
s_and_b32 s73, s73, 0xffff
s_mov_b32 s74, .Lend-.Lcode0
s_mov_b32 s75, 0x20000
s_lshl_b32 s58, s57, 2
s_add_u32 s58, s58, s52
s_lshl_b32 s58, s58, 10
v_and_b32 v13, 7, v1
v_lshlrev_b32 v13, 7, v13
buffer_load_dword v255, v13, s[72:75], s58 offen
s_mul_i32 s58, s57, 0x1c72
s_lshr_b32 s58, s58, 16
s_mul_i32 s58, s58, 9
s_sub_u32 s62, s57, s58
s_lshl_b32 s60, s52, 1
s_cmp_ge_u32 s52, 2
s_addc_u32 s60, s60, 1
v_lshrrev_b32 v13, 3, v1
v_and_b32 v14, 7, v1
v_lshlrev_b32 v72, 5, v13
v_add_u32 v72, 0x26000, v72
s_lshl_b32 s58, s60, 8
v_add_u32 v71, s58, v72
v_subrev_u32 v70, 0x100, v71
v_add_u32 v72, 0x400, v72
s_mov_b32 s58, 0x480
v_lshlrev_b32 v15, 4, v13
v_mad_u32_u24 v69, v14, s58, v15
s_lshl_b32 s58, s60, 7
v_add_u32 v68, s58, v69
v_subrev_u32 v67, 0x80, v68
s_cmp_eq_u32 s52, 0
s_cselect_b32 s58, 0, 0x40000000
s_add_u32 s58, s58, 0x200
v_add_u32 v69, s58, v69
v_lshlrev_b32 v15, 9, v13
v_lshl_add_u32 v66, v14, 4, v15
s_lshl_b32 s58, s60, 12
v_add_u32 v65, s58, v66
v_subrev_u32 v64, 0x1000, v65
s_cmp_eq_u32 s52, 0
s_cselect_b32 s58, 0, 0x40000000
s_add_u32 s58, s58, 0x4000
v_add_u32 v66, s58, v66
s_lshl_b32 s58, s52, 2
s_add_u32 s58, s58, 0x27800
v_lshl_add_u32 v73, v14, 4, s58
v_lshrrev_b32 v15, 5, v1
v_lshl_add_u32 v73, v15, 7, v73
s_mov_b32 s58, 0x27800
v_lshl_add_u32 v74, v14, 4, s58
v_mov_b32 v77, 0x3ae38e39
v_mov_b32 v75, 0
v_lshlrev_b32 v80, 12, v1
v_add_u32 v81, 0x600, v80
s_lshl_b32 s58, s57, 12
s_sub_u32 s58, s58, 0x2000
v_lshl_add_u32 v82, v1, 8, s58
v_cmp_gt_u32 vcc, 32, v1
s_nop 1
v_cndmask_b32 v81, v82, v81, vcc
v_lshrrev_b32 v83, 3, %1
v_and_b32 v84, 7, v1
v_lshlrev_b32 v84, 4, v84
s_mov_b32 s58, 0x1b00
v_mad_u32_u24 v78, v83, s58, v84
v_lshlrev_b32 v79, 1, v83
s_waitcnt lgkmcnt(0)
s_lshl_b32 s58, s57, 3
s_add_u32 s58, s58, s56
s_mul_i32 s58, s58, 0x2400
s_add_u32 s16, s16, s58
s_addc_u32 s17, s17, 0
s_and_b32 s17, s17, 0xffff
s_sub_u32 s18, 0x240000, s58
s_mov_b32 s19, 0x20000
s_lshr_b32 s58, s56, 2
s_lshl_b32 s59, s57, 1
s_add_u32 s58, s58, s59
s_mul_i32 s58, s58, 0x9000
s_and_b32 s59, s56, 3
s_lshl_b32 s59, s59, 7
s_add_u32 s58, s58, s59
s_add_u32 s20, s20, s58
s_addc_u32 s21, s21, 0
s_and_b32 s21, s21, 0xffff
s_sub_u32 s22, 0x240000, s58
s_mov_b32 s23, 0x20000
buffer_load_dwordx4 v[88:91], v67, s[16:19], 0 offen
buffer_load_dwordx4 v[92:95], v68, s[16:19], 0 offen
buffer_load_dwordx4 v[96:99], v69, s[16:19], 0 offen
s_mov_b32 s58, 0
s_add_u32 s12, s12, s58
s_addc_u32 s13, s13, 0
s_and_b32 s13, s13, 0xffff
s_sub_u32 s14, 0x20000, s58
s_mov_b32 s15, 0x20000
buffer_load_dword v76, v81, s[12:15], 0 offen sc1
s_and_b32 s81, %2, 3
s_lshr_b32 s82, %2, 2
s_mul_i32 s58, s81, 0xf3000
s_lshl_b32 s59, s82, 7
s_add_u32 s58, s58, s59
s_add_u32 s24, s24, s58
s_addc_u32 s25, s25, 0
s_and_b32 s25, s25, 0xffff
s_sub_u32 s26, 0, s58
s_mov_b32 s27, 0x20000
s_mul_i32 s58, s81, 0x120
s_add_u32 s28, s28, s58
s_addc_u32 s29, s29, 0
s_and_b32 s29, s29, 0xffff
s_sub_u32 s30, 0, s58
s_mov_b32 s31, 0x20000
s_cmp_lt_u32 %2, 0xd8
s_cselect_b32 s26, 0xf1580, 0
s_cselect_b32 s30, 0x120, 0
buffer_load_ushort v172, v79, s[28:31], 0 offen offset:0
buffer_load_ushort v173, v79, s[28:31], 0 offen offset:64
buffer_load_ushort v174, v79, s[28:31], 0 offen offset:128
buffer_load_ushort v175, v79, s[28:31], 0 offen offset:192
buffer_load_ushort v176, v79, s[28:31], 0 offen offset:256
s_mov_b32 s58, 0
buffer_load_dwordx4 v[152:155], v78, s[24:27], s58 offen
s_mov_b32 s58, 0x36000
buffer_load_dwordx4 v[156:159], v78, s[24:27], s58 offen
s_mov_b32 s58, 0x6c000
buffer_load_dwordx4 v[160:163], v78, s[24:27], s58 offen
s_mov_b32 s58, 0xa2000
buffer_load_dwordx4 v[164:167], v78, s[24:27], s58 offen
s_mov_b32 s58, 0xd8000
buffer_load_dwordx4 v[168:171], v78, s[24:27], s58 offen
s_lshl_b32 s58, s56, 2
s_add_u32 s58, s58, s52
s_lshl_b32 s58, s58, 8
s_add_u32 s36, s36, s58
s_addc_u32 s37, s37, 0
s_sub_u32 s36, s36, 0x1000
s_subb_u32 s37, s37, 0
s_and_b32 s37, s37, 0xffff
s_sub_u32 s38, 0x481000, s58
s_mov_b32 s39, 0x20000
s_mov_b32 s75, 0
s_mov_b32 s78, 0x480000
s_mul_i32 s72, s62, 0x80000
s_lshl_b32 s53, s52, 12
s_add_u32 s63, s53, 0x20000
s_mov_b32 s58, 0x2000
v_bfe_u32 v13, v1, 4, 1
v_lshlrev_b32 v13, 2, v13
v_and_b32 v14, 7, v1
v_xor_b32 v13, v13, v14
v_lshlrev_b32 v13, 4, v13
v_lshrrev_b32 v14, 3, v1
v_mad_u32_u24 v15, v14, s58, v13
v_add_u32 v16, 0x1000, v15
v_add_u32 v17, 0x10c00, v15
v_add_u32 v18, 0x21000, v15
v_add_u32 v19, 0x30c00, v15
v_add_u32 v20, 0x41000, v15
v_add_u32 v21, 0x50c00, v15
v_add_u32 v22, 0x61000, v15
v_add_u32 v23, 0x70c00, v15
v_add_u32 v15, 0x80, v15
v_add_u32 v118, 0x800, v15
v_add_u32 v119, 0x10400, v15
v_add_u32 v120, 0x20800, v15
v_add_u32 v121, 0x30400, v15
v_add_u32 v122, 0x40800, v15
v_add_u32 v123, 0x50400, v15
v_add_u32 v124, 0x60800, v15
v_add_u32 v125, 0x70400, v15
v_bfe_u32 v13, v1, 4, 1
v_lshlrev_b32 v13, 5, v13
v_and_b32 v14, 1, v1
v_lshl_add_u32 v13, v14, 4, v13
v_bfe_u32 v14, v1, 1, 1
v_lshl_add_u32 v13, v14, 3, v13
v_bfe_u32 v14, v1, 2, 2
)ASM"
R"ASM(
v_lshrrev_b32 v15, 5, v1
v_lshl_add_u32 v3, v14, 7, v13
v_lshl_add_u32 v3, v15, 10, v3
v_bfe_u32 v14, v1, 3, 1
v_lshl_add_u32 v3, v14, 6, v3
v_xor_b32 v4, 64, v3
s_lshr_b32 s58, s52, 1
s_lshl_b32 s58, s58, 13
v_add_u32 v56, s58, v3
v_add_u32 v58, 0x8000, v56
v_add_u32 v57, s58, v4
v_add_u32 v59, 0x8000, v57
v_add_u32 v3, s53, v3
v_add_u32 v4, s53, v4
v_and_b32 v13, 19, v1
v_bfe_u32 v14, v1, 2, 1
v_lshl_add_u32 v13, v14, 3, v13
v_bfe_u32 v14, v1, 3, 1
v_lshl_add_u32 v13, v14, 2, v13
v_bfe_u32 v14, v13, 1, 3
v_lshrrev_b32 v15, 5, v1
v_xor_b32 v14, v14, v15
s_add_u32 s58, s58, 0x10000
v_lshl_add_u32 v13, v13, 7, s58
v_xor_b32 v15, 0, v14
v_lshl_add_u32 v48, v15, 4, v13
v_add_u32 v52, 0x8000, v48
v_xor_b32 v15, 2, v14
v_lshl_add_u32 v49, v15, 4, v13
v_add_u32 v53, 0x8000, v49
v_xor_b32 v15, 4, v14
v_lshl_add_u32 v50, v15, 4, v13
v_add_u32 v54, 0x8000, v50
v_xor_b32 v15, 6, v14
v_lshl_add_u32 v51, v15, 4, v13
v_add_u32 v55, 0x8000, v51
v_lshrrev_b32 v13, 3, v1
v_lshlrev_b32 v13, 8, v13
v_add_u32 v13, 0x1000, v13
v_and_b32 v14, 7, v1
v_lshrrev_b32 v15, 4, v1
v_xor_b32 v15, v15, v14
v_lshl_add_u32 v32, v15, 4, v13
v_xor_b32 v33, 64, v32
v_add_u32 v33, 0x400, v33
v_add_u32 v34, 0x800, v32
v_add_u32 v35, 0x800, v33
v_bfe_u32 v15, v1, 4, 1
v_lshlrev_b32 v15, 2, v15
v_xor_b32 v15, v15, v14
v_lshl_add_u32 v36, v15, 4, v13
v_add_u32 v37, 0x400, v36
v_add_u32 v38, 0x800, v36
v_add_u32 v39, 0xc00, v36
s_lshr_b32 s58, s52, 1
s_lshl_b32 s59, s56, 1
s_add_u32 s58, s58, s59
s_lshl_b32 s58, s58, 19
s_add_u32 s58, s58, 0x60000
s_and_b32 s59, s52, 1
s_lshl_b32 s59, s59, 7
s_add_u32 s58, s58, s59
s_add_u32 s40, s40, s58
s_addc_u32 s41, s41, 0
s_sub_u32 s40, s40, 0x1000
s_subb_u32 s41, s41, 0
s_and_b32 s41, s41, 0xffff
s_sub_u32 s42, 0x801000, s58
s_mov_b32 s43, 0x20000
s_add_u32 s44, s44, s58
s_addc_u32 s45, s45, 0
s_sub_u32 s44, s44, 0x1000
s_subb_u32 s45, s45, 0
s_and_b32 s45, s45, 0xffff
s_sub_u32 s46, 0x801000, s58
s_mov_b32 s47, 0x20000
s_waitcnt vmcnt(13)
v_lshlrev_b32 v128, 16, v88
v_and_b32 v129, 0xffff0000, v88
v_lshlrev_b32 v130, 16, v89
v_and_b32 v131, 0xffff0000, v89
v_lshlrev_b32 v132, 16, v90
v_and_b32 v133, 0xffff0000, v90
v_lshlrev_b32 v134, 16, v91
v_and_b32 v135, 0xffff0000, v91
s_waitcnt vmcnt(12)
v_lshlrev_b32 v136, 16, v92
v_and_b32 v137, 0xffff0000, v92
v_lshlrev_b32 v138, 16, v93
v_and_b32 v139, 0xffff0000, v93
v_lshlrev_b32 v140, 16, v94
v_and_b32 v141, 0xffff0000, v94
v_lshlrev_b32 v142, 16, v95
v_and_b32 v143, 0xffff0000, v95
s_waitcnt vmcnt(11)
v_lshlrev_b32 v144, 16, v96
v_and_b32 v145, 0xffff0000, v96
v_lshlrev_b32 v146, 16, v97
v_and_b32 v147, 0xffff0000, v97
v_lshlrev_b32 v148, 16, v98
v_and_b32 v149, 0xffff0000, v98
v_lshlrev_b32 v150, 16, v99
v_and_b32 v151, 0xffff0000, v99
v_add_f32 v13, v128, v129
v_mul_f32 v15, v128, v128
v_mul_f32 v82, v129, v129
v_add_f32 v81, v130, v131
v_fmac_f32 v15, v130, v130
v_fmac_f32 v82, v131, v131
v_fmac_f32 v15, v132, v132
v_fmac_f32 v82, v133, v133
v_add_f32 v13, v13, v132
v_add_f32 v81, v81, v133
v_fmac_f32 v15, v134, v134
v_fmac_f32 v82, v135, v135
v_add_f32 v13, v13, v134
v_add_f32 v81, v81, v135
v_fmac_f32 v15, v136, v136
v_fmac_f32 v82, v137, v137
v_add_f32 v13, v13, v136
v_add_f32 v81, v81, v137
v_fmac_f32 v15, v138, v138
v_fmac_f32 v82, v139, v139
v_add_f32 v13, v13, v138
v_add_f32 v81, v81, v139
v_fmac_f32 v15, v140, v140
v_fmac_f32 v82, v141, v141
v_add_f32 v13, v13, v140
v_add_f32 v81, v81, v141
v_fmac_f32 v15, v142, v142
v_fmac_f32 v82, v143, v143
v_add_f32 v13, v13, v142
v_add_f32 v81, v81, v143
v_fmac_f32 v15, v144, v144
v_fmac_f32 v82, v145, v145
v_add_f32 v13, v13, v144
v_add_f32 v81, v81, v145
v_fmac_f32 v15, v146, v146
v_fmac_f32 v82, v147, v147
v_add_f32 v13, v13, v146
v_add_f32 v81, v81, v147
v_fmac_f32 v15, v148, v148
v_fmac_f32 v82, v149, v149
v_add_f32 v13, v13, v148
v_add_f32 v81, v81, v149
v_fmac_f32 v15, v150, v150
v_fmac_f32 v82, v151, v151
v_add_f32 v13, v13, v150
v_add_f32 v81, v81, v151
v_add_f32 v13, v13, v81
v_add_f32 v14, v15, v82
s_nop 1
v_permlane32_swap_b32 v13, v14
v_add_f32 v13, v13, v14
s_nop 1
v_add_f32_dpp v13, v13, v13 row_ror:8 row_mask:0xf bank_mask:0xf
v_mov_b32 v14, v13
s_nop 1
v_permlane16_swap_b32 v13, v14
v_add_f32 v13, v13, v14
ds_write_b32 v73, v13 offset:0
s_waitcnt lgkmcnt(0)
s_barrier
ds_read_b128 v[112:115], v74 offset:0
ds_read_b128 v[188:191], v74 offset:128
s_waitcnt lgkmcnt(1)
v_add_f32 v13, v112, v113
v_add_f32 v14, v114, v115
v_add_f32 v13, v13, v14
v_mul_f32 v81, 0x3ae38e39, v13
s_waitcnt lgkmcnt(0)
v_add_f32 v13, v188, v189
v_add_f32 v14, v190, v191
v_add_f32 v13, v13, v14
v_mul_f32 v14, v81, v81
v_fma_f32 v13, v77, v13, -v14
v_max_f32 v13, 0, v13
v_add_f32 v13, 0x3727c5ac, v13
v_rsq_f32 v82, v13
v_sub_f32 v128, v128, v81
v_mul_f32 v128, v128, v82
v_sub_f32 v129, v129, v81
v_mul_f32 v129, v129, v82
v_sub_f32 v130, v130, v81
v_mul_f32 v130, v130, v82
v_sub_f32 v131, v131, v81
v_mul_f32 v131, v131, v82
v_sub_f32 v132, v132, v81
v_mul_f32 v132, v132, v82
v_sub_f32 v133, v133, v81
v_mul_f32 v133, v133, v82
v_sub_f32 v134, v134, v81
v_mul_f32 v134, v134, v82
v_sub_f32 v135, v135, v81
v_mul_f32 v135, v135, v82
v_cvt_pk_f16_f32 v100, v128, v129
v_cvt_pk_f16_f32 v101, v130, v131
v_cvt_pk_f16_f32 v102, v132, v133
v_cvt_pk_f16_f32 v103, v134, v135
buffer_store_dwordx4 v[100:103], v64, s[20:23], 0 offen sc1
)ASM"
R"ASM(
v_sub_f32 v136, v136, v81
v_mul_f32 v136, v136, v82
v_sub_f32 v137, v137, v81
v_mul_f32 v137, v137, v82
v_sub_f32 v138, v138, v81
v_mul_f32 v138, v138, v82
v_sub_f32 v139, v139, v81
v_mul_f32 v139, v139, v82
v_sub_f32 v140, v140, v81
v_mul_f32 v140, v140, v82
v_sub_f32 v141, v141, v81
v_mul_f32 v141, v141, v82
v_sub_f32 v142, v142, v81
v_mul_f32 v142, v142, v82
v_sub_f32 v143, v143, v81
v_mul_f32 v143, v143, v82
v_cvt_pk_f16_f32 v104, v136, v137
v_cvt_pk_f16_f32 v105, v138, v139
v_cvt_pk_f16_f32 v106, v140, v141
v_cvt_pk_f16_f32 v107, v142, v143
buffer_store_dwordx4 v[104:107], v65, s[20:23], 0 offen sc1
v_sub_f32 v144, v144, v81
v_mul_f32 v144, v144, v82
v_sub_f32 v145, v145, v81
v_mul_f32 v145, v145, v82
v_sub_f32 v146, v146, v81
v_mul_f32 v146, v146, v82
v_sub_f32 v147, v147, v81
v_mul_f32 v147, v147, v82
v_sub_f32 v148, v148, v81
v_mul_f32 v148, v148, v82
v_sub_f32 v149, v149, v81
v_mul_f32 v149, v149, v82
v_sub_f32 v150, v150, v81
v_mul_f32 v150, v150, v82
v_sub_f32 v151, v151, v81
v_mul_f32 v151, v151, v82
v_cvt_pk_f16_f32 v108, v144, v145
v_cvt_pk_f16_f32 v109, v146, v147
v_cvt_pk_f16_f32 v110, v148, v149
v_cvt_pk_f16_f32 v111, v150, v151
buffer_store_dwordx4 v[108:111], v66, s[20:23], 0 offen sc1
s_waitcnt vmcnt(12)
v_lshlrev_b32 v172, 16, v172
v_mul_f32 v81, 0xbfb8aa3b, v172
v_exp_f32 v81, v81
s_nop 0
v_add_f32 v81, 1.0, v81
v_rcp_f32 v81, v81
s_nop 0
v_mul_f32 v172, v172, v81
v_cvt_pk_bf16_f32 v172, v172, v172
v_and_b32 v172, 0xffff0000, v172
s_waitcnt vmcnt(11)
v_lshlrev_b32 v173, 16, v173
v_mul_f32 v81, 0xbfb8aa3b, v173
v_exp_f32 v81, v81
s_nop 0
v_add_f32 v81, 1.0, v81
v_rcp_f32 v81, v81
s_nop 0
v_mul_f32 v173, v173, v81
v_cvt_pk_bf16_f32 v173, v173, v173
v_and_b32 v173, 0xffff0000, v173
s_waitcnt vmcnt(10)
v_lshlrev_b32 v174, 16, v174
v_mul_f32 v81, 0xbfb8aa3b, v174
v_exp_f32 v81, v81
s_nop 0
v_add_f32 v81, 1.0, v81
v_rcp_f32 v81, v81
s_nop 0
v_mul_f32 v174, v174, v81
v_cvt_pk_bf16_f32 v174, v174, v174
v_and_b32 v174, 0xffff0000, v174
s_waitcnt vmcnt(9)
v_lshlrev_b32 v175, 16, v175
v_mul_f32 v81, 0xbfb8aa3b, v175
v_exp_f32 v81, v81
s_nop 0
v_add_f32 v81, 1.0, v81
v_rcp_f32 v81, v81
s_nop 0
v_mul_f32 v175, v175, v81
v_cvt_pk_bf16_f32 v175, v175, v175
v_and_b32 v175, 0xffff0000, v175
s_waitcnt vmcnt(8)
v_lshlrev_b32 v176, 16, v176
v_mul_f32 v81, 0xbfb8aa3b, v176
v_exp_f32 v81, v81
s_nop 0
v_add_f32 v81, 1.0, v81
v_rcp_f32 v81, v81
s_nop 0
v_mul_f32 v176, v176, v81
v_cvt_pk_bf16_f32 v176, v176, v176
v_and_b32 v176, 0xffff0000, v176
s_waitcnt vmcnt(7)
v_lshlrev_b32 v81, 16, v152
v_and_b32 v82, 0xffff0000, v152
v_mul_f32 v178, v172, v81
v_mul_f32 v179, v172, v82
v_lshlrev_b32 v83, 16, v153
v_and_b32 v84, 0xffff0000, v153
v_mul_f32 v180, v172, v83
v_mul_f32 v181, v172, v84
v_lshlrev_b32 v81, 16, v154
v_and_b32 v82, 0xffff0000, v154
v_mul_f32 v182, v172, v81
v_mul_f32 v183, v172, v82
v_lshlrev_b32 v83, 16, v155
v_and_b32 v84, 0xffff0000, v155
v_mul_f32 v184, v172, v83
v_mul_f32 v185, v172, v84
s_waitcnt vmcnt(6)
v_lshlrev_b32 v81, 16, v156
v_and_b32 v82, 0xffff0000, v156
v_fmac_f32 v178, v173, v81
v_fmac_f32 v179, v173, v82
v_lshlrev_b32 v83, 16, v157
v_and_b32 v84, 0xffff0000, v157
v_fmac_f32 v180, v173, v83
v_fmac_f32 v181, v173, v84
v_lshlrev_b32 v81, 16, v158
v_and_b32 v82, 0xffff0000, v158
v_fmac_f32 v182, v173, v81
v_fmac_f32 v183, v173, v82
v_lshlrev_b32 v83, 16, v159
v_and_b32 v84, 0xffff0000, v159
v_fmac_f32 v184, v173, v83
v_fmac_f32 v185, v173, v84
s_waitcnt vmcnt(5)
v_lshlrev_b32 v81, 16, v160
v_and_b32 v82, 0xffff0000, v160
v_fmac_f32 v178, v174, v81
v_fmac_f32 v179, v174, v82
v_lshlrev_b32 v83, 16, v161
v_and_b32 v84, 0xffff0000, v161
v_fmac_f32 v180, v174, v83
v_fmac_f32 v181, v174, v84
v_lshlrev_b32 v81, 16, v162
v_and_b32 v82, 0xffff0000, v162
v_fmac_f32 v182, v174, v81
v_fmac_f32 v183, v174, v82
v_lshlrev_b32 v83, 16, v163
v_and_b32 v84, 0xffff0000, v163
v_fmac_f32 v184, v174, v83
v_fmac_f32 v185, v174, v84
s_waitcnt vmcnt(4)
v_lshlrev_b32 v81, 16, v164
v_and_b32 v82, 0xffff0000, v164
v_fmac_f32 v178, v175, v81
v_fmac_f32 v179, v175, v82
v_lshlrev_b32 v83, 16, v165
v_and_b32 v84, 0xffff0000, v165
v_fmac_f32 v180, v175, v83
v_fmac_f32 v181, v175, v84
v_lshlrev_b32 v81, 16, v166
v_and_b32 v82, 0xffff0000, v166
v_fmac_f32 v182, v175, v81
v_fmac_f32 v183, v175, v82
v_lshlrev_b32 v83, 16, v167
v_and_b32 v84, 0xffff0000, v167
v_fmac_f32 v184, v175, v83
v_fmac_f32 v185, v175, v84
s_waitcnt vmcnt(3)
v_lshlrev_b32 v81, 16, v168
v_and_b32 v82, 0xffff0000, v168
v_fmac_f32 v178, v176, v81
v_fmac_f32 v179, v176, v82
v_lshlrev_b32 v83, 16, v169
v_and_b32 v84, 0xffff0000, v169
v_fmac_f32 v180, v176, v83
v_fmac_f32 v181, v176, v84
v_lshlrev_b32 v81, 16, v170
v_and_b32 v82, 0xffff0000, v170
v_fmac_f32 v182, v176, v81
v_fmac_f32 v183, v176, v82
v_lshlrev_b32 v83, 16, v171
v_and_b32 v84, 0xffff0000, v171
v_fmac_f32 v184, v176, v83
v_fmac_f32 v185, v176, v84
v_permlane32_swap_b32 v178, v182
v_permlane32_swap_b32 v179, v183
v_permlane32_swap_b32 v180, v184
v_permlane32_swap_b32 v181, v185
v_add_f32 v178, v178, v182
v_add_f32 v179, v179, v183
v_add_f32 v180, v180, v184
v_add_f32 v181, v181, v185
s_nop 0
v_permlane16_swap_b32 v178, v180
v_permlane16_swap_b32 v179, v181
v_add_f32 v178, v178, v180
v_add_f32 v179, v179, v181
s_nop 0
v_add_f32_dpp v178, v178, v178 row_ror:8 row_mask:0xf bank_mask:0xf
v_add_f32_dpp v179, v179, v179 row_ror:8 row_mask:0xf bank_mask:0xf
v_and_b32 v81, 7, v1
v_lshrrev_b32 v82, 4, v1
)ASM"
R"ASM(
v_lshlrev_b32 v82, 3, v82
v_lshl_add_u32 v81, v81, 5, v82
s_lshl_b32 s58, s52, 8
s_add_u32 s58, s58, 0x27400
v_add_u32 v81, s58, v81
ds_write_b64 v81, v[178:179]
v_readlane_b32 s4, v76, 32
v_readlane_b32 s5, v76, 33
v_readlane_b32 s6, v76, 34
v_readlane_b32 s7, v76, 35
v_readlane_b32 s8, v76, 36
v_readlane_b32 s9, v76, 37
v_readlane_b32 s10, v76, 38
v_add_u32 v76, 1, v76
v_lshlrev_b32 v79, 2, v1
v_add_u32 v85, 0x27400, v79
s_nop 0
s_add_u32 s11, s10, 1
s_mov_b32 s58, 0
s_add_u32 s96, s96, s58
s_addc_u32 s97, s97, 0
s_and_b32 s97, s97, 0xffff
s_sub_u32 s98, 0xd800, s58
s_mov_b32 s99, 0x20000
v_cmp_gt_u32 vcc, 32, v1
v_mov_b32 v86, 0x40000000
s_nop 0
v_cndmask_b32 v80, v86, v80, vcc
s_mov_b32 s24, s12
s_mov_b32 s25, s13
s_mov_b32 s26, s14
s_mov_b32 s27, s15
s_lshl_b32 s58, s57, 12
s_add_u32 s12, s12, s58
s_addc_u32 s13, s13, 0
s_mov_b32 s14, 0x1000
v_accvgpr_write_b32 a252, v76
v_accvgpr_write_b32 a253, v80
s_load_dwordx2 s[16:17], %0, 0x8
s_waitcnt vmcnt(0) lgkmcnt(0)
s_barrier
s_cmp_lg_u32 s52, 3
s_cbranch_scc1 .Lk1_pub_skip
v_mov_b32 v81, s11
v_lshlrev_b32 v82, 16, v1
s_lshl_b32 s58, s56, 2
buffer_store_dword v81, v82, s[12:15], s58 offen offset:2912 sc1
ds_read_b32 v152, v85 offset:0
ds_read_b32 v153, v85 offset:256
ds_read_b32 v154, v85 offset:512
ds_read_b32 v155, v85 offset:768
s_mul_i32 s58, s81, 0x3600
s_lshl_b32 s59, s82, 8
s_add_u32 s58, s58, s59
s_cmp_lt_u32 %2, 0xd8
s_cselect_b32 s58, s58, 0x40000000
s_waitcnt lgkmcnt(2)
v_add_f32 v152, v152, v153
s_waitcnt lgkmcnt(0)
v_add_f32 v154, v154, v155
v_add_f32 v152, v152, v154
buffer_store_dword v152, v79, s[96:99], s58 offen sc1
s_waitcnt vmcnt(0)
s_lshl_b32 s58, %2, 2
s_cmp_lt_u32 %2, 0xd8
s_cselect_b32 s58, s58, 0x40000000
buffer_store_dword v76, v80, s[24:27], s58 offen offset:2048 sc1
.Lk1_pub_skip:
s_lshr_b32 s85, s72, 7
s_mov_b32 s28, s96
s_mov_b32 s29, s97
s_mov_b32 s30, s98
s_mov_b32 s31, s99
s_add_u32 s34, s53, 0x10000
s_add_u32 s35, s34, 0x8000
s_mov_b32 s87, s53
s_add_u32 s96, s53, 0x8000
s_mul_i32 s58, s56, 0x90000
s_add_u32 s48, s48, s58
s_addc_u32 s49, s49, 0
s_sub_u32 s48, s48, 0x1000
s_subb_u32 s49, s49, 0
s_and_b32 s49, s49, 0xffff
s_sub_u32 s50, 0x481000, s58
s_mov_b32 s51, 0x20000
s_lshl_b32 s60, s52, 8
s_cmp_ge_u32 s52, 2
s_cselect_b32 s58, 0x80, 0
s_add_u32 s60, s60, s58
s_and_b32 s62, s57, 7
s_add_u32 s76, s60, 0
s_add_u32 s79, s76, 0x90000
s_mul_i32 s58, s62, 0x12000
s_add_u32 s73, s76, s58
s_add_u32 s77, s60, 0x80
s_add_u32 s80, s77, 0x90000
s_mul_i32 s58, s62, 0x12000
s_add_u32 s74, s77, s58
s_mov_b32 s97, s73
s_lshl_b32 s86, s62, 13
s_add_u32 s86, s86, 0x10000
s_sub_u32 s59, 0x200, s60
v_bfe_u32 v13, v1, 4, 1
v_lshlrev_b32 v13, 1, v13
v_and_b32 v14, 7, v1
v_xor_b32 v13, v13, v14
v_lshlrev_b32 v13, 4, v13
s_mov_b32 s58, 0x480
v_lshrrev_b32 v14, 3, v1
v_mad_u32_u24 v81, v14, s58, v13
v_xor_b32 v13, 64, v13
v_mad_u32_u24 v82, v14, s58, v13
s_and_b32 s58, s52, 1
s_cselect_b64 vcc, -1, 0
v_cndmask_b32 v83, v81, v82, vcc
s_mul_i32 s58, s52, 0x2400
s_add_u32 s58, s58, s59
v_add_u32 v83, s58, v83
v_add_u32 v24, 0x1000, v81
v_add_u32 v25, 0x3000, v82
v_add_u32 v26, 0x5000, v81
v_add_u32 v27, 0x7000, v82
v_add_u32 v28, 0xa000, v81
v_add_u32 v29, 0xc000, v82
v_add_u32 v30, 0xe000, v81
v_add_u32 v31, 0x10000, v82
v_add_u32 v40, 0x1000, v83
v_add_u32 v41, 0xa000, v83
v_and_b32 v15, 3, v1
v_lshlrev_b32 v15, 3, v15
v_bfe_u32 v13, v1, 2, 2
v_lshl_add_u32 v15, v13, 7, v15
v_lshrrev_b32 v13, 4, v1
v_lshl_add_u32 v15, v13, 10, v15
v_bfe_u32 v13, v1, 3, 2
v_lshl_add_u32 v15, v13, 5, v15
s_and_b32 s58, s52, 2
s_lshl_b32 s58, s58, 5
v_xor_b32 v46, s58, v15
v_xor_b32 v47, 32, v46
v_add_u32 v42, s53, v15
v_xor_b32 v43, 32, v42
v_xor_b32 v44, 64, v42
v_xor_b32 v45, 0x60, v42
s_and_b32 s58, s52, 1
s_lshl_b32 s58, s58, 11
v_add_u32 v10, s58, v2
v_xor_b32 v11, 0x800, v10
s_lshl_b32 s61, s52, 10
v_and_b32 v13, 15, v1
v_lshlrev_b32 v13, 4, v13
v_bfe_u32 v14, v1, 4, 1
v_lshl_add_u32 v13, v14, 10, v13
v_lshrrev_b32 v14, 5, v1
v_lshl_add_u32 v13, v14, 8, v13
s_lshl_b32 s58, s52, 14
s_add_u32 s58, s58, 0x10000
v_add_u32 v12, s58, v13
v_mov_b32 v62, 0xff800000
v_mov_b32 v63, 0xff800000
s_mov_b32 s55, 0x20000
v_add_u32 v9, s55, v2
v_add_u32 v126, 0x8c00, v2
s_lshl_b32 s58, s56, 5
s_add_u32 s58, s58, s57
s_mul_i32 s58, s58, 0x12000
s_add_u32 s68, s68, s58
s_addc_u32 s69, s69, 0
s_and_b32 s69, s69, 0xffff
s_sub_u32 s70, 0x1200000, s58
s_mov_b32 s71, 0x20000
s_mul_i32 s58, s57, 0x12000
s_add_u32 s64, s64, s58
s_addc_u32 s65, s65, 0
s_and_b32 s65, s65, 0xffff
s_sub_u32 s66, 0x240000, s58
s_mov_b32 s67, 0x20000
s_lshl_b32 s84, s52, 10
s_add_u32 s83, s84, 0x9000
s_add_u32 s81, s85, s84
s_lshr_b32 s20, s85, 4
s_add_u32 s20, s20, 0x26000
s_lshl_b32 s82, s52, 11
s_add_u32 s82, s82, 0x20000
s_mov_b32 s85, 0
v_mov_b32 v176, 0
v_mov_b32 v177, 0
v_mov_b32 v178, 0
v_mov_b32 v179, 0
v_mov_b32 v180, 0
v_mov_b32 v181, 0
v_mov_b32 v182, 0
v_mov_b32 v183, 0
v_mov_b32 v184, 0
v_mov_b32 v185, 0
v_mov_b32 v186, 0
v_mov_b32 v187, 0
v_mov_b32 v188, 0
v_mov_b32 v189, 0
v_mov_b32 v190, 0
)ASM"
R"ASM(
v_mov_b32 v191, 0
v_mov_b32 v192, 0
v_mov_b32 v193, 0
v_mov_b32 v194, 0
v_mov_b32 v195, 0
v_mov_b32 v196, 0
v_mov_b32 v197, 0
v_mov_b32 v198, 0
v_mov_b32 v199, 0
v_mov_b32 v200, 0
v_mov_b32 v201, 0
v_mov_b32 v202, 0
v_mov_b32 v203, 0
v_mov_b32 v204, 0
v_mov_b32 v205, 0
v_mov_b32 v206, 0
v_mov_b32 v207, 0
v_mov_b32 v208, 0
v_mov_b32 v209, 0
v_mov_b32 v210, 0
v_mov_b32 v211, 0
v_mov_b32 v212, 0
v_mov_b32 v213, 0
v_mov_b32 v214, 0
v_mov_b32 v215, 0
v_mov_b32 v216, 0
v_mov_b32 v217, 0
v_mov_b32 v218, 0
v_mov_b32 v219, 0
v_mov_b32 v220, 0
v_mov_b32 v221, 0
v_mov_b32 v222, 0
v_mov_b32 v223, 0
v_mov_b32 v224, 0
v_mov_b32 v225, 0
v_mov_b32 v226, 0
v_mov_b32 v227, 0
v_mov_b32 v228, 0
v_mov_b32 v229, 0
v_mov_b32 v230, 0
v_mov_b32 v231, 0
v_mov_b32 v232, 0
v_mov_b32 v233, 0
v_mov_b32 v234, 0
v_mov_b32 v235, 0
v_mov_b32 v236, 0
v_mov_b32 v237, 0
v_mov_b32 v238, 0
v_mov_b32 v239, 0
v_mov_b32 v240, 0
v_mov_b32 v241, 0
v_mov_b32 v242, 0
v_mov_b32 v243, 0
v_mov_b32 v244, 0
v_mov_b32 v245, 0
v_mov_b32 v246, 0
v_mov_b32 v247, 0
v_mov_b32 v248, 0
v_mov_b32 v249, 0
v_mov_b32 v250, 0
v_mov_b32 v251, 0
v_mov_b32 v252, 0
v_mov_b32 v253, 0
v_mov_b32 v254, 0
v_mov_b32 v255, 0
v_lshrrev_b32 v116, 5, v1
v_lshlrev_b32 v116, 5, v116
v_add_u32 v127, s20, v116
v_lshlrev_b32 v83, 4, %1
v_add_u32 v84, 0x26000, v83
v_cmp_lt_u32 vcc, 0x8f, %1
v_mov_b32 v86, 1.0
s_nop 0
v_cndmask_b32 v85, v75, v86, vcc
v_cmp_gt_u32 vcc, 32, %1
v_mov_b32 v81, 0x40000000
s_nop 0
v_cndmask_b32 v81, v81, v83, vcc
v_mov_b32 v82, 0x26240
v_cndmask_b32 v82, v82, v84, vcc
s_sub_u32 s59, %2, 0xc6
s_lshl_b32 s62, s59, 8
s_sub_u32 s59, %2, 0xd8
s_cmp_lt_u32 s59, 36
s_cselect_b32 s62, s62, 0x40000000
s_cmp_eq_u32 s52, 3
s_cselect_b32 s62, s62, 0x40000000
s_mov_b32 s58, 0
s_add_u32 s16, s16, s58
s_addc_u32 s17, s17, 0
s_and_b32 s17, s17, 0xffff
s_sub_u32 s18, 0x3600, s58
s_mov_b32 s19, 0x20000
s_cmp_eq_u32 s52, 3
s_cbranch_scc1 .Lk1_fa
s_add_u32 m0, s53, 0
s_nop 0
buffer_load_dwordx4 v16, s[36:39], s72 offen lds
buffer_load_dwordx4 v17, s[36:39], s72 offen offset:1024 lds
buffer_load_dwordx4 v118, s[36:39], s72 offen offset:2048 lds
buffer_load_dwordx4 v119, s[36:39], s72 offen offset:3072 lds
s_add_u32 m0, s53, 0x4000
s_nop 0
buffer_load_dwordx4 v18, s[36:39], s72 offen lds
buffer_load_dwordx4 v19, s[36:39], s72 offen offset:1024 lds
buffer_load_dwordx4 v120, s[36:39], s72 offen offset:2048 lds
buffer_load_dwordx4 v121, s[36:39], s72 offen offset:3072 lds
s_add_u32 m0, s53, 0x8000
s_nop 0
buffer_load_dwordx4 v20, s[36:39], s72 offen lds
buffer_load_dwordx4 v21, s[36:39], s72 offen offset:1024 lds
buffer_load_dwordx4 v122, s[36:39], s72 offen offset:2048 lds
buffer_load_dwordx4 v123, s[36:39], s72 offen offset:3072 lds
s_add_u32 m0, s53, 0xc000
s_nop 0
buffer_load_dwordx4 v22, s[36:39], s72 offen lds
buffer_load_dwordx4 v23, s[36:39], s72 offen offset:1024 lds
buffer_load_dwordx4 v124, s[36:39], s72 offen offset:2048 lds
s_lshl_b32 s58, s52, 14
s_add_u32 m0, s58, 0x3000
s_mul_i32 s58, s52, 0x20000
s_add_u32 s58, s58, s72
s_sub_u32 s59, 3, s52
s_lshl_b32 s59, s59, 8
s_add_u32 s58, s58, s59
buffer_load_dwordx4 v16, s[36:39], s58 offen lds
buffer_load_dwordx4 v17, s[36:39], s58 offen offset:1024 lds
buffer_load_dwordx4 v118, s[36:39], s58 offen offset:2048 lds
buffer_load_dwordx4 v119, s[36:39], s58 offen offset:3072 lds
s_mov_b32 s54, 3
s_cmp_lg_u32 s52, 0
s_cbranch_scc1 .Lk1_fax
s_lshl_b32 s58, s54, 14
s_add_u32 m0, s58, 0x3000
s_mul_i32 s58, s54, 0x20000
s_add_u32 s58, s58, s72
s_sub_u32 s59, 3, s52
s_lshl_b32 s59, s59, 8
s_add_u32 s58, s58, s59
buffer_load_dwordx4 v16, s[36:39], s58 offen lds
buffer_load_dwordx4 v17, s[36:39], s58 offen offset:1024 lds
buffer_load_dwordx4 v118, s[36:39], s58 offen offset:2048 lds
buffer_load_dwordx4 v119, s[36:39], s58 offen offset:3072 lds
.Lk1_fax:
.Lk1_fa:
s_cmp_lg_u32 s52, 3
s_cbranch_scc1 .Lk1_g_skip
s_cmp_lt_u32 %2, 0xd8
s_cselect_b32 s98, 0x3ffff, -1
s_cselect_b32 s99, 0, 0x3fffff
s_or_b32 s99, s99, 0xc00000
s_mov_b32 s54, 0x20000
.Lk1_h1_p:
buffer_load_dwordx4 v[112:115], v2, s[12:15], 0 offen offset:2048 sc1
s_waitcnt vmcnt(0)
v_xor_b32 v112, s11, v112
v_xor_b32 v113, s11, v113
v_xor_b32 v114, s11, v114
v_xor_b32 v115, s11, v115
v_or_b32 v112, v112, v113
v_or_b32 v112, v112, v114
v_or_b32 v112, v112, v115
v_cmp_ne_u32 vcc, 0, v112
s_nop 3
s_and_b64 vcc, vcc, s[98:99]
s_cmp_eq_u64 vcc, 0
s_cbranch_scc1 .Lk1_h1_d
s_sleep 1
s_add_u32 s54, s54, -1
s_cbranch_scc1 .Lk1_h1_p
.Lk1_h1_d:
.Lk1_g_skip:
s_barrier
s_mov_b32 s58, 0
buffer_load_dwordx4 v[128:131], v83, s[28:31], s58 offen sc1
s_mov_b32 s58, 0x3600
buffer_load_dwordx4 v[132:135], v83, s[28:31], s58 offen sc1
s_mov_b32 s58, 0x6c00
buffer_load_dwordx4 v[136:139], v83, s[28:31], s58 offen sc1
s_mov_b32 s58, 0xa200
buffer_load_dwordx4 v[140:143], v83, s[28:31], s58 offen sc1
s_mov_b32 s58, 0x1000
buffer_load_dwordx4 v[144:147], v81, s[28:31], s58 offen sc1
s_mov_b32 s58, 0x4600
buffer_load_dwordx4 v[148:151], v81, s[28:31], s58 offen sc1
s_mov_b32 s58, 0x7c00
buffer_load_dwordx4 v[152:155], v81, s[28:31], s58 offen sc1
s_mov_b32 s58, 0xb200
buffer_load_dwordx4 v[156:159], v81, s[28:31], s58 offen sc1
s_mov_b32 m0, s82
s_nop 0
buffer_load_dwordx4 v2, s[64:67], s81 offen sc1 lds
buffer_load_dwordx4 v126, s[64:67], s81 offen offset:1024 sc1 lds
s_add_u32 s81, s81, 0x1000
s_cmp_eq_u32 s81, s83
s_cselect_b32 s81, s84, s81
s_add_u32 s82, s82, 0x2000
s_cmp_ge_u32 s82, 0x26000
s_cselect_b32 s58, 0x6000, 0
s_sub_u32 s82, s82, s58
)ASM"
R"ASM(
s_mov_b32 m0, s82
s_nop 0
buffer_load_dwordx4 v2, s[64:67], s81 offen sc1 lds
buffer_load_dwordx4 v126, s[64:67], s81 offen offset:1024 sc1 lds
s_add_u32 s81, s81, 0x1000
s_cmp_eq_u32 s81, s83
s_cselect_b32 s81, s84, s81
s_add_u32 s82, s82, 0x2000
s_cmp_ge_u32 s82, 0x26000
s_cselect_b32 s58, 0x6000, 0
s_sub_u32 s82, s82, s58
s_add_u32 s58, s62, 0
buffer_load_dword v160, v79, s[28:31], s58 offen sc1
s_add_u32 s58, s62, 0x3600
buffer_load_dword v161, v79, s[28:31], s58 offen sc1
s_add_u32 s58, s62, 0x6c00
buffer_load_dword v162, v79, s[28:31], s58 offen sc1
s_add_u32 s58, s62, 0xa200
buffer_load_dword v163, v79, s[28:31], s58 offen sc1
s_waitcnt vmcnt(14)
v_add_f32 v128, v128, v132
s_waitcnt vmcnt(12)
v_add_f32 v136, v136, v140
v_add_f32 v128, v128, v136
v_add_f32 v129, v129, v133
v_add_f32 v137, v137, v141
v_add_f32 v129, v129, v137
v_add_f32 v130, v130, v134
v_add_f32 v138, v138, v142
v_add_f32 v130, v130, v138
v_add_f32 v131, v131, v135
v_add_f32 v139, v139, v143
v_add_f32 v131, v131, v139
v_cvt_pk_bf16_f32 v129, v128, v129
v_lshlrev_b32 v128, 16, v129
v_and_b32 v129, 0xffff0000, v129
v_cvt_pk_bf16_f32 v131, v130, v131
v_lshlrev_b32 v130, 16, v131
v_and_b32 v131, 0xffff0000, v131
v_add_f32 v128, v85, v128
v_add_f32 v129, v85, v129
v_add_f32 v130, v85, v130
v_add_f32 v131, v85, v131
v_cvt_pk_bf16_f32 v129, v128, v129
v_lshlrev_b32 v128, 16, v129
v_and_b32 v129, 0xffff0000, v129
v_cvt_pk_bf16_f32 v131, v130, v131
v_lshlrev_b32 v130, 16, v131
v_and_b32 v131, 0xffff0000, v131
v_mul_f32 v128, 0x3e0293ee, v128
v_mul_f32 v129, 0x3e0293ee, v129
v_mul_f32 v130, 0x3e0293ee, v130
v_mul_f32 v131, 0x3e0293ee, v131
ds_write_b128 v84, v[128:131] offset:0
s_waitcnt vmcnt(10)
v_add_f32 v144, v144, v148
s_waitcnt vmcnt(8)
v_add_f32 v152, v152, v156
v_add_f32 v144, v144, v152
v_add_f32 v145, v145, v149
v_add_f32 v153, v153, v157
v_add_f32 v145, v145, v153
v_add_f32 v146, v146, v150
v_add_f32 v154, v154, v158
v_add_f32 v146, v146, v154
v_add_f32 v147, v147, v151
v_add_f32 v155, v155, v159
v_add_f32 v147, v147, v155
v_cvt_pk_bf16_f32 v145, v144, v145
v_lshlrev_b32 v144, 16, v145
v_and_b32 v145, 0xffff0000, v145
v_cvt_pk_bf16_f32 v147, v146, v147
v_lshlrev_b32 v146, 16, v147
v_and_b32 v147, 0xffff0000, v147
v_add_f32 v144, v86, v144
v_add_f32 v145, v86, v145
v_add_f32 v146, v86, v146
v_add_f32 v147, v86, v147
v_cvt_pk_bf16_f32 v145, v144, v145
v_lshlrev_b32 v144, 16, v145
v_and_b32 v145, 0xffff0000, v145
v_cvt_pk_bf16_f32 v147, v146, v147
v_lshlrev_b32 v146, 16, v147
v_and_b32 v147, 0xffff0000, v147
v_mul_f32 v144, 0x3e0293ee, v144
v_mul_f32 v145, 0x3e0293ee, v145
v_mul_f32 v146, 0x3e0293ee, v146
v_mul_f32 v147, 0x3e0293ee, v147
ds_write_b128 v82, v[144:147] offset:4096
s_sub_u32 s58, %2, 0xea
s_cmp_lt_u32 s58, 9
s_cselect_b32 s58, 1.0, 0
s_waitcnt vmcnt(2)
v_add_f32 v160, v160, v161
s_waitcnt vmcnt(0)
v_add_f32 v162, v162, v163
v_add_f32 v160, v160, v162
v_cvt_pk_bf16_f32 v160, v160, v160
v_and_b32 v160, 0xffff0000, v160
v_add_f32 v160, s58, v160
v_cvt_pk_bf16_f32 v160, v160, v160
v_and_b32 v160, 0xffff0000, v160
buffer_store_dword v160, v79, s[16:19], s62 offen sc1
s_and_b32 s16, s57, 15
s_lshl_b32 s16, s16, 13
s_waitcnt lgkmcnt(0)
s_barrier
ds_read_b128 v[80:83], v9 offset:0
ds_read_b128 v[84:87], v9 offset:1024
ds_read_b128 v[96:99], v127 offset:2304
ds_read_b128 v[104:107], v127 offset:0
ds_read_b128 v[100:103], v127 offset:2320
ds_read_b128 v[108:111], v127 offset:16
ds_read_b128 v[88:91], v9 offset:2048
ds_read_b128 v[92:95], v9 offset:3072
ds_read_b64_tr_b16 a[128:129], v3 offset:0
ds_read_b64_tr_b16 a[130:131], v3 offset:512
ds_read_b64_tr_b16 a[132:133], v4 offset:0
ds_read_b64_tr_b16 a[134:135], v4 offset:512
ds_read_b64_tr_b16 a[136:137], v3 offset:2048
ds_read_b64_tr_b16 a[138:139], v3 offset:2560
ds_read_b64_tr_b16 a[140:141], v4 offset:2048
ds_read_b64_tr_b16 a[142:143], v4 offset:2560
v_mov_b32 v128, 0
v_mov_b32 v129, 0
v_mov_b32 v130, 0
v_mov_b32 v131, 0
v_mov_b32 v132, 0
v_mov_b32 v133, 0
v_mov_b32 v134, 0
v_mov_b32 v135, 0
v_mov_b32 v136, 0
v_mov_b32 v137, 0
v_mov_b32 v138, 0
v_mov_b32 v139, 0
v_mov_b32 v140, 0
v_mov_b32 v141, 0
v_mov_b32 v142, 0
v_mov_b32 v143, 0
v_mov_b32 v144, 0
v_mov_b32 v145, 0
v_mov_b32 v146, 0
v_mov_b32 v147, 0
v_mov_b32 v148, 0
v_mov_b32 v149, 0
v_mov_b32 v150, 0
v_mov_b32 v151, 0
v_mov_b32 v152, 0
v_mov_b32 v153, 0
v_mov_b32 v154, 0
v_mov_b32 v155, 0
v_mov_b32 v156, 0
v_mov_b32 v157, 0
v_mov_b32 v158, 0
v_mov_b32 v159, 0
v_mov_b32 v160, 0
v_mov_b32 v161, 0
v_mov_b32 v162, 0
v_mov_b32 v163, 0
v_mov_b32 v164, 0
v_mov_b32 v165, 0
v_mov_b32 v166, 0
v_mov_b32 v167, 0
v_mov_b32 v168, 0
v_mov_b32 v169, 0
v_mov_b32 v170, 0
v_mov_b32 v171, 0
v_mov_b32 v172, 0
v_mov_b32 v173, 0
v_mov_b32 v174, 0
v_mov_b32 v175, 0
s_waitcnt lgkmcnt(12)
v_fma_mix_f32 v112, v80, v96, v104 op_sel_hi:[1,0,0]
v_fma_mix_f32 v113, v80, v97, v105 op_sel:[1,0,0] op_sel_hi:[1,0,0]
v_fma_mix_f32 v114, v81, v98, v106 op_sel_hi:[1,0,0]
v_fma_mix_f32 v115, v81, v99, v107 op_sel:[1,0,0] op_sel_hi:[1,0,0]
v_cvt_pk_bf16_f32 v64, v112, v113
v_cvt_pk_bf16_f32 v65, v114, v115
v_fma_mix_f32 v112, v84, v96, v104 op_sel_hi:[1,0,0]
v_fma_mix_f32 v113, v84, v97, v105 op_sel:[1,0,0] op_sel_hi:[1,0,0]
v_fma_mix_f32 v114, v85, v98, v106 op_sel_hi:[1,0,0]
v_fma_mix_f32 v115, v85, v99, v107 op_sel:[1,0,0] op_sel_hi:[1,0,0]
v_cvt_pk_bf16_f32 v68, v112, v113
v_cvt_pk_bf16_f32 v69, v114, v115
ds_read_b128 v[96:99], v127 offset:2368
ds_read_b128 v[104:107], v127 offset:64
s_waitcnt lgkmcnt(12)
v_fma_mix_f32 v112, v82, v100, v108 op_sel_hi:[1,0,0]
v_fma_mix_f32 v113, v82, v101, v109 op_sel:[1,0,0] op_sel_hi:[1,0,0]
v_fma_mix_f32 v114, v83, v102, v110 op_sel_hi:[1,0,0]
v_fma_mix_f32 v115, v83, v103, v111 op_sel:[1,0,0] op_sel_hi:[1,0,0]
v_cvt_pk_bf16_f32 v66, v112, v113
v_cvt_pk_bf16_f32 v67, v114, v115
v_fma_mix_f32 v112, v86, v100, v108 op_sel_hi:[1,0,0]
v_fma_mix_f32 v113, v86, v101, v109 op_sel:[1,0,0] op_sel_hi:[1,0,0]
v_fma_mix_f32 v114, v87, v102, v110 op_sel_hi:[1,0,0]
v_fma_mix_f32 v115, v87, v103, v111 op_sel:[1,0,0] op_sel_hi:[1,0,0]
v_cvt_pk_bf16_f32 v70, v112, v113
v_cvt_pk_bf16_f32 v71, v114, v115
ds_read_b128 v[100:103], v127 offset:2384
)ASM"
R"ASM(
ds_read_b128 v[108:111], v127 offset:80
s_waitcnt lgkmcnt(0)
s_add_u32 m0, s53, 0xc000
s_mov_b32 s54, 5
.Lk1_Q:
s_barrier
v_mfma_f32_32x32x16_bf16 v[128:143], a[128:131], v[64:67], v[128:143]
s_waitcnt vmcnt(7)
ds_read_b64_tr_b16 a[144:145], v3 offset:16384
ds_read_b128 v[80:83], v9 offset:4096
ds_read_b128 v[84:87], v9 offset:5120
s_waitcnt lgkmcnt(9)
v_fma_mix_f32 v112, v88, v96, v104 op_sel_hi:[1,0,0]
v_fma_mix_f32 v113, v88, v97, v105 op_sel:[1,0,0] op_sel_hi:[1,0,0]
v_mfma_f32_32x32x16_bf16 v[144:159], a[128:131], v[68:71], v[144:159]
buffer_load_dwordx4 v125, s[36:39], s72 offen offset:3072 lds
ds_read_b64_tr_b16 a[146:147], v3 offset:16896
s_mov_b32 m0, s82
s_nop 0
buffer_load_dwordx4 v2, s[64:67], s81 offen sc1 lds
buffer_load_dwordx4 v126, s[64:67], s81 offen offset:1024 sc1 lds
v_mfma_f32_32x32x16_bf16 v[176:191], a[132:135], v[68:71], v[176:191]
ds_read_b64_tr_b16 a[148:149], v4 offset:16384
v_fma_mix_f32 v114, v89, v98, v106 op_sel_hi:[1,0,0]
v_fma_mix_f32 v115, v89, v99, v107 op_sel:[1,0,0] op_sel_hi:[1,0,0]
v_cvt_pk_bf16_f32 v72, v112, v113
v_cvt_pk_bf16_f32 v73, v114, v115
v_fma_mix_f32 v112, v92, v96, v104 op_sel_hi:[1,0,0]
s_add_u32 s72, s72, 0x80000
s_cmp_eq_u32 s72, s78
s_cselect_b32 s72, s75, s72
s_add_u32 m0, s53, 0
v_mfma_f32_32x32x16_bf16 v[160:175], a[132:135], v[64:67], v[160:175]
buffer_load_dwordx4 v16, s[36:39], s72 offen lds
ds_read_b64_tr_b16 a[150:151], v4 offset:16896
v_fma_mix_f32 v113, v92, v97, v105 op_sel:[1,0,0] op_sel_hi:[1,0,0]
v_fma_mix_f32 v114, v93, v98, v106 op_sel_hi:[1,0,0]
v_fma_mix_f32 v115, v93, v99, v107 op_sel:[1,0,0] op_sel_hi:[1,0,0]
s_waitcnt lgkmcnt(6)
v_mfma_f32_32x32x16_bf16 v[192:207], a[136:139], v[64:67], v[192:207]
ds_read_b64_tr_b16 a[152:153], v3 offset:18432
v_cvt_pk_bf16_f32 v76, v112, v113
v_cvt_pk_bf16_f32 v77, v114, v115
ds_read_b128 v[96:99], v127 offset:2432
ds_read_b128 v[104:107], v127 offset:128
v_fma_mix_f32 v112, v90, v100, v108 op_sel_hi:[1,0,0]
v_mfma_f32_32x32x16_bf16 v[208:223], a[136:139], v[68:71], v[208:223]
buffer_load_dwordx4 v17, s[36:39], s72 offen offset:1024 lds
ds_read_b64_tr_b16 a[154:155], v3 offset:18944
v_fma_mix_f32 v113, v90, v101, v109 op_sel:[1,0,0] op_sel_hi:[1,0,0]
v_fma_mix_f32 v114, v91, v102, v110 op_sel_hi:[1,0,0]
v_fma_mix_f32 v115, v91, v103, v111 op_sel:[1,0,0] op_sel_hi:[1,0,0]
v_cvt_pk_bf16_f32 v74, v112, v113
v_mfma_f32_32x32x16_bf16 v[240:255], a[140:143], v[68:71], v[240:255]
ds_read_b64_tr_b16 a[156:157], v4 offset:18432
v_cvt_pk_bf16_f32 v75, v114, v115
v_fma_mix_f32 v112, v94, v100, v108 op_sel_hi:[1,0,0]
v_fma_mix_f32 v113, v94, v101, v109 op_sel:[1,0,0] op_sel_hi:[1,0,0]
v_fma_mix_f32 v114, v95, v102, v110 op_sel_hi:[1,0,0]
v_fma_mix_f32 v115, v95, v103, v111 op_sel:[1,0,0] op_sel_hi:[1,0,0]
v_mfma_f32_32x32x16_bf16 v[224:239], a[140:143], v[64:67], v[224:239]
buffer_load_dwordx4 v118, s[36:39], s72 offen offset:2048 lds
ds_read_b64_tr_b16 a[158:159], v4 offset:18944
v_cvt_pk_bf16_f32 v78, v112, v113
v_cvt_pk_bf16_f32 v79, v114, v115
ds_read_b128 v[100:103], v127 offset:2448
ds_read_b128 v[108:111], v127 offset:144
s_waitcnt lgkmcnt(8)
v_mfma_f32_32x32x16_bf16 v[128:143], a[144:147], v[72:75], v[128:143]
s_waitcnt vmcnt(9)
ds_read_b64_tr_b16 a[128:129], v3 offset:32768
ds_read_b128 v[88:91], v9 offset:6144
ds_read_b128 v[92:95], v9 offset:7168
s_waitcnt lgkmcnt(8)
v_fma_mix_f32 v112, v80, v96, v104 op_sel_hi:[1,0,0]
v_fma_mix_f32 v113, v80, v97, v105 op_sel:[1,0,0] op_sel_hi:[1,0,0]
v_mfma_f32_32x32x16_bf16 v[144:159], a[144:147], v[76:79], v[144:159]
buffer_load_dwordx4 v119, s[36:39], s72 offen offset:3072 lds
ds_read_b64_tr_b16 a[130:131], v3 offset:33280
s_add_u32 s81, s81, 0x1000
s_cmp_eq_u32 s81, s83
s_cselect_b32 s81, s84, s81
v_fma_mix_f32 v114, v81, v98, v106 op_sel_hi:[1,0,0]
v_mfma_f32_32x32x16_bf16 v[176:191], a[148:151], v[76:79], v[176:191]
ds_read_b64_tr_b16 a[132:133], v4 offset:32768
v_fma_mix_f32 v115, v81, v99, v107 op_sel:[1,0,0] op_sel_hi:[1,0,0]
v_cvt_pk_bf16_f32 v64, v112, v113
v_cvt_pk_bf16_f32 v65, v114, v115
v_fma_mix_f32 v112, v84, v96, v104 op_sel_hi:[1,0,0]
v_fma_mix_f32 v113, v84, v97, v105 op_sel:[1,0,0] op_sel_hi:[1,0,0]
s_add_u32 m0, s53, 0x4000
v_mfma_f32_32x32x16_bf16 v[160:175], a[148:151], v[72:75], v[160:175]
buffer_load_dwordx4 v18, s[36:39], s72 offen lds
ds_read_b64_tr_b16 a[134:135], v4 offset:33280
v_fma_mix_f32 v114, v85, v98, v106 op_sel_hi:[1,0,0]
v_fma_mix_f32 v115, v85, v99, v107 op_sel:[1,0,0] op_sel_hi:[1,0,0]
v_cvt_pk_bf16_f32 v68, v112, v113
s_waitcnt lgkmcnt(8)
v_mfma_f32_32x32x16_bf16 v[192:207], a[152:155], v[72:75], v[192:207]
ds_read_b64_tr_b16 a[136:137], v3 offset:34816
v_cvt_pk_bf16_f32 v69, v114, v115
ds_read_b128 v[96:99], v127 offset:2496
ds_read_b128 v[104:107], v127 offset:192
s_waitcnt lgkmcnt(9)
v_fma_mix_f32 v112, v82, v100, v108 op_sel_hi:[1,0,0]
v_fma_mix_f32 v113, v82, v101, v109 op_sel:[1,0,0] op_sel_hi:[1,0,0]
v_mfma_f32_32x32x16_bf16 v[208:223], a[152:155], v[76:79], v[208:223]
buffer_load_dwordx4 v19, s[36:39], s72 offen offset:1024 lds
ds_read_b64_tr_b16 a[138:139], v3 offset:35328
s_add_u32 s82, s82, 0x2000
s_cmp_ge_u32 s82, 0x26000
s_cselect_b32 s58, 0x6000, 0
s_sub_u32 s82, s82, s58
v_mfma_f32_32x32x16_bf16 v[240:255], a[156:159], v[76:79], v[240:255]
ds_read_b64_tr_b16 a[140:141], v4 offset:34816
v_fma_mix_f32 v114, v83, v102, v110 op_sel_hi:[1,0,0]
v_fma_mix_f32 v115, v83, v103, v111 op_sel:[1,0,0] op_sel_hi:[1,0,0]
v_cvt_pk_bf16_f32 v66, v112, v113
v_cvt_pk_bf16_f32 v67, v114, v115
v_fma_mix_f32 v112, v86, v100, v108 op_sel_hi:[1,0,0]
v_mfma_f32_32x32x16_bf16 v[224:239], a[156:159], v[72:75], v[224:239]
buffer_load_dwordx4 v120, s[36:39], s72 offen offset:2048 lds
ds_read_b64_tr_b16 a[142:143], v4 offset:35328
v_fma_mix_f32 v113, v86, v101, v109 op_sel:[1,0,0] op_sel_hi:[1,0,0]
v_fma_mix_f32 v114, v87, v102, v110 op_sel_hi:[1,0,0]
v_fma_mix_f32 v115, v87, v103, v111 op_sel:[1,0,0] op_sel_hi:[1,0,0]
v_cvt_pk_bf16_f32 v70, v112, v113
v_cvt_pk_bf16_f32 v71, v114, v115
ds_read_b128 v[100:103], v127 offset:2512
ds_read_b128 v[108:111], v127 offset:208
s_waitcnt lgkmcnt(8)
v_mfma_f32_32x32x16_bf16 v[128:143], a[128:131], v[64:67], v[128:143]
s_waitcnt vmcnt(9)
ds_read_b64_tr_b16 a[144:145], v3 offset:49152
s_add_u32 s55, s55, 0x2000
s_cmp_eq_u32 s55, 0x26000
s_cselect_b32 s55, 0x20000, s55
v_add_u32 v9, s55, v2
v_mfma_f32_32x32x16_bf16 v[144:159], a[128:131], v[68:71], v[144:159]
buffer_load_dwordx4 v121, s[36:39], s72 offen offset:3072 lds
ds_read_b64_tr_b16 a[146:147], v3 offset:49664
ds_read_b128 v[80:83], v9 offset:0
ds_read_b128 v[84:87], v9 offset:1024
s_waitcnt lgkmcnt(9)
v_fma_mix_f32 v112, v88, v96, v104 op_sel_hi:[1,0,0]
v_fma_mix_f32 v113, v88, v97, v105 op_sel:[1,0,0] op_sel_hi:[1,0,0]
v_mfma_f32_32x32x16_bf16 v[176:191], a[132:135], v[68:71], v[176:191]
ds_read_b64_tr_b16 a[148:149], v4 offset:49152
v_fma_mix_f32 v114, v89, v98, v106 op_sel_hi:[1,0,0]
v_fma_mix_f32 v115, v89, v99, v107 op_sel:[1,0,0] op_sel_hi:[1,0,0]
v_cvt_pk_bf16_f32 v72, v112, v113
v_cvt_pk_bf16_f32 v73, v114, v115
v_fma_mix_f32 v112, v92, v96, v104 op_sel_hi:[1,0,0]
s_add_u32 m0, s53, 0x8000
v_mfma_f32_32x32x16_bf16 v[160:175], a[132:135], v[64:67], v[160:175]
buffer_load_dwordx4 v20, s[36:39], s72 offen lds
ds_read_b64_tr_b16 a[150:151], v4 offset:49664
v_fma_mix_f32 v113, v92, v97, v105 op_sel:[1,0,0] op_sel_hi:[1,0,0]
v_fma_mix_f32 v114, v93, v98, v106 op_sel_hi:[1,0,0]
v_fma_mix_f32 v115, v93, v99, v107 op_sel:[1,0,0] op_sel_hi:[1,0,0]
s_waitcnt lgkmcnt(8)
v_mfma_f32_32x32x16_bf16 v[192:207], a[136:139], v[64:67], v[192:207]
ds_read_b64_tr_b16 a[152:153], v3 offset:51200
v_cvt_pk_bf16_f32 v76, v112, v113
v_cvt_pk_bf16_f32 v77, v114, v115
s_add_u32 s20, s20, 0x100
s_cmp_eq_u32 s20, 0x26900
s_cselect_b32 s20, 0x26000, s20
v_mfma_f32_32x32x16_bf16 v[208:223], a[136:139], v[68:71], v[208:223]
buffer_load_dwordx4 v21, s[36:39], s72 offen offset:1024 lds
ds_read_b64_tr_b16 a[154:155], v3 offset:51712
v_add_u32 v127, s20, v116
ds_read_b128 v[96:99], v127 offset:2304
ds_read_b128 v[104:107], v127 offset:0
s_waitcnt lgkmcnt(10)
v_fma_mix_f32 v112, v90, v100, v108 op_sel_hi:[1,0,0]
v_mfma_f32_32x32x16_bf16 v[240:255], a[140:143], v[68:71], v[240:255]
ds_read_b64_tr_b16 a[156:157], v4 offset:51200
v_fma_mix_f32 v113, v90, v101, v109 op_sel:[1,0,0] op_sel_hi:[1,0,0]
v_fma_mix_f32 v114, v91, v102, v110 op_sel_hi:[1,0,0]
v_fma_mix_f32 v115, v91, v103, v111 op_sel:[1,0,0] op_sel_hi:[1,0,0]
v_cvt_pk_bf16_f32 v74, v112, v113
v_cvt_pk_bf16_f32 v75, v114, v115
v_mfma_f32_32x32x16_bf16 v[224:239], a[140:143], v[64:67], v[224:239]
buffer_load_dwordx4 v122, s[36:39], s72 offen offset:2048 lds
ds_read_b64_tr_b16 a[158:159], v4 offset:51712
v_fma_mix_f32 v112, v94, v100, v108 op_sel_hi:[1,0,0]
v_fma_mix_f32 v113, v94, v101, v109 op_sel:[1,0,0] op_sel_hi:[1,0,0]
v_fma_mix_f32 v114, v95, v102, v110 op_sel_hi:[1,0,0]
v_fma_mix_f32 v115, v95, v103, v111 op_sel:[1,0,0] op_sel_hi:[1,0,0]
v_cvt_pk_bf16_f32 v78, v112, v113
v_cvt_pk_bf16_f32 v79, v114, v115
ds_read_b128 v[100:103], v127 offset:2320
ds_read_b128 v[108:111], v127 offset:16
s_waitcnt lgkmcnt(8)
v_mfma_f32_32x32x16_bf16 v[128:143], a[144:147], v[72:75], v[128:143]
s_waitcnt vmcnt(7)
ds_read_b64_tr_b16 a[128:129], v3 offset:0
ds_read_b128 v[88:91], v9 offset:2048
ds_read_b128 v[92:95], v9 offset:3072
)ASM"
R"ASM(
s_waitcnt lgkmcnt(7)
v_fma_mix_f32 v112, v80, v96, v104 op_sel_hi:[1,0,0]
v_fma_mix_f32 v113, v80, v97, v105 op_sel:[1,0,0] op_sel_hi:[1,0,0]
v_mfma_f32_32x32x16_bf16 v[144:159], a[144:147], v[76:79], v[144:159]
buffer_load_dwordx4 v123, s[36:39], s72 offen offset:3072 lds
ds_read_b64_tr_b16 a[130:131], v3 offset:512
v_fma_mix_f32 v114, v81, v98, v106 op_sel_hi:[1,0,0]
v_fma_mix_f32 v115, v81, v99, v107 op_sel:[1,0,0] op_sel_hi:[1,0,0]
v_cvt_pk_bf16_f32 v64, v112, v113
v_cvt_pk_bf16_f32 v65, v114, v115
v_mfma_f32_32x32x16_bf16 v[176:191], a[148:151], v[76:79], v[176:191]
ds_read_b64_tr_b16 a[132:133], v4 offset:0
v_fma_mix_f32 v112, v84, v96, v104 op_sel_hi:[1,0,0]
v_fma_mix_f32 v113, v84, v97, v105 op_sel:[1,0,0] op_sel_hi:[1,0,0]
v_fma_mix_f32 v114, v85, v98, v106 op_sel_hi:[1,0,0]
v_fma_mix_f32 v115, v85, v99, v107 op_sel:[1,0,0] op_sel_hi:[1,0,0]
v_cvt_pk_bf16_f32 v68, v112, v113
s_add_u32 m0, s53, 0xc000
v_mfma_f32_32x32x16_bf16 v[160:175], a[148:151], v[72:75], v[160:175]
buffer_load_dwordx4 v22, s[36:39], s72 offen lds
ds_read_b64_tr_b16 a[134:135], v4 offset:512
v_cvt_pk_bf16_f32 v69, v114, v115
ds_read_b128 v[96:99], v127 offset:2368
ds_read_b128 v[104:107], v127 offset:64
s_waitcnt lgkmcnt(10)
v_mfma_f32_32x32x16_bf16 v[192:207], a[152:155], v[72:75], v[192:207]
ds_read_b64_tr_b16 a[136:137], v3 offset:2048
s_waitcnt lgkmcnt(9)
v_fma_mix_f32 v112, v82, v100, v108 op_sel_hi:[1,0,0]
v_fma_mix_f32 v113, v82, v101, v109 op_sel:[1,0,0] op_sel_hi:[1,0,0]
v_fma_mix_f32 v114, v83, v102, v110 op_sel_hi:[1,0,0]
v_fma_mix_f32 v115, v83, v103, v111 op_sel:[1,0,0] op_sel_hi:[1,0,0]
v_cvt_pk_bf16_f32 v66, v112, v113
v_mfma_f32_32x32x16_bf16 v[208:223], a[152:155], v[76:79], v[208:223]
buffer_load_dwordx4 v23, s[36:39], s72 offen offset:1024 lds
ds_read_b64_tr_b16 a[138:139], v3 offset:2560
v_cvt_pk_bf16_f32 v67, v114, v115
v_fma_mix_f32 v112, v86, v100, v108 op_sel_hi:[1,0,0]
v_fma_mix_f32 v113, v86, v101, v109 op_sel:[1,0,0] op_sel_hi:[1,0,0]
v_fma_mix_f32 v114, v87, v102, v110 op_sel_hi:[1,0,0]
v_mfma_f32_32x32x16_bf16 v[240:255], a[156:159], v[76:79], v[240:255]
ds_read_b64_tr_b16 a[140:141], v4 offset:2048
v_fma_mix_f32 v115, v87, v103, v111 op_sel:[1,0,0] op_sel_hi:[1,0,0]
v_cvt_pk_bf16_f32 v70, v112, v113
v_cvt_pk_bf16_f32 v71, v114, v115
ds_read_b128 v[100:103], v127 offset:2384
ds_read_b128 v[108:111], v127 offset:80
v_mfma_f32_32x32x16_bf16 v[224:239], a[156:159], v[72:75], v[224:239]
buffer_load_dwordx4 v124, s[36:39], s72 offen offset:2048 lds
ds_read_b64_tr_b16 a[142:143], v4 offset:2560
s_waitcnt lgkmcnt(8)
s_add_u32 s54, s54, -1
s_cbranch_scc1 .Lk1_Q
s_barrier
v_mfma_f32_32x32x16_bf16 v[128:143], a[128:131], v[64:67], v[128:143]
s_waitcnt vmcnt(7)
ds_read_b64_tr_b16 a[144:145], v3 offset:16384
ds_read_b128 v[80:83], v9 offset:4096
ds_read_b128 v[84:87], v9 offset:5120
s_waitcnt lgkmcnt(9)
v_fma_mix_f32 v112, v88, v96, v104 op_sel_hi:[1,0,0]
v_fma_mix_f32 v113, v88, v97, v105 op_sel:[1,0,0] op_sel_hi:[1,0,0]
v_mfma_f32_32x32x16_bf16 v[144:159], a[128:131], v[68:71], v[144:159]
buffer_load_dwordx4 v125, s[36:39], s72 offen offset:3072 lds
ds_read_b64_tr_b16 a[146:147], v3 offset:16896
s_mov_b32 m0, s82
s_nop 0
buffer_load_dwordx4 v2, s[64:67], s81 offen sc1 lds
buffer_load_dwordx4 v126, s[64:67], s81 offen offset:1024 sc1 lds
v_mfma_f32_32x32x16_bf16 v[176:191], a[132:135], v[68:71], v[176:191]
ds_read_b64_tr_b16 a[148:149], v4 offset:16384
v_fma_mix_f32 v114, v89, v98, v106 op_sel_hi:[1,0,0]
v_fma_mix_f32 v115, v89, v99, v107 op_sel:[1,0,0] op_sel_hi:[1,0,0]
v_cvt_pk_bf16_f32 v72, v112, v113
v_cvt_pk_bf16_f32 v73, v114, v115
v_fma_mix_f32 v112, v92, v96, v104 op_sel_hi:[1,0,0]
s_add_u32 s72, s72, 0x80000
s_cmp_eq_u32 s72, s78
s_cselect_b32 s72, s75, s72
s_add_u32 m0, s53, 0
v_mfma_f32_32x32x16_bf16 v[160:175], a[132:135], v[64:67], v[160:175]
buffer_load_dwordx4 v16, s[36:39], s72 offen lds
ds_read_b64_tr_b16 a[150:151], v4 offset:16896
v_fma_mix_f32 v113, v92, v97, v105 op_sel:[1,0,0] op_sel_hi:[1,0,0]
v_fma_mix_f32 v114, v93, v98, v106 op_sel_hi:[1,0,0]
v_fma_mix_f32 v115, v93, v99, v107 op_sel:[1,0,0] op_sel_hi:[1,0,0]
s_waitcnt lgkmcnt(6)
v_mfma_f32_32x32x16_bf16 v[192:207], a[136:139], v[64:67], v[192:207]
ds_read_b64_tr_b16 a[152:153], v3 offset:18432
v_cvt_pk_bf16_f32 v76, v112, v113
v_cvt_pk_bf16_f32 v77, v114, v115
ds_read_b128 v[96:99], v127 offset:2432
ds_read_b128 v[104:107], v127 offset:128
v_fma_mix_f32 v112, v90, v100, v108 op_sel_hi:[1,0,0]
v_mfma_f32_32x32x16_bf16 v[208:223], a[136:139], v[68:71], v[208:223]
buffer_load_dwordx4 v17, s[36:39], s72 offen offset:1024 lds
ds_read_b64_tr_b16 a[154:155], v3 offset:18944
v_fma_mix_f32 v113, v90, v101, v109 op_sel:[1,0,0] op_sel_hi:[1,0,0]
v_fma_mix_f32 v114, v91, v102, v110 op_sel_hi:[1,0,0]
v_fma_mix_f32 v115, v91, v103, v111 op_sel:[1,0,0] op_sel_hi:[1,0,0]
v_cvt_pk_bf16_f32 v74, v112, v113
v_mfma_f32_32x32x16_bf16 v[240:255], a[140:143], v[68:71], v[240:255]
ds_read_b64_tr_b16 a[156:157], v4 offset:18432
v_cvt_pk_bf16_f32 v75, v114, v115
v_fma_mix_f32 v112, v94, v100, v108 op_sel_hi:[1,0,0]
v_fma_mix_f32 v113, v94, v101, v109 op_sel:[1,0,0] op_sel_hi:[1,0,0]
v_fma_mix_f32 v114, v95, v102, v110 op_sel_hi:[1,0,0]
v_fma_mix_f32 v115, v95, v103, v111 op_sel:[1,0,0] op_sel_hi:[1,0,0]
v_mfma_f32_32x32x16_bf16 v[224:239], a[140:143], v[64:67], v[224:239]
buffer_load_dwordx4 v118, s[36:39], s72 offen offset:2048 lds
ds_read_b64_tr_b16 a[158:159], v4 offset:18944
v_cvt_pk_bf16_f32 v78, v112, v113
v_cvt_pk_bf16_f32 v79, v114, v115
ds_read_b128 v[100:103], v127 offset:2448
ds_read_b128 v[108:111], v127 offset:144
s_waitcnt lgkmcnt(8)
v_mfma_f32_32x32x16_bf16 v[128:143], a[144:147], v[72:75], v[128:143]
s_waitcnt vmcnt(9)
ds_read_b64_tr_b16 a[128:129], v3 offset:32768
ds_read_b128 v[88:91], v9 offset:6144
ds_read_b128 v[92:95], v9 offset:7168
s_waitcnt lgkmcnt(8)
v_fma_mix_f32 v112, v80, v96, v104 op_sel_hi:[1,0,0]
v_fma_mix_f32 v113, v80, v97, v105 op_sel:[1,0,0] op_sel_hi:[1,0,0]
v_mfma_f32_32x32x16_bf16 v[144:159], a[144:147], v[76:79], v[144:159]
buffer_load_dwordx4 v119, s[36:39], s72 offen offset:3072 lds
ds_read_b64_tr_b16 a[130:131], v3 offset:33280
s_add_u32 s81, s81, 0x1000
s_cmp_eq_u32 s81, s83
s_cselect_b32 s81, s84, s81
v_fma_mix_f32 v114, v81, v98, v106 op_sel_hi:[1,0,0]
v_mfma_f32_32x32x16_bf16 v[176:191], a[148:151], v[76:79], v[176:191]
ds_read_b64_tr_b16 a[132:133], v4 offset:32768
v_fma_mix_f32 v115, v81, v99, v107 op_sel:[1,0,0] op_sel_hi:[1,0,0]
v_cvt_pk_bf16_f32 v64, v112, v113
v_cvt_pk_bf16_f32 v65, v114, v115
v_fma_mix_f32 v112, v84, v96, v104 op_sel_hi:[1,0,0]
v_fma_mix_f32 v113, v84, v97, v105 op_sel:[1,0,0] op_sel_hi:[1,0,0]
s_add_u32 m0, s53, 0x4000
v_mfma_f32_32x32x16_bf16 v[160:175], a[148:151], v[72:75], v[160:175]
buffer_load_dwordx4 v18, s[36:39], s72 offen lds
ds_read_b64_tr_b16 a[134:135], v4 offset:33280
v_fma_mix_f32 v114, v85, v98, v106 op_sel_hi:[1,0,0]
v_fma_mix_f32 v115, v85, v99, v107 op_sel:[1,0,0] op_sel_hi:[1,0,0]
v_cvt_pk_bf16_f32 v68, v112, v113
s_waitcnt lgkmcnt(8)
v_mfma_f32_32x32x16_bf16 v[192:207], a[152:155], v[72:75], v[192:207]
ds_read_b64_tr_b16 a[136:137], v3 offset:34816
v_cvt_pk_bf16_f32 v69, v114, v115
ds_read_b128 v[96:99], v127 offset:2496
ds_read_b128 v[104:107], v127 offset:192
s_waitcnt lgkmcnt(9)
v_fma_mix_f32 v112, v82, v100, v108 op_sel_hi:[1,0,0]
v_fma_mix_f32 v113, v82, v101, v109 op_sel:[1,0,0] op_sel_hi:[1,0,0]
v_mfma_f32_32x32x16_bf16 v[208:223], a[152:155], v[76:79], v[208:223]
buffer_load_dwordx4 v19, s[36:39], s72 offen offset:1024 lds
ds_read_b64_tr_b16 a[138:139], v3 offset:35328
s_add_u32 s82, s82, 0x2000
s_cmp_ge_u32 s82, 0x26000
s_cselect_b32 s58, 0x6000, 0
s_sub_u32 s82, s82, s58
v_mfma_f32_32x32x16_bf16 v[240:255], a[156:159], v[76:79], v[240:255]
ds_read_b64_tr_b16 a[140:141], v4 offset:34816
v_fma_mix_f32 v114, v83, v102, v110 op_sel_hi:[1,0,0]
v_fma_mix_f32 v115, v83, v103, v111 op_sel:[1,0,0] op_sel_hi:[1,0,0]
v_cvt_pk_bf16_f32 v66, v112, v113
v_cvt_pk_bf16_f32 v67, v114, v115
v_fma_mix_f32 v112, v86, v100, v108 op_sel_hi:[1,0,0]
v_mfma_f32_32x32x16_bf16 v[224:239], a[156:159], v[72:75], v[224:239]
buffer_load_dwordx4 v120, s[36:39], s72 offen offset:2048 lds
ds_read_b64_tr_b16 a[142:143], v4 offset:35328
v_fma_mix_f32 v113, v86, v101, v109 op_sel:[1,0,0] op_sel_hi:[1,0,0]
v_fma_mix_f32 v114, v87, v102, v110 op_sel_hi:[1,0,0]
v_fma_mix_f32 v115, v87, v103, v111 op_sel:[1,0,0] op_sel_hi:[1,0,0]
v_cvt_pk_bf16_f32 v70, v112, v113
v_cvt_pk_bf16_f32 v71, v114, v115
ds_read_b128 v[100:103], v127 offset:2512
ds_read_b128 v[108:111], v127 offset:208
s_waitcnt lgkmcnt(8)
v_mfma_f32_32x32x16_bf16 v[128:143], a[128:131], v[64:67], v[128:143]
s_waitcnt vmcnt(9)
ds_read_b64_tr_b16 a[144:145], v3 offset:49152
s_add_u32 s55, s55, 0x2000
s_cmp_eq_u32 s55, 0x26000
s_cselect_b32 s55, 0x20000, s55
v_add_u32 v9, s55, v2
v_mfma_f32_32x32x16_bf16 v[144:159], a[128:131], v[68:71], v[144:159]
buffer_load_dwordx4 v121, s[36:39], s72 offen offset:3072 lds
ds_read_b64_tr_b16 a[146:147], v3 offset:49664
ds_read_b128 v[80:83], v9 offset:0
ds_read_b128 v[84:87], v9 offset:1024
s_waitcnt lgkmcnt(9)
v_fma_mix_f32 v112, v88, v96, v104 op_sel_hi:[1,0,0]
v_fma_mix_f32 v113, v88, v97, v105 op_sel:[1,0,0] op_sel_hi:[1,0,0]
v_mfma_f32_32x32x16_bf16 v[176:191], a[132:135], v[68:71], v[176:191]
ds_read_b64_tr_b16 a[148:149], v4 offset:49152
v_fma_mix_f32 v114, v89, v98, v106 op_sel_hi:[1,0,0]
v_fma_mix_f32 v115, v89, v99, v107 op_sel:[1,0,0] op_sel_hi:[1,0,0]
v_cvt_pk_bf16_f32 v72, v112, v113
v_cvt_pk_bf16_f32 v73, v114, v115
)ASM"
R"ASM(
v_fma_mix_f32 v112, v92, v96, v104 op_sel_hi:[1,0,0]
s_add_u32 m0, s53, 0x8000
v_mfma_f32_32x32x16_bf16 v[160:175], a[132:135], v[64:67], v[160:175]
buffer_load_dwordx4 v20, s[36:39], s72 offen lds
ds_read_b64_tr_b16 a[150:151], v4 offset:49664
v_fma_mix_f32 v113, v92, v97, v105 op_sel:[1,0,0] op_sel_hi:[1,0,0]
v_fma_mix_f32 v114, v93, v98, v106 op_sel_hi:[1,0,0]
v_fma_mix_f32 v115, v93, v99, v107 op_sel:[1,0,0] op_sel_hi:[1,0,0]
s_waitcnt lgkmcnt(8)
v_mfma_f32_32x32x16_bf16 v[192:207], a[136:139], v[64:67], v[192:207]
ds_read_b64_tr_b16 a[152:153], v3 offset:51200
v_cvt_pk_bf16_f32 v76, v112, v113
v_cvt_pk_bf16_f32 v77, v114, v115
s_add_u32 s20, s20, 0x100
s_cmp_eq_u32 s20, 0x26900
s_cselect_b32 s20, 0x26000, s20
v_mfma_f32_32x32x16_bf16 v[208:223], a[136:139], v[68:71], v[208:223]
buffer_load_dwordx4 v21, s[36:39], s72 offen offset:1024 lds
ds_read_b64_tr_b16 a[154:155], v3 offset:51712
v_add_u32 v127, s20, v116
ds_read_b128 v[96:99], v127 offset:2304
ds_read_b128 v[104:107], v127 offset:0
s_waitcnt lgkmcnt(10)
v_fma_mix_f32 v112, v90, v100, v108 op_sel_hi:[1,0,0]
v_mfma_f32_32x32x16_bf16 v[240:255], a[140:143], v[68:71], v[240:255]
ds_read_b64_tr_b16 a[156:157], v4 offset:51200
v_fma_mix_f32 v113, v90, v101, v109 op_sel:[1,0,0] op_sel_hi:[1,0,0]
v_fma_mix_f32 v114, v91, v102, v110 op_sel_hi:[1,0,0]
v_fma_mix_f32 v115, v91, v103, v111 op_sel:[1,0,0] op_sel_hi:[1,0,0]
v_cvt_pk_bf16_f32 v74, v112, v113
v_cvt_pk_bf16_f32 v75, v114, v115
v_mfma_f32_32x32x16_bf16 v[224:239], a[140:143], v[64:67], v[224:239]
buffer_load_dwordx4 v122, s[36:39], s72 offen offset:2048 lds
ds_read_b64_tr_b16 a[158:159], v4 offset:51712
v_fma_mix_f32 v112, v94, v100, v108 op_sel_hi:[1,0,0]
v_fma_mix_f32 v113, v94, v101, v109 op_sel:[1,0,0] op_sel_hi:[1,0,0]
v_fma_mix_f32 v114, v95, v102, v110 op_sel_hi:[1,0,0]
v_fma_mix_f32 v115, v95, v103, v111 op_sel:[1,0,0] op_sel_hi:[1,0,0]
v_cvt_pk_bf16_f32 v78, v112, v113
v_cvt_pk_bf16_f32 v79, v114, v115
ds_read_b128 v[100:103], v127 offset:2320
ds_read_b128 v[108:111], v127 offset:16
s_waitcnt lgkmcnt(8)
v_mfma_f32_32x32x16_bf16 v[128:143], a[144:147], v[72:75], v[128:143]
s_waitcnt vmcnt(7)
ds_read_b64_tr_b16 a[128:129], v3 offset:0
ds_read_b128 v[88:91], v9 offset:2048
ds_read_b128 v[92:95], v9 offset:3072
s_waitcnt lgkmcnt(7)
v_fma_mix_f32 v112, v80, v96, v104 op_sel_hi:[1,0,0]
v_fma_mix_f32 v113, v80, v97, v105 op_sel:[1,0,0] op_sel_hi:[1,0,0]
v_mfma_f32_32x32x16_bf16 v[144:159], a[144:147], v[76:79], v[144:159]
buffer_load_dwordx4 v123, s[36:39], s72 offen offset:3072 lds
ds_read_b64_tr_b16 a[130:131], v3 offset:512
v_fma_mix_f32 v114, v81, v98, v106 op_sel_hi:[1,0,0]
v_fma_mix_f32 v115, v81, v99, v107 op_sel:[1,0,0] op_sel_hi:[1,0,0]
v_cvt_pk_bf16_f32 v64, v112, v113
v_cvt_pk_bf16_f32 v65, v114, v115
v_mfma_f32_32x32x16_bf16 v[176:191], a[148:151], v[76:79], v[176:191]
ds_read_b64_tr_b16 a[132:133], v4 offset:0
v_fma_mix_f32 v112, v84, v96, v104 op_sel_hi:[1,0,0]
v_fma_mix_f32 v113, v84, v97, v105 op_sel:[1,0,0] op_sel_hi:[1,0,0]
v_fma_mix_f32 v114, v85, v98, v106 op_sel_hi:[1,0,0]
v_fma_mix_f32 v115, v85, v99, v107 op_sel:[1,0,0] op_sel_hi:[1,0,0]
v_cvt_pk_bf16_f32 v68, v112, v113
s_add_u32 m0, s53, 0xc000
v_mfma_f32_32x32x16_bf16 v[160:175], a[148:151], v[72:75], v[160:175]
buffer_load_dwordx4 v22, s[36:39], s72 offen lds
ds_read_b64_tr_b16 a[134:135], v4 offset:512
v_cvt_pk_bf16_f32 v69, v114, v115
ds_read_b128 v[96:99], v127 offset:2368
ds_read_b128 v[104:107], v127 offset:64
s_waitcnt lgkmcnt(10)
v_mfma_f32_32x32x16_bf16 v[192:207], a[152:155], v[72:75], v[192:207]
ds_read_b64_tr_b16 a[136:137], v3 offset:2048
s_waitcnt lgkmcnt(9)
v_fma_mix_f32 v112, v82, v100, v108 op_sel_hi:[1,0,0]
v_fma_mix_f32 v113, v82, v101, v109 op_sel:[1,0,0] op_sel_hi:[1,0,0]
v_fma_mix_f32 v114, v83, v102, v110 op_sel_hi:[1,0,0]
v_fma_mix_f32 v115, v83, v103, v111 op_sel:[1,0,0] op_sel_hi:[1,0,0]
v_cvt_pk_bf16_f32 v66, v112, v113
v_mfma_f32_32x32x16_bf16 v[208:223], a[152:155], v[76:79], v[208:223]
buffer_load_dwordx4 v23, s[36:39], s72 offen offset:1024 lds
ds_read_b64_tr_b16 a[138:139], v3 offset:2560
v_cvt_pk_bf16_f32 v67, v114, v115
v_fma_mix_f32 v112, v86, v100, v108 op_sel_hi:[1,0,0]
v_fma_mix_f32 v113, v86, v101, v109 op_sel:[1,0,0] op_sel_hi:[1,0,0]
v_fma_mix_f32 v114, v87, v102, v110 op_sel_hi:[1,0,0]
v_mfma_f32_32x32x16_bf16 v[240:255], a[156:159], v[76:79], v[240:255]
ds_read_b64_tr_b16 a[140:141], v4 offset:2048
v_fma_mix_f32 v115, v87, v103, v111 op_sel:[1,0,0] op_sel_hi:[1,0,0]
v_cvt_pk_bf16_f32 v70, v112, v113
v_cvt_pk_bf16_f32 v71, v114, v115
ds_read_b128 v[100:103], v127 offset:2384
ds_read_b128 v[108:111], v127 offset:80
v_mfma_f32_32x32x16_bf16 v[224:239], a[156:159], v[72:75], v[224:239]
buffer_load_dwordx4 v124, s[36:39], s72 offen offset:2048 lds
ds_read_b64_tr_b16 a[142:143], v4 offset:2560
s_waitcnt lgkmcnt(8)
s_barrier
s_mov_b32 s83, s16
s_mov_b32 s84, s16
v_mfma_f32_32x32x16_bf16 v[128:143], a[128:131], v[64:67], v[128:143]
s_waitcnt vmcnt(7)
ds_read_b64_tr_b16 a[144:145], v3 offset:16384
ds_read_b128 v[80:83], v9 offset:4096
ds_read_b128 v[84:87], v9 offset:5120
s_waitcnt lgkmcnt(9)
v_fma_mix_f32 v112, v88, v96, v104 op_sel_hi:[1,0,0]
v_fma_mix_f32 v113, v88, v97, v105 op_sel:[1,0,0] op_sel_hi:[1,0,0]
v_mfma_f32_32x32x16_bf16 v[144:159], a[128:131], v[68:71], v[144:159]
buffer_load_dwordx4 v125, s[36:39], s72 offen offset:3072 lds
ds_read_b64_tr_b16 a[146:147], v3 offset:16896
s_add_u32 m0, s34, 0
s_nop 0
buffer_load_dwordx4 v32, s[40:43], s83 offen lds
buffer_load_dwordx4 v33, s[40:43], s83 offen offset:1024 lds
v_mfma_f32_32x32x16_bf16 v[176:191], a[132:135], v[68:71], v[176:191]
ds_read_b64_tr_b16 a[148:149], v4 offset:16384
v_fma_mix_f32 v114, v89, v98, v106 op_sel_hi:[1,0,0]
v_fma_mix_f32 v115, v89, v99, v107 op_sel:[1,0,0] op_sel_hi:[1,0,0]
v_cvt_pk_bf16_f32 v72, v112, v113
v_cvt_pk_bf16_f32 v73, v114, v115
v_fma_mix_f32 v112, v92, v96, v104 op_sel_hi:[1,0,0]
s_add_u32 s72, s72, 0x80000
s_cmp_eq_u32 s72, s78
s_cselect_b32 s72, s75, s72
s_add_u32 m0, s53, 0
v_mfma_f32_32x32x16_bf16 v[160:175], a[132:135], v[64:67], v[160:175]
buffer_load_dwordx4 v16, s[36:39], s72 offen lds
ds_read_b64_tr_b16 a[150:151], v4 offset:16896
v_fma_mix_f32 v113, v92, v97, v105 op_sel:[1,0,0] op_sel_hi:[1,0,0]
v_fma_mix_f32 v114, v93, v98, v106 op_sel_hi:[1,0,0]
v_fma_mix_f32 v115, v93, v99, v107 op_sel:[1,0,0] op_sel_hi:[1,0,0]
s_waitcnt lgkmcnt(6)
v_mfma_f32_32x32x16_bf16 v[192:207], a[136:139], v[64:67], v[192:207]
ds_read_b64_tr_b16 a[152:153], v3 offset:18432
v_cvt_pk_bf16_f32 v76, v112, v113
v_cvt_pk_bf16_f32 v77, v114, v115
ds_read_b128 v[96:99], v127 offset:2432
ds_read_b128 v[104:107], v127 offset:128
v_fma_mix_f32 v112, v90, v100, v108 op_sel_hi:[1,0,0]
v_mfma_f32_32x32x16_bf16 v[208:223], a[136:139], v[68:71], v[208:223]
buffer_load_dwordx4 v17, s[36:39], s72 offen offset:1024 lds
ds_read_b64_tr_b16 a[154:155], v3 offset:18944
v_fma_mix_f32 v113, v90, v101, v109 op_sel:[1,0,0] op_sel_hi:[1,0,0]
v_fma_mix_f32 v114, v91, v102, v110 op_sel_hi:[1,0,0]
v_fma_mix_f32 v115, v91, v103, v111 op_sel:[1,0,0] op_sel_hi:[1,0,0]
v_cvt_pk_bf16_f32 v74, v112, v113
v_mfma_f32_32x32x16_bf16 v[240:255], a[140:143], v[68:71], v[240:255]
ds_read_b64_tr_b16 a[156:157], v4 offset:18432
v_cvt_pk_bf16_f32 v75, v114, v115
v_fma_mix_f32 v112, v94, v100, v108 op_sel_hi:[1,0,0]
v_fma_mix_f32 v113, v94, v101, v109 op_sel:[1,0,0] op_sel_hi:[1,0,0]
v_fma_mix_f32 v114, v95, v102, v110 op_sel_hi:[1,0,0]
v_fma_mix_f32 v115, v95, v103, v111 op_sel:[1,0,0] op_sel_hi:[1,0,0]
v_mfma_f32_32x32x16_bf16 v[224:239], a[140:143], v[64:67], v[224:239]
buffer_load_dwordx4 v118, s[36:39], s72 offen offset:2048 lds
ds_read_b64_tr_b16 a[158:159], v4 offset:18944
v_cvt_pk_bf16_f32 v78, v112, v113
v_cvt_pk_bf16_f32 v79, v114, v115
ds_read_b128 v[100:103], v127 offset:2448
ds_read_b128 v[108:111], v127 offset:144
s_waitcnt lgkmcnt(8)
v_mfma_f32_32x32x16_bf16 v[128:143], a[144:147], v[72:75], v[128:143]
s_waitcnt vmcnt(9)
ds_read_b64_tr_b16 a[128:129], v3 offset:32768
ds_read_b128 v[88:91], v9 offset:6144
ds_read_b128 v[92:95], v9 offset:7168
s_waitcnt lgkmcnt(8)
v_fma_mix_f32 v112, v80, v96, v104 op_sel_hi:[1,0,0]
v_fma_mix_f32 v113, v80, v97, v105 op_sel:[1,0,0] op_sel_hi:[1,0,0]
v_mfma_f32_32x32x16_bf16 v[144:159], a[144:147], v[76:79], v[144:159]
buffer_load_dwordx4 v119, s[36:39], s72 offen offset:3072 lds
ds_read_b64_tr_b16 a[130:131], v3 offset:33280
s_add_u32 m0, s34, 0
s_nop 0
buffer_load_dwordx4 v34, s[40:43], s83 offen offset:2048 lds
buffer_load_dwordx4 v35, s[40:43], s83 offen offset:3072 lds
s_add_u32 s83, s83, 0x2000
s_and_b32 s83, s83, 0x1ffff
v_mfma_f32_32x32x16_bf16 v[176:191], a[148:151], v[76:79], v[176:191]
ds_read_b64_tr_b16 a[132:133], v4 offset:32768
v_fma_mix_f32 v114, v81, v98, v106 op_sel_hi:[1,0,0]
v_fma_mix_f32 v115, v81, v99, v107 op_sel:[1,0,0] op_sel_hi:[1,0,0]
v_cvt_pk_bf16_f32 v64, v112, v113
v_cvt_pk_bf16_f32 v65, v114, v115
v_fma_mix_f32 v112, v84, v96, v104 op_sel_hi:[1,0,0]
s_add_u32 m0, s53, 0x4000
v_mfma_f32_32x32x16_bf16 v[160:175], a[148:151], v[72:75], v[160:175]
buffer_load_dwordx4 v18, s[36:39], s72 offen lds
ds_read_b64_tr_b16 a[134:135], v4 offset:33280
v_fma_mix_f32 v113, v84, v97, v105 op_sel:[1,0,0] op_sel_hi:[1,0,0]
v_fma_mix_f32 v114, v85, v98, v106 op_sel_hi:[1,0,0]
v_fma_mix_f32 v115, v85, v99, v107 op_sel:[1,0,0] op_sel_hi:[1,0,0]
s_waitcnt lgkmcnt(8)
v_mfma_f32_32x32x16_bf16 v[192:207], a[152:155], v[72:75], v[192:207]
ds_read_b64_tr_b16 a[136:137], v3 offset:34816
v_cvt_pk_bf16_f32 v68, v112, v113
v_cvt_pk_bf16_f32 v69, v114, v115
)ASM"
R"ASM(
ds_read_b128 v[96:99], v127 offset:2496
ds_read_b128 v[104:107], v127 offset:192
s_waitcnt lgkmcnt(9)
v_fma_mix_f32 v112, v82, v100, v108 op_sel_hi:[1,0,0]
v_mfma_f32_32x32x16_bf16 v[208:223], a[152:155], v[76:79], v[208:223]
buffer_load_dwordx4 v19, s[36:39], s72 offen offset:1024 lds
ds_read_b64_tr_b16 a[138:139], v3 offset:35328
v_fma_mix_f32 v113, v82, v101, v109 op_sel:[1,0,0] op_sel_hi:[1,0,0]
v_fma_mix_f32 v114, v83, v102, v110 op_sel_hi:[1,0,0]
v_fma_mix_f32 v115, v83, v103, v111 op_sel:[1,0,0] op_sel_hi:[1,0,0]
v_cvt_pk_bf16_f32 v66, v112, v113
v_mfma_f32_32x32x16_bf16 v[240:255], a[156:159], v[76:79], v[240:255]
ds_read_b64_tr_b16 a[140:141], v4 offset:34816
v_cvt_pk_bf16_f32 v67, v114, v115
v_fma_mix_f32 v112, v86, v100, v108 op_sel_hi:[1,0,0]
v_fma_mix_f32 v113, v86, v101, v109 op_sel:[1,0,0] op_sel_hi:[1,0,0]
v_fma_mix_f32 v114, v87, v102, v110 op_sel_hi:[1,0,0]
v_fma_mix_f32 v115, v87, v103, v111 op_sel:[1,0,0] op_sel_hi:[1,0,0]
v_mfma_f32_32x32x16_bf16 v[224:239], a[156:159], v[72:75], v[224:239]
buffer_load_dwordx4 v120, s[36:39], s72 offen offset:2048 lds
ds_read_b64_tr_b16 a[142:143], v4 offset:35328
v_cvt_pk_bf16_f32 v70, v112, v113
v_cvt_pk_bf16_f32 v71, v114, v115
ds_read_b128 v[100:103], v127 offset:2512
ds_read_b128 v[108:111], v127 offset:208
s_waitcnt lgkmcnt(8)
v_mfma_f32_32x32x16_bf16 v[128:143], a[128:131], v[64:67], v[128:143]
s_waitcnt vmcnt(11)
ds_read_b64_tr_b16 a[144:145], v3 offset:49152
s_add_u32 s55, s55, 0x2000
s_cmp_eq_u32 s55, 0x26000
s_cselect_b32 s55, 0x20000, s55
v_add_u32 v9, s55, v2
v_mfma_f32_32x32x16_bf16 v[144:159], a[128:131], v[68:71], v[144:159]
buffer_load_dwordx4 v121, s[36:39], s72 offen offset:3072 lds
ds_read_b64_tr_b16 a[146:147], v3 offset:49664
s_add_u32 m0, s34, 0x4000
s_nop 0
buffer_load_dwordx4 v32, s[40:43], s83 offen lds
buffer_load_dwordx4 v33, s[40:43], s83 offen offset:1024 lds
v_mfma_f32_32x32x16_bf16 v[176:191], a[132:135], v[68:71], v[176:191]
ds_read_b64_tr_b16 a[148:149], v4 offset:49152
ds_read_b128 v[80:83], v9 offset:0
ds_read_b128 v[84:87], v9 offset:1024
s_waitcnt lgkmcnt(10)
v_fma_mix_f32 v112, v88, v96, v104 op_sel_hi:[1,0,0]
v_fma_mix_f32 v113, v88, v97, v105 op_sel:[1,0,0] op_sel_hi:[1,0,0]
v_fma_mix_f32 v114, v89, v98, v106 op_sel_hi:[1,0,0]
s_add_u32 m0, s53, 0x8000
v_mfma_f32_32x32x16_bf16 v[160:175], a[132:135], v[64:67], v[160:175]
buffer_load_dwordx4 v20, s[36:39], s72 offen lds
ds_read_b64_tr_b16 a[150:151], v4 offset:49664
v_fma_mix_f32 v115, v89, v99, v107 op_sel:[1,0,0] op_sel_hi:[1,0,0]
v_cvt_pk_bf16_f32 v72, v112, v113
v_cvt_pk_bf16_f32 v73, v114, v115
s_waitcnt lgkmcnt(8)
v_mfma_f32_32x32x16_bf16 v[192:207], a[136:139], v[64:67], v[192:207]
ds_read_b64_tr_b16 a[152:153], v3 offset:51200
v_fma_mix_f32 v112, v92, v96, v104 op_sel_hi:[1,0,0]
v_fma_mix_f32 v113, v92, v97, v105 op_sel:[1,0,0] op_sel_hi:[1,0,0]
v_fma_mix_f32 v114, v93, v98, v106 op_sel_hi:[1,0,0]
v_fma_mix_f32 v115, v93, v99, v107 op_sel:[1,0,0] op_sel_hi:[1,0,0]
v_cvt_pk_bf16_f32 v76, v112, v113
v_mfma_f32_32x32x16_bf16 v[208:223], a[136:139], v[68:71], v[208:223]
buffer_load_dwordx4 v21, s[36:39], s72 offen offset:1024 lds
ds_read_b64_tr_b16 a[154:155], v3 offset:51712
v_cvt_pk_bf16_f32 v77, v114, v115
s_add_u32 s20, s20, 0x100
s_cmp_eq_u32 s20, 0x26900
s_cselect_b32 s20, 0x26000, s20
v_mfma_f32_32x32x16_bf16 v[240:255], a[140:143], v[68:71], v[240:255]
ds_read_b64_tr_b16 a[156:157], v4 offset:51200
v_add_u32 v127, s20, v116
ds_read_b128 v[96:99], v127 offset:2304
ds_read_b128 v[104:107], v127 offset:0
s_waitcnt lgkmcnt(11)
v_fma_mix_f32 v112, v90, v100, v108 op_sel_hi:[1,0,0]
v_fma_mix_f32 v113, v90, v101, v109 op_sel:[1,0,0] op_sel_hi:[1,0,0]
v_mfma_f32_32x32x16_bf16 v[224:239], a[140:143], v[64:67], v[224:239]
buffer_load_dwordx4 v122, s[36:39], s72 offen offset:2048 lds
ds_read_b64_tr_b16 a[158:159], v4 offset:51712
v_fma_mix_f32 v114, v91, v102, v110 op_sel_hi:[1,0,0]
v_fma_mix_f32 v115, v91, v103, v111 op_sel:[1,0,0] op_sel_hi:[1,0,0]
v_cvt_pk_bf16_f32 v74, v112, v113
v_cvt_pk_bf16_f32 v75, v114, v115
v_fma_mix_f32 v112, v94, v100, v108 op_sel_hi:[1,0,0]
v_fma_mix_f32 v113, v94, v101, v109 op_sel:[1,0,0] op_sel_hi:[1,0,0]
v_fma_mix_f32 v114, v95, v102, v110 op_sel_hi:[1,0,0]
v_fma_mix_f32 v115, v95, v103, v111 op_sel:[1,0,0] op_sel_hi:[1,0,0]
v_cvt_pk_bf16_f32 v78, v112, v113
v_cvt_pk_bf16_f32 v79, v114, v115
ds_read_b128 v[100:103], v127 offset:2320
ds_read_b128 v[108:111], v127 offset:16
s_waitcnt lgkmcnt(8)
v_mfma_f32_32x32x16_bf16 v[128:143], a[144:147], v[72:75], v[128:143]
s_waitcnt vmcnt(11)
ds_read_b64_tr_b16 a[128:129], v3 offset:0
ds_read_b128 v[88:91], v9 offset:2048
ds_read_b128 v[92:95], v9 offset:3072
s_waitcnt lgkmcnt(6)
v_fma_mix_f32 v112, v80, v96, v104 op_sel_hi:[1,0,0]
v_fma_mix_f32 v113, v80, v97, v105 op_sel:[1,0,0] op_sel_hi:[1,0,0]
v_mfma_f32_32x32x16_bf16 v[144:159], a[144:147], v[76:79], v[144:159]
buffer_load_dwordx4 v123, s[36:39], s72 offen offset:3072 lds
ds_read_b64_tr_b16 a[130:131], v3 offset:512
s_add_u32 m0, s34, 0x4000
s_nop 0
buffer_load_dwordx4 v34, s[40:43], s83 offen offset:2048 lds
buffer_load_dwordx4 v35, s[40:43], s83 offen offset:3072 lds
s_add_u32 s83, s83, 0x2000
s_and_b32 s83, s83, 0x1ffff
v_mfma_f32_32x32x16_bf16 v[176:191], a[148:151], v[76:79], v[176:191]
ds_read_b64_tr_b16 a[132:133], v4 offset:0
v_fma_mix_f32 v114, v81, v98, v106 op_sel_hi:[1,0,0]
v_fma_mix_f32 v115, v81, v99, v107 op_sel:[1,0,0] op_sel_hi:[1,0,0]
v_cvt_pk_bf16_f32 v64, v112, v113
v_cvt_pk_bf16_f32 v65, v114, v115
v_fma_mix_f32 v112, v84, v96, v104 op_sel_hi:[1,0,0]
s_add_u32 m0, s53, 0xc000
v_mfma_f32_32x32x16_bf16 v[160:175], a[148:151], v[72:75], v[160:175]
buffer_load_dwordx4 v22, s[36:39], s72 offen lds
ds_read_b64_tr_b16 a[134:135], v4 offset:512
v_fma_mix_f32 v113, v84, v97, v105 op_sel:[1,0,0] op_sel_hi:[1,0,0]
v_fma_mix_f32 v114, v85, v98, v106 op_sel_hi:[1,0,0]
v_fma_mix_f32 v115, v85, v99, v107 op_sel:[1,0,0] op_sel_hi:[1,0,0]
s_waitcnt lgkmcnt(8)
v_mfma_f32_32x32x16_bf16 v[192:207], a[152:155], v[72:75], v[192:207]
ds_read_b64_tr_b16 a[136:137], v3 offset:2048
v_cvt_pk_bf16_f32 v68, v112, v113
v_cvt_pk_bf16_f32 v69, v114, v115
ds_read_b128 v[96:99], v127 offset:2368
ds_read_b128 v[104:107], v127 offset:64
s_waitcnt lgkmcnt(9)
v_fma_mix_f32 v112, v82, v100, v108 op_sel_hi:[1,0,0]
v_mfma_f32_32x32x16_bf16 v[208:223], a[152:155], v[76:79], v[208:223]
buffer_load_dwordx4 v23, s[36:39], s72 offen offset:1024 lds
ds_read_b64_tr_b16 a[138:139], v3 offset:2560
v_fma_mix_f32 v113, v82, v101, v109 op_sel:[1,0,0] op_sel_hi:[1,0,0]
v_fma_mix_f32 v114, v83, v102, v110 op_sel_hi:[1,0,0]
v_fma_mix_f32 v115, v83, v103, v111 op_sel:[1,0,0] op_sel_hi:[1,0,0]
v_cvt_pk_bf16_f32 v66, v112, v113
v_mfma_f32_32x32x16_bf16 v[240:255], a[156:159], v[76:79], v[240:255]
ds_read_b64_tr_b16 a[140:141], v4 offset:2048
v_cvt_pk_bf16_f32 v67, v114, v115
v_fma_mix_f32 v112, v86, v100, v108 op_sel_hi:[1,0,0]
v_fma_mix_f32 v113, v86, v101, v109 op_sel:[1,0,0] op_sel_hi:[1,0,0]
v_fma_mix_f32 v114, v87, v102, v110 op_sel_hi:[1,0,0]
v_fma_mix_f32 v115, v87, v103, v111 op_sel:[1,0,0] op_sel_hi:[1,0,0]
v_mfma_f32_32x32x16_bf16 v[224:239], a[156:159], v[72:75], v[224:239]
buffer_load_dwordx4 v124, s[36:39], s72 offen offset:2048 lds
ds_read_b64_tr_b16 a[142:143], v4 offset:2560
v_cvt_pk_bf16_f32 v70, v112, v113
v_cvt_pk_bf16_f32 v71, v114, v115
ds_read_b128 v[100:103], v127 offset:2384
ds_read_b128 v[108:111], v127 offset:80
s_waitcnt lgkmcnt(8)
v_mfma_f32_32x32x16_bf16 v[128:143], a[128:131], v[64:67], v[128:143]
s_waitcnt vmcnt(11)
ds_read_b64_tr_b16 a[144:145], v3 offset:16384
ds_read_b128 v[80:83], v9 offset:4096
ds_read_b128 v[84:87], v9 offset:5120
s_waitcnt lgkmcnt(8)
v_fma_mix_f32 v112, v88, v96, v104 op_sel_hi:[1,0,0]
v_fma_mix_f32 v113, v88, v97, v105 op_sel:[1,0,0] op_sel_hi:[1,0,0]
v_mfma_f32_32x32x16_bf16 v[144:159], a[128:131], v[68:71], v[144:159]
buffer_load_dwordx4 v125, s[36:39], s72 offen offset:3072 lds
ds_read_b64_tr_b16 a[146:147], v3 offset:16896
s_add_u32 m0, s35, 0
s_nop 0
buffer_load_dwordx4 v32, s[40:43], s83 offen lds
v_fma_mix_f32 v114, v89, v98, v106 op_sel_hi:[1,0,0]
v_mfma_f32_32x32x16_bf16 v[176:191], a[132:135], v[68:71], v[176:191]
ds_read_b64_tr_b16 a[148:149], v4 offset:16384
v_fma_mix_f32 v115, v89, v99, v107 op_sel:[1,0,0] op_sel_hi:[1,0,0]
v_cvt_pk_bf16_f32 v72, v112, v113
v_cvt_pk_bf16_f32 v73, v114, v115
v_fma_mix_f32 v112, v92, v96, v104 op_sel_hi:[1,0,0]
v_fma_mix_f32 v113, v92, v97, v105 op_sel:[1,0,0] op_sel_hi:[1,0,0]
v_fma_mix_f32 v114, v93, v98, v106 op_sel_hi:[1,0,0]
v_mfma_f32_32x32x16_bf16 v[160:175], a[132:135], v[64:67], v[160:175]
ds_read_b64_tr_b16 a[150:151], v4 offset:16896
buffer_load_dwordx4 v33, s[40:43], s83 offen offset:1024 lds
v_fma_mix_f32 v115, v93, v99, v107 op_sel:[1,0,0] op_sel_hi:[1,0,0]
v_cvt_pk_bf16_f32 v76, v112, v113
v_cvt_pk_bf16_f32 v77, v114, v115
s_waitcnt lgkmcnt(8)
v_mfma_f32_32x32x16_bf16 v[192:207], a[136:139], v[64:67], v[192:207]
ds_read_b64_tr_b16 a[152:153], v3 offset:18432
ds_read_b128 v[96:99], v127 offset:2432
ds_read_b128 v[104:107], v127 offset:128
s_waitcnt lgkmcnt(9)
v_fma_mix_f32 v112, v90, v100, v108 op_sel_hi:[1,0,0]
v_fma_mix_f32 v113, v90, v101, v109 op_sel:[1,0,0] op_sel_hi:[1,0,0]
v_fma_mix_f32 v114, v91, v102, v110 op_sel_hi:[1,0,0]
v_fma_mix_f32 v115, v91, v103, v111 op_sel:[1,0,0] op_sel_hi:[1,0,0]
v_mfma_f32_32x32x16_bf16 v[208:223], a[136:139], v[68:71], v[208:223]
ds_read_b64_tr_b16 a[154:155], v3 offset:18944
buffer_load_dwordx4 v34, s[40:43], s83 offen offset:2048 lds
v_cvt_pk_bf16_f32 v74, v112, v113
v_cvt_pk_bf16_f32 v75, v114, v115
)ASM"
R"ASM(
v_fma_mix_f32 v112, v94, v100, v108 op_sel_hi:[1,0,0]
v_fma_mix_f32 v113, v94, v101, v109 op_sel:[1,0,0] op_sel_hi:[1,0,0]
v_mfma_f32_32x32x16_bf16 v[240:255], a[140:143], v[68:71], v[240:255]
ds_read_b64_tr_b16 a[156:157], v4 offset:18432
v_fma_mix_f32 v114, v95, v102, v110 op_sel_hi:[1,0,0]
v_fma_mix_f32 v115, v95, v103, v111 op_sel:[1,0,0] op_sel_hi:[1,0,0]
v_cvt_pk_bf16_f32 v78, v112, v113
v_cvt_pk_bf16_f32 v79, v114, v115
ds_read_b128 v[100:103], v127 offset:2448
ds_read_b128 v[108:111], v127 offset:144
v_mfma_f32_32x32x16_bf16 v[224:239], a[140:143], v[64:67], v[224:239]
ds_read_b64_tr_b16 a[158:159], v4 offset:18944
buffer_load_dwordx4 v35, s[40:43], s83 offen offset:3072 lds
s_add_u32 s83, s83, 0x2000
s_and_b32 s83, s83, 0x1ffff
s_waitcnt lgkmcnt(8)
v_mfma_f32_32x32x16_bf16 v[128:143], a[144:147], v[72:75], v[128:143]
s_waitcnt vmcnt(10)
ds_read_b64_tr_b16 a[128:129], v3 offset:32768
ds_read_b128 v[88:91], v9 offset:6144
ds_read_b128 v[92:95], v9 offset:7168
s_waitcnt lgkmcnt(8)
v_fma_mix_f32 v112, v80, v96, v104 op_sel_hi:[1,0,0]
v_fma_mix_f32 v113, v80, v97, v105 op_sel:[1,0,0] op_sel_hi:[1,0,0]
v_fma_mix_f32 v114, v81, v98, v106 op_sel_hi:[1,0,0]
v_mfma_f32_32x32x16_bf16 v[144:159], a[144:147], v[76:79], v[144:159]
ds_read_b64_tr_b16 a[130:131], v3 offset:33280
s_add_u32 m0, s35, 0x4000
s_nop 0
buffer_load_dwordx4 v32, s[40:43], s83 offen lds
v_fma_mix_f32 v115, v81, v99, v107 op_sel:[1,0,0] op_sel_hi:[1,0,0]
v_cvt_pk_bf16_f32 v64, v112, v113
v_cvt_pk_bf16_f32 v65, v114, v115
v_mfma_f32_32x32x16_bf16 v[176:191], a[148:151], v[76:79], v[176:191]
ds_read_b64_tr_b16 a[132:133], v4 offset:32768
v_fma_mix_f32 v112, v84, v96, v104 op_sel_hi:[1,0,0]
v_fma_mix_f32 v113, v84, v97, v105 op_sel:[1,0,0] op_sel_hi:[1,0,0]
v_fma_mix_f32 v114, v85, v98, v106 op_sel_hi:[1,0,0]
v_fma_mix_f32 v115, v85, v99, v107 op_sel:[1,0,0] op_sel_hi:[1,0,0]
v_cvt_pk_bf16_f32 v68, v112, v113
v_cvt_pk_bf16_f32 v69, v114, v115
v_mfma_f32_32x32x16_bf16 v[160:175], a[148:151], v[72:75], v[160:175]
ds_read_b64_tr_b16 a[134:135], v4 offset:33280
buffer_load_dwordx4 v33, s[40:43], s83 offen offset:1024 lds
ds_read_b128 v[96:99], v127 offset:2496
ds_read_b128 v[104:107], v127 offset:192
s_waitcnt lgkmcnt(9)
v_fma_mix_f32 v112, v82, v100, v108 op_sel_hi:[1,0,0]
s_waitcnt lgkmcnt(8)
v_mfma_f32_32x32x16_bf16 v[192:207], a[152:155], v[72:75], v[192:207]
ds_read_b64_tr_b16 a[136:137], v3 offset:34816
v_fma_mix_f32 v113, v82, v101, v109 op_sel:[1,0,0] op_sel_hi:[1,0,0]
v_fma_mix_f32 v114, v83, v102, v110 op_sel_hi:[1,0,0]
v_fma_mix_f32 v115, v83, v103, v111 op_sel:[1,0,0] op_sel_hi:[1,0,0]
v_cvt_pk_bf16_f32 v66, v112, v113
v_cvt_pk_bf16_f32 v67, v114, v115
v_fma_mix_f32 v112, v86, v100, v108 op_sel_hi:[1,0,0]
v_mfma_f32_32x32x16_bf16 v[208:223], a[152:155], v[76:79], v[208:223]
ds_read_b64_tr_b16 a[138:139], v3 offset:35328
buffer_load_dwordx4 v34, s[40:43], s83 offen offset:2048 lds
v_fma_mix_f32 v113, v86, v101, v109 op_sel:[1,0,0] op_sel_hi:[1,0,0]
v_fma_mix_f32 v114, v87, v102, v110 op_sel_hi:[1,0,0]
v_fma_mix_f32 v115, v87, v103, v111 op_sel:[1,0,0] op_sel_hi:[1,0,0]
v_cvt_pk_bf16_f32 v70, v112, v113
v_mfma_f32_32x32x16_bf16 v[240:255], a[156:159], v[76:79], v[240:255]
ds_read_b64_tr_b16 a[140:141], v4 offset:34816
v_cvt_pk_bf16_f32 v71, v114, v115
ds_read_b128 v[100:103], v127 offset:2512
ds_read_b128 v[108:111], v127 offset:208
v_mfma_f32_32x32x16_bf16 v[224:239], a[156:159], v[72:75], v[224:239]
ds_read_b64_tr_b16 a[142:143], v4 offset:35328
buffer_load_dwordx4 v35, s[40:43], s83 offen offset:3072 lds
s_add_u32 s83, s83, 0x2000
s_and_b32 s83, s83, 0x1ffff
s_waitcnt lgkmcnt(8)
v_mfma_f32_32x32x16_bf16 v[128:143], a[128:131], v[64:67], v[128:143]
s_waitcnt vmcnt(8)
ds_read_b64_tr_b16 a[144:145], v3 offset:49152
s_waitcnt lgkmcnt(7)
v_fma_mix_f32 v112, v88, v96, v104 op_sel_hi:[1,0,0]
v_fma_mix_f32 v113, v88, v97, v105 op_sel:[1,0,0] op_sel_hi:[1,0,0]
v_fma_mix_f32 v114, v89, v98, v106 op_sel_hi:[1,0,0]
v_fma_mix_f32 v115, v89, v99, v107 op_sel:[1,0,0] op_sel_hi:[1,0,0]
v_cvt_pk_bf16_f32 v72, v112, v113
v_mfma_f32_32x32x16_bf16 v[144:159], a[128:131], v[68:71], v[144:159]
ds_read_b64_tr_b16 a[146:147], v3 offset:49664
s_add_u32 m0, s87, 0
s_nop 0
buffer_load_dwordx4 v36, s[44:47], s84 offen lds
v_cvt_pk_bf16_f32 v73, v114, v115
v_fma_mix_f32 v112, v92, v96, v104 op_sel_hi:[1,0,0]
v_fma_mix_f32 v113, v92, v97, v105 op_sel:[1,0,0] op_sel_hi:[1,0,0]
v_mfma_f32_32x32x16_bf16 v[176:191], a[132:135], v[68:71], v[176:191]
ds_read_b64_tr_b16 a[148:149], v4 offset:49152
v_fma_mix_f32 v114, v93, v98, v106 op_sel_hi:[1,0,0]
v_fma_mix_f32 v115, v93, v99, v107 op_sel:[1,0,0] op_sel_hi:[1,0,0]
v_cvt_pk_bf16_f32 v76, v112, v113
v_cvt_pk_bf16_f32 v77, v114, v115
s_waitcnt lgkmcnt(4)
v_fma_mix_f32 v112, v90, v100, v108 op_sel_hi:[1,0,0]
v_fma_mix_f32 v113, v90, v101, v109 op_sel:[1,0,0] op_sel_hi:[1,0,0]
v_mfma_f32_32x32x16_bf16 v[160:175], a[132:135], v[64:67], v[160:175]
ds_read_b64_tr_b16 a[150:151], v4 offset:49664
buffer_load_dwordx4 v37, s[44:47], s84 offen offset:1024 lds
v_fma_mix_f32 v114, v91, v102, v110 op_sel_hi:[1,0,0]
v_fma_mix_f32 v115, v91, v103, v111 op_sel:[1,0,0] op_sel_hi:[1,0,0]
v_cvt_pk_bf16_f32 v74, v112, v113
s_waitcnt lgkmcnt(4)
v_mfma_f32_32x32x16_bf16 v[192:207], a[136:139], v[64:67], v[192:207]
ds_read_b64_tr_b16 a[152:153], v3 offset:51200
v_cvt_pk_bf16_f32 v75, v114, v115
v_fma_mix_f32 v112, v94, v100, v108 op_sel_hi:[1,0,0]
v_fma_mix_f32 v113, v94, v101, v109 op_sel:[1,0,0] op_sel_hi:[1,0,0]
v_fma_mix_f32 v114, v95, v102, v110 op_sel_hi:[1,0,0]
v_fma_mix_f32 v115, v95, v103, v111 op_sel:[1,0,0] op_sel_hi:[1,0,0]
v_cvt_pk_bf16_f32 v78, v112, v113
v_mfma_f32_32x32x16_bf16 v[208:223], a[136:139], v[68:71], v[208:223]
ds_read_b64_tr_b16 a[154:155], v3 offset:51712
buffer_load_dwordx4 v38, s[44:47], s84 offen offset:2048 lds
v_cvt_pk_bf16_f32 v79, v114, v115
v_mfma_f32_32x32x16_bf16 v[240:255], a[140:143], v[68:71], v[240:255]
ds_read_b64_tr_b16 a[156:157], v4 offset:51200
v_mfma_f32_32x32x16_bf16 v[224:239], a[140:143], v[64:67], v[224:239]
ds_read_b64_tr_b16 a[158:159], v4 offset:51712
buffer_load_dwordx4 v39, s[44:47], s84 offen offset:3072 lds
s_add_u32 s84, s84, 0x2000
s_and_b32 s84, s84, 0x1ffff
s_waitcnt lgkmcnt(4)
v_mfma_f32_32x32x16_bf16 v[128:143], a[144:147], v[72:75], v[128:143]
v_mfma_f32_32x32x16_bf16 v[144:159], a[144:147], v[76:79], v[144:159]
s_add_u32 m0, s87, 0x4000
s_nop 0
buffer_load_dwordx4 v36, s[44:47], s84 offen lds
v_mfma_f32_32x32x16_bf16 v[176:191], a[148:151], v[76:79], v[176:191]
v_mfma_f32_32x32x16_bf16 v[160:175], a[148:151], v[72:75], v[160:175]
buffer_load_dwordx4 v37, s[44:47], s84 offen offset:1024 lds
s_waitcnt lgkmcnt(0)
v_mfma_f32_32x32x16_bf16 v[192:207], a[152:155], v[72:75], v[192:207]
v_mfma_f32_32x32x16_bf16 v[208:223], a[152:155], v[76:79], v[208:223]
buffer_load_dwordx4 v38, s[44:47], s84 offen offset:2048 lds
v_mfma_f32_32x32x16_bf16 v[240:255], a[156:159], v[76:79], v[240:255]
v_mfma_f32_32x32x16_bf16 v[224:239], a[156:159], v[72:75], v[224:239]
buffer_load_dwordx4 v39, s[44:47], s84 offen offset:3072 lds
s_add_u32 s84, s84, 0x2000
s_and_b32 s84, s84, 0x1ffff
s_load_dwordx4 s[24:27], %0, 0x40
s_load_dwordx2 s[28:29], %0, 0x50
s_lshl_b32 s19, s57, 2
s_add_u32 s19, s19, s52
s_mov_b32 s18, 0
v_cvt_pk_bf16_f32 v64, v128, v129
v_cvt_pk_bf16_f32 v65, v130, v131
v_cvt_pk_bf16_f32 v66, v132, v133
v_cvt_pk_bf16_f32 v67, v134, v135
v_cvt_pk_bf16_f32 v68, v136, v137
v_cvt_pk_bf16_f32 v69, v138, v139
v_cvt_pk_bf16_f32 v70, v140, v141
v_cvt_pk_bf16_f32 v71, v142, v143
v_cvt_pk_bf16_f32 v96, v144, v145
v_cvt_pk_bf16_f32 v97, v146, v147
v_cvt_pk_bf16_f32 v98, v148, v149
v_cvt_pk_bf16_f32 v99, v150, v151
v_cvt_pk_bf16_f32 v100, v152, v153
v_cvt_pk_bf16_f32 v101, v154, v155
v_cvt_pk_bf16_f32 v102, v156, v157
v_cvt_pk_bf16_f32 v103, v158, v159
v_cvt_pk_bf16_f32 v72, v160, v161
v_cvt_pk_bf16_f32 v73, v162, v163
v_cvt_pk_bf16_f32 v74, v164, v165
v_cvt_pk_bf16_f32 v75, v166, v167
v_cvt_pk_bf16_f32 v76, v168, v169
v_cvt_pk_bf16_f32 v77, v170, v171
v_cvt_pk_bf16_f32 v78, v172, v173
v_cvt_pk_bf16_f32 v79, v174, v175
v_cvt_pk_bf16_f32 v104, v176, v177
v_cvt_pk_bf16_f32 v105, v178, v179
v_cvt_pk_bf16_f32 v106, v180, v181
v_cvt_pk_bf16_f32 v107, v182, v183
v_cvt_pk_bf16_f32 v108, v184, v185
v_cvt_pk_bf16_f32 v109, v186, v187
v_cvt_pk_bf16_f32 v110, v188, v189
v_cvt_pk_bf16_f32 v111, v190, v191
v_cvt_pk_bf16_f32 v80, v192, v193
v_cvt_pk_bf16_f32 v81, v194, v195
v_cvt_pk_bf16_f32 v82, v196, v197
v_cvt_pk_bf16_f32 v83, v198, v199
v_cvt_pk_bf16_f32 v84, v200, v201
v_cvt_pk_bf16_f32 v85, v202, v203
v_cvt_pk_bf16_f32 v86, v204, v205
v_cvt_pk_bf16_f32 v87, v206, v207
v_cvt_pk_bf16_f32 v112, v208, v209
v_cvt_pk_bf16_f32 v113, v210, v211
v_cvt_pk_bf16_f32 v114, v212, v213
v_cvt_pk_bf16_f32 v115, v214, v215
v_cvt_pk_bf16_f32 v116, v216, v217
v_cvt_pk_bf16_f32 v117, v218, v219
v_cvt_pk_bf16_f32 v118, v220, v221
v_cvt_pk_bf16_f32 v119, v222, v223
v_cvt_pk_bf16_f32 v88, v224, v225
v_cvt_pk_bf16_f32 v89, v226, v227
)ASM"
R"ASM(
v_cvt_pk_bf16_f32 v90, v228, v229
v_cvt_pk_bf16_f32 v91, v230, v231
v_cvt_pk_bf16_f32 v92, v232, v233
v_cvt_pk_bf16_f32 v93, v234, v235
v_cvt_pk_bf16_f32 v94, v236, v237
v_cvt_pk_bf16_f32 v95, v238, v239
v_cvt_pk_bf16_f32 v120, v240, v241
v_cvt_pk_bf16_f32 v121, v242, v243
v_cvt_pk_bf16_f32 v122, v244, v245
v_cvt_pk_bf16_f32 v123, v246, v247
v_cvt_pk_bf16_f32 v124, v248, v249
v_cvt_pk_bf16_f32 v125, v250, v251
v_cvt_pk_bf16_f32 v126, v252, v253
v_cvt_pk_bf16_f32 v127, v254, v255
s_waitcnt lgkmcnt(0)
s_lshl_b32 s58, s56, 7
s_add_u32 s58, s58, s19
s_mul_i32 s58, s58, 0x51
s_lshr_b32 s58, s58, 4
s_lshl_b32 s58, s58, 10
s_add_u32 s36, s24, s58
s_addc_u32 s37, s25, 0
s_sub_u32 s36, s36, 0x400
s_subb_u32 s37, s37, 0
s_and_b32 s37, s37, 0xffff
s_sub_u32 s38, 0x510400, s58
s_mov_b32 s39, 0x20000
s_add_u32 s64, s26, s58
s_addc_u32 s65, s27, 0
s_sub_u32 s64, s64, 0x800
s_subb_u32 s65, s65, 0
s_and_b32 s65, s65, 0xffff
s_sub_u32 s66, 0x510800, s58
s_mov_b32 s67, 0x20000
s_add_u32 s20, s28, s58
s_addc_u32 s21, s29, 0
s_sub_u32 s20, s20, 0xc00
s_subb_u32 s21, s21, 0
s_and_b32 s21, s21, 0xffff
s_sub_u32 s22, 0x510c00, s58
s_mov_b32 s23, 0x20000
s_mul_i32 s58, s19, 9
s_lshr_b32 s58, s58, 1
s_lshl_b32 s58, s58, 10
s_add_u32 s58, s58, 0x1000
s_add_u32 s24, s48, s58
s_addc_u32 s25, s49, 0
s_sub_u32 s26, s50, s58
s_mov_b32 s27, 0x20000
.Lk1_attentry:
s_barrier
ds_read_b128 a[128:131], v48 offset:0
ds_read_b128 a[132:135], v49 offset:0
ds_read_b128 a[136:139], v50 offset:0
ds_read_b128 a[140:143], v51 offset:0
s_waitcnt lgkmcnt(3)
v_mfma_f32_32x32x16_bf16 v[128:143], a[128:131], v[64:67], 0
ds_read_b128 a[144:147], v48 offset:4096
s_add_u32 m0, s96, 0
v_mfma_f32_32x32x16_bf16 v[144:159], a[128:131], v[96:99], 0
buffer_load_dwordx4 v36, s[44:47], s84 offen lds
s_waitcnt lgkmcnt(3)
v_mfma_f32_32x32x16_bf16 v[128:143], a[132:135], v[68:71], v[128:143]
buffer_load_dwordx4 v37, s[44:47], s84 offen offset:1024 lds
ds_read_b128 a[148:151], v49 offset:4096
v_mfma_f32_32x32x16_bf16 v[144:159], a[132:135], v[100:103], v[144:159]
buffer_load_dwordx4 v38, s[44:47], s84 offen offset:2048 lds
s_waitcnt lgkmcnt(2)
v_mfma_f32_32x32x16_bf16 v[128:143], a[136:139], v[72:75], v[128:143]
buffer_load_dwordx4 v39, s[44:47], s84 offen offset:3072 lds
s_add_u32 s84, s84, 0x2000
s_and_b32 s84, s84, 0x1ffff
ds_read_b128 a[152:155], v50 offset:4096
s_add_u32 m0, s96, 0x4000
v_mfma_f32_32x32x16_bf16 v[144:159], a[136:139], v[104:107], v[144:159]
buffer_load_dwordx4 v36, s[44:47], s84 offen lds
v_mfma_f32_32x32x16_bf16 v[128:143], a[140:143], v[76:79], v[128:143]
buffer_load_dwordx4 v37, s[44:47], s84 offen offset:1024 lds
ds_read_b128 a[156:159], v51 offset:4096
v_mfma_f32_32x32x16_bf16 v[144:159], a[140:143], v[108:111], v[144:159]
buffer_load_dwordx4 v38, s[44:47], s84 offen offset:2048 lds
s_waitcnt lgkmcnt(2)
v_mfma_f32_32x32x16_bf16 v[128:143], a[144:147], v[80:83], v[128:143]
buffer_load_dwordx4 v39, s[44:47], s84 offen offset:3072 lds
s_add_u32 s84, s84, 0x2000
s_and_b32 s84, s84, 0x1ffff
ds_read_b128 a[128:131], v48 offset:16384
v_mfma_f32_32x32x16_bf16 v[144:159], a[144:147], v[112:115], v[144:159]
v_mfma_f32_32x32x16_bf16 v[128:143], a[148:151], v[84:87], v[128:143]
ds_read_b128 a[132:135], v49 offset:16384
v_mfma_f32_32x32x16_bf16 v[144:159], a[148:151], v[116:119], v[144:159]
s_waitcnt lgkmcnt(2)
v_mfma_f32_32x32x16_bf16 v[128:143], a[152:155], v[88:91], v[128:143]
ds_read_b128 a[136:139], v50 offset:16384
v_mfma_f32_32x32x16_bf16 v[144:159], a[152:155], v[120:123], v[144:159]
v_mfma_f32_32x32x16_bf16 v[128:143], a[156:159], v[92:95], v[128:143]
ds_read_b128 a[140:143], v51 offset:16384
v_mfma_f32_32x32x16_bf16 v[144:159], a[156:159], v[124:127], v[144:159]
s_waitcnt lgkmcnt(2)
s_waitcnt vmcnt(12)
s_barrier
v_mfma_f32_32x32x16_bf16 v[160:175], a[128:131], v[64:67], 0
ds_read_b128 a[144:147], v48 offset:20480
s_add_u32 m0, s34, 0
v_mfma_f32_32x32x16_bf16 v[176:191], a[128:131], v[96:99], 0
buffer_load_dwordx4 v32, s[40:43], s83 offen lds
v_mfma_f32_32x32x16_bf16 v[160:175], a[132:135], v[68:71], v[160:175]
ds_read_b128 a[148:151], v49 offset:20480
v_max3_f32 v13, v128, v129, v62
s_nop 0
v_max3_f32 v14, v144, v145, v63
v_max3_f32 v13, v130, v131, v13
v_max3_f32 v14, v146, v147, v14
v_max3_f32 v13, v132, v133, v13
v_mfma_f32_32x32x16_bf16 v[176:191], a[132:135], v[100:103], v[176:191]
buffer_load_dwordx4 v33, s[40:43], s83 offen offset:1024 lds
v_max3_f32 v14, v148, v149, v14
v_max3_f32 v13, v134, v135, v13
v_max3_f32 v14, v150, v151, v14
s_waitcnt lgkmcnt(2)
v_mfma_f32_32x32x16_bf16 v[160:175], a[136:139], v[72:75], v[160:175]
ds_read_b128 a[152:155], v50 offset:20480
v_max3_f32 v13, v136, v137, v13
v_max3_f32 v14, v152, v153, v14
v_max3_f32 v13, v138, v139, v13
v_max3_f32 v14, v154, v155, v14
v_max3_f32 v13, v140, v141, v13
v_mfma_f32_32x32x16_bf16 v[176:191], a[136:139], v[104:107], v[176:191]
buffer_load_dwordx4 v34, s[40:43], s83 offen offset:2048 lds
v_max3_f32 v14, v156, v157, v14
v_max3_f32 v13, v142, v143, v13
v_max3_f32 v14, v158, v159, v14
v_mov_b32 v225, v13
v_mfma_f32_32x32x16_bf16 v[160:175], a[140:143], v[76:79], v[160:175]
ds_read_b128 a[156:159], v51 offset:20480
v_mov_b32 v241, v14
s_nop 0
v_permlane32_swap_b32 v13, v225
v_permlane32_swap_b32 v14, v241
v_max_f32 v13, v13, v225
v_mfma_f32_32x32x16_bf16 v[176:191], a[140:143], v[108:111], v[176:191]
buffer_load_dwordx4 v35, s[40:43], s83 offen offset:3072 lds
s_add_u32 s83, s83, 0x2000
s_and_b32 s83, s83, 0x1ffff
v_max_f32 v14, v14, v241
s_waitcnt lgkmcnt(2)
v_mfma_f32_32x32x16_bf16 v[160:175], a[144:147], v[80:83], v[160:175]
ds_read_b64_tr_b16 a[128:129], v56 offset:0
v_sub_f32 v224, 0, v13
v_sub_f32 v225, 0, v13
v_sub_f32 v240, 0, v14
v_sub_f32 v241, 0, v14
v_mfma_f32_32x32x16_bf16 v[176:191], a[144:147], v[112:115], v[176:191]
ds_read_b64_tr_b16 a[130:131], v56 offset:512
v_pk_add_f32 v[128:129], v[128:129], v[224:225]
v_exp_f32 v128, v128
v_exp_f32 v129, v129
v_mfma_f32_32x32x16_bf16 v[160:175], a[148:151], v[84:87], v[160:175]
ds_read_b64_tr_b16 a[132:133], v57 offset:0
v_pk_add_f32 v[130:131], v[130:131], v[224:225]
v_exp_f32 v130, v130
v_exp_f32 v131, v131
v_mfma_f32_32x32x16_bf16 v[176:191], a[148:151], v[116:119], v[176:191]
ds_read_b64_tr_b16 a[134:135], v57 offset:512
v_cvt_pk_bf16_f32 v192, v128, v129
v_mov_b32 v60, v128
v_pk_add_f32 v[132:133], v[132:133], v[224:225]
v_exp_f32 v132, v132
s_waitcnt lgkmcnt(4)
v_mfma_f32_32x32x16_bf16 v[160:175], a[152:155], v[88:91], v[160:175]
ds_read_b64_tr_b16 a[136:137], v56 offset:4096
v_add_f32 v60, v60, v129
v_exp_f32 v133, v133
v_cvt_pk_bf16_f32 v193, v130, v131
v_add_f32 v60, v60, v130
v_mfma_f32_32x32x16_bf16 v[176:191], a[152:155], v[120:123], v[176:191]
ds_read_b64_tr_b16 a[138:139], v56 offset:4608
v_pk_add_f32 v[134:135], v[134:135], v[224:225]
v_exp_f32 v134, v134
v_add_f32 v60, v60, v131
v_cvt_pk_bf16_f32 v194, v132, v133
v_mfma_f32_32x32x16_bf16 v[160:175], a[156:159], v[92:95], v[160:175]
ds_read_b64_tr_b16 a[140:141], v57 offset:4096
v_exp_f32 v135, v135
v_add_f32 v60, v60, v132
v_pk_add_f32 v[144:145], v[144:145], v[240:241]
v_exp_f32 v144, v144
v_mfma_f32_32x32x16_bf16 v[176:191], a[156:159], v[124:127], v[176:191]
ds_read_b64_tr_b16 a[142:143], v57 offset:4608
v_add_f32 v60, v60, v133
v_exp_f32 v145, v145
v_cvt_pk_bf16_f32 v195, v134, v135
s_waitcnt lgkmcnt(4)
v_cvt_pk_bf16_f32 v200, v144, v145
v_pk_add_f32 v[146:147], v[146:147], v[240:241]
v_exp_f32 v146, v146
v_exp_f32 v147, v147
v_pk_add_f32 v[148:149], v[148:149], v[240:241]
v_exp_f32 v148, v148
v_cvt_pk_bf16_f32 v201, v146, v147
)ASM"
R"ASM(
v_exp_f32 v149, v149
v_pk_add_f32 v[150:151], v[150:151], v[240:241]
v_exp_f32 v150, v150
v_cvt_pk_bf16_f32 v202, v148, v149
v_exp_f32 v151, v151
s_nop 0
v_cvt_pk_bf16_f32 v203, v150, v151
v_mfma_f32_32x32x16_bf16 a[0:15], a[128:131], v[192:195], 0
ds_read_b64_tr_b16 a[144:145], v56 offset:2048
v_add_f32 v60, v60, v134
v_add_f32 v60, v60, v135
v_mov_b32 v61, v144
v_add_f32 v61, v61, v145
v_add_f32 v61, v61, v146
v_add_f32 v61, v61, v147
v_add_f32 v61, v61, v148
v_mfma_f32_32x32x16_bf16 a[16:31], a[128:131], v[200:203], 0
ds_read_b64_tr_b16 a[146:147], v56 offset:2560
v_pk_add_f32 v[136:137], v[136:137], v[224:225]
v_exp_f32 v136, v136
v_add_f32 v61, v61, v149
v_exp_f32 v137, v137
v_add_f32 v61, v61, v150
v_mfma_f32_32x32x16_bf16 a[48:63], a[132:135], v[200:203], 0
ds_read_b64_tr_b16 a[148:149], v57 offset:2048
v_pk_add_f32 v[138:139], v[138:139], v[224:225]
v_exp_f32 v138, v138
v_add_f32 v61, v61, v151
v_exp_f32 v139, v139
v_cvt_pk_bf16_f32 v196, v136, v137
v_mfma_f32_32x32x16_bf16 a[32:47], a[132:135], v[192:195], 0
ds_read_b64_tr_b16 a[150:151], v57 offset:2560
v_add_f32 v60, v60, v136
v_pk_add_f32 v[140:141], v[140:141], v[224:225]
v_exp_f32 v140, v140
v_add_f32 v60, v60, v137
v_cvt_pk_bf16_f32 v197, v138, v139
s_waitcnt lgkmcnt(4)
v_mfma_f32_32x32x16_bf16 a[64:79], a[136:139], v[192:195], 0
ds_read_b64_tr_b16 a[152:153], v56 offset:6144
v_exp_f32 v141, v141
v_add_f32 v60, v60, v138
v_pk_add_f32 v[142:143], v[142:143], v[224:225]
v_exp_f32 v142, v142
v_add_f32 v60, v60, v139
v_mfma_f32_32x32x16_bf16 a[80:95], a[136:139], v[200:203], 0
ds_read_b64_tr_b16 a[154:155], v56 offset:6656
v_exp_f32 v143, v143
v_cvt_pk_bf16_f32 v198, v140, v141
v_add_f32 v60, v60, v140
v_pk_add_f32 v[152:153], v[152:153], v[240:241]
v_exp_f32 v152, v152
v_mfma_f32_32x32x16_bf16 a[112:127], a[140:143], v[200:203], 0
ds_read_b64_tr_b16 a[156:157], v57 offset:6144
v_add_f32 v60, v60, v141
v_exp_f32 v153, v153
v_cvt_pk_bf16_f32 v199, v142, v143
v_add_f32 v60, v60, v142
v_pk_add_f32 v[154:155], v[154:155], v[240:241]
v_exp_f32 v154, v154
v_mfma_f32_32x32x16_bf16 a[96:111], a[140:143], v[192:195], 0
ds_read_b64_tr_b16 a[158:159], v57 offset:6656
v_add_f32 v60, v60, v143
v_exp_f32 v155, v155
v_cvt_pk_bf16_f32 v204, v152, v153
v_add_f32 v61, v61, v152
v_add_f32 v61, v61, v153
v_cvt_pk_bf16_f32 v205, v154, v155
v_pk_add_f32 v[156:157], v[156:157], v[240:241]
v_exp_f32 v156, v156
v_exp_f32 v157, v157
v_pk_add_f32 v[158:159], v[158:159], v[240:241]
v_exp_f32 v158, v158
v_cvt_pk_bf16_f32 v206, v156, v157
v_exp_f32 v159, v159
s_nop 0
v_cvt_pk_bf16_f32 v207, v158, v159
s_waitcnt lgkmcnt(4)
v_mfma_f32_32x32x16_bf16 a[0:15], a[144:147], v[196:199], a[0:15]
ds_read_b128 a[128:131], v52 offset:0
v_add_f32 v61, v61, v154
v_add_f32 v61, v61, v155
v_add_f32 v61, v61, v156
v_add_f32 v61, v61, v157
v_add_f32 v61, v61, v158
v_add_f32 v61, v61, v159
v_pk_add_f32 v[160:161], v[160:161], v[224:225]
v_pk_add_f32 v[162:163], v[162:163], v[224:225]
v_mfma_f32_32x32x16_bf16 a[16:31], a[144:147], v[204:207], a[16:31]
v_pk_add_f32 v[164:165], v[164:165], v[224:225]
v_pk_add_f32 v[166:167], v[166:167], v[224:225]
v_pk_add_f32 v[176:177], v[176:177], v[240:241]
v_pk_add_f32 v[178:179], v[178:179], v[240:241]
v_pk_add_f32 v[180:181], v[180:181], v[240:241]
v_pk_add_f32 v[182:183], v[182:183], v[240:241]
v_pk_add_f32 v[168:169], v[168:169], v[224:225]
v_pk_add_f32 v[170:171], v[170:171], v[224:225]
v_pk_add_f32 v[172:173], v[172:173], v[224:225]
v_mfma_f32_32x32x16_bf16 a[48:63], a[148:151], v[204:207], a[48:63]
ds_read_b128 a[132:135], v53 offset:0
v_pk_add_f32 v[174:175], v[174:175], v[224:225]
v_pk_add_f32 v[184:185], v[184:185], v[240:241]
v_pk_add_f32 v[186:187], v[186:187], v[240:241]
v_pk_add_f32 v[188:189], v[188:189], v[240:241]
v_pk_add_f32 v[190:191], v[190:191], v[240:241]
v_mov_b64 v[226:227], v[224:225]
v_mov_b64 v[228:229], v[224:225]
v_mov_b64 v[230:231], v[224:225]
v_mfma_f32_32x32x16_bf16 a[32:47], a[148:151], v[196:199], a[32:47]
v_mov_b64 v[232:233], v[224:225]
v_mov_b64 v[234:235], v[224:225]
v_mov_b64 v[236:237], v[224:225]
v_mov_b64 v[238:239], v[224:225]
v_mov_b64 v[242:243], v[240:241]
v_mov_b64 v[244:245], v[240:241]
v_mov_b64 v[246:247], v[240:241]
v_mov_b64 v[248:249], v[240:241]
s_waitcnt lgkmcnt(2)
v_mfma_f32_32x32x16_bf16 a[64:79], a[152:155], v[196:199], a[64:79]
ds_read_b128 a[136:139], v54 offset:0
v_mov_b64 v[250:251], v[240:241]
v_mov_b64 v[252:253], v[240:241]
v_mov_b64 v[254:255], v[240:241]
v_mfma_f32_32x32x16_bf16 a[80:95], a[152:155], v[204:207], a[80:95]
v_mfma_f32_32x32x16_bf16 a[112:127], a[156:159], v[204:207], a[112:127]
ds_read_b128 a[140:143], v55 offset:0
v_mfma_f32_32x32x16_bf16 a[96:111], a[156:159], v[196:199], a[96:111]
s_waitcnt lgkmcnt(2)
s_waitcnt vmcnt(0)
s_cmp_lg_u32 s52, 3
s_cbranch_scc1 .Lk1_fin_skip
s_sub_u32 s59, %2, 0xd8
s_cmp_lt_u32 s59, 36
s_cbranch_scc0 .Lk1_fin_skip
v_accvgpr_read_b32 v13, a252
v_accvgpr_read_b32 v14, a253
s_lshl_b32 s58, s57, 12
s_sub_u32 s12, s12, s58
s_subb_u32 s13, s13, 0
s_mov_b32 s14, 0x20000
s_lshl_b32 s59, s59, 2
buffer_store_dword v13, v14, s[12:15], s59 offen offset:1856 sc1
s_add_u32 s12, s12, s58
s_addc_u32 s13, s13, 0
s_mov_b32 s14, 0x1000
.Lk1_fin_skip:
v_exp_f32 v160, v160
v_exp_f32 v161, v161
v_add_f32 v60, v60, v160
v_cvt_pk_bf16_f32 v208, v160, v161
v_add_f32 v60, v60, v161
v_exp_f32 v162, v162
v_exp_f32 v163, v163
v_add_f32 v60, v60, v162
v_cvt_pk_bf16_f32 v209, v162, v163
v_add_f32 v60, v60, v163
v_exp_f32 v164, v164
v_exp_f32 v165, v165
v_add_f32 v60, v60, v164
v_cvt_pk_bf16_f32 v210, v164, v165
v_add_f32 v60, v60, v165
v_exp_f32 v166, v166
v_exp_f32 v167, v167
v_add_f32 v60, v60, v166
v_cvt_pk_bf16_f32 v211, v166, v167
v_add_f32 v60, v60, v167
v_exp_f32 v176, v176
v_exp_f32 v177, v177
v_add_f32 v61, v61, v176
v_cvt_pk_bf16_f32 v216, v176, v177
v_add_f32 v61, v61, v177
v_exp_f32 v178, v178
v_exp_f32 v179, v179
v_exp_f32 v180, v180
v_cvt_pk_bf16_f32 v217, v178, v179
v_exp_f32 v181, v181
s_mov_b32 s54, 5
.Lk1_att:
s_waitcnt vmcnt(16)
s_barrier
v_mfma_f32_32x32x16_bf16 v[128:143], a[128:131], v[64:67], v[224:239]
ds_read_b128 a[144:147], v52 offset:4096
v_add_f32 v61, v61, v178
v_exp_f32 v182, v182
v_add_f32 v61, v61, v179
s_add_u32 m0, s34, 0x4000
v_mfma_f32_32x32x16_bf16 v[144:159], a[128:131], v[96:99], v[240:255]
buffer_load_dwordx4 v32, s[40:43], s83 offen lds
v_exp_f32 v183, v183
v_cvt_pk_bf16_f32 v218, v180, v181
v_add_f32 v61, v61, v180
v_mfma_f32_32x32x16_bf16 v[128:143], a[132:135], v[68:71], v[128:143]
ds_read_b128 a[148:151], v53 offset:4096
v_exp_f32 v168, v168
v_add_f32 v61, v61, v181
v_exp_f32 v169, v169
v_mfma_f32_32x32x16_bf16 v[144:159], a[132:135], v[100:103], v[144:159]
buffer_load_dwordx4 v33, s[40:43], s83 offen offset:1024 lds
v_cvt_pk_bf16_f32 v219, v182, v183
v_add_f32 v61, v61, v182
)ASM"
R"ASM(
v_add_f32 v61, v61, v183
s_waitcnt lgkmcnt(2)
v_mfma_f32_32x32x16_bf16 v[128:143], a[136:139], v[72:75], v[128:143]
ds_read_b128 a[152:155], v54 offset:4096
v_exp_f32 v170, v170
v_exp_f32 v171, v171
v_cvt_pk_bf16_f32 v212, v168, v169
v_mfma_f32_32x32x16_bf16 v[144:159], a[136:139], v[104:107], v[144:159]
buffer_load_dwordx4 v34, s[40:43], s83 offen offset:2048 lds
v_add_f32 v60, v60, v168
v_exp_f32 v172, v172
v_add_f32 v60, v60, v169
v_mfma_f32_32x32x16_bf16 v[128:143], a[140:143], v[76:79], v[128:143]
ds_read_b128 a[156:159], v55 offset:4096
v_exp_f32 v173, v173
v_cvt_pk_bf16_f32 v213, v170, v171
v_add_f32 v60, v60, v170
v_add_f32 v60, v60, v171
v_mfma_f32_32x32x16_bf16 v[144:159], a[140:143], v[108:111], v[144:159]
buffer_load_dwordx4 v35, s[40:43], s83 offen offset:3072 lds
s_add_u32 s83, s83, 0x2000
s_and_b32 s83, s83, 0x1ffff
v_cvt_pk_bf16_f32 v214, v172, v173
s_waitcnt lgkmcnt(2)
v_mfma_f32_32x32x16_bf16 v[128:143], a[144:147], v[80:83], v[128:143]
ds_read_b64_tr_b16 a[128:129], v56 offset:16384
v_exp_f32 v174, v174
v_exp_f32 v175, v175
s_add_u32 m0, s63, 0
v_mfma_f32_32x32x16_bf16 v[144:159], a[144:147], v[112:115], v[144:159]
buffer_load_dwordx4 v2, s[24:27], s18 offen lds
ds_read_b64_tr_b16 a[130:131], v56 offset:16896
v_add_f32 v60, v60, v172
v_exp_f32 v184, v184
v_mfma_f32_32x32x16_bf16 v[128:143], a[148:151], v[84:87], v[128:143]
ds_read_b64_tr_b16 a[132:133], v57 offset:16384
v_add_f32 v60, v60, v173
v_exp_f32 v185, v185
v_cvt_pk_bf16_f32 v215, v174, v175
v_add_f32 v60, v60, v174
v_mfma_f32_32x32x16_bf16 v[144:159], a[148:151], v[116:119], v[144:159]
buffer_load_dwordx4 v2, s[36:39], s18 offen offset:1024 lds
ds_read_b64_tr_b16 a[134:135], v57 offset:16896
v_exp_f32 v186, v186
s_waitcnt lgkmcnt(4)
v_mfma_f32_32x32x16_bf16 v[128:143], a[152:155], v[88:91], v[128:143]
ds_read_b64_tr_b16 a[136:137], v56 offset:20480
v_add_f32 v60, v60, v175
v_exp_f32 v187, v187
v_cvt_pk_bf16_f32 v220, v184, v185
v_add_f32 v61, v61, v184
v_mfma_f32_32x32x16_bf16 v[144:159], a[152:155], v[120:123], v[144:159]
ds_read_b64_tr_b16 a[138:139], v56 offset:20992
v_exp_f32 v188, v188
v_add_f32 v61, v61, v185
v_cvt_pk_bf16_f32 v221, v186, v187
v_add_f32 v61, v61, v186
v_mfma_f32_32x32x16_bf16 v[128:143], a[156:159], v[92:95], v[128:143]
ds_read_b64_tr_b16 a[140:141], v57 offset:20480
v_exp_f32 v189, v189
v_add_f32 v61, v61, v187
v_cvt_pk_bf16_f32 v222, v188, v189
v_add_f32 v61, v61, v188
v_mfma_f32_32x32x16_bf16 v[144:159], a[156:159], v[124:127], v[144:159]
ds_read_b64_tr_b16 a[142:143], v57 offset:20992
v_exp_f32 v190, v190
v_add_f32 v61, v61, v189
v_add_f32 v61, v61, v190
s_waitcnt lgkmcnt(4)
v_mfma_f32_32x32x16_bf16 a[0:15], a[128:131], v[208:211], a[0:15]
ds_read_b64_tr_b16 a[144:145], v56 offset:18432
v_exp_f32 v191, v191
v_mfma_f32_32x32x16_bf16 a[16:31], a[128:131], v[216:219], a[16:31]
ds_read_b64_tr_b16 a[146:147], v56 offset:18944
v_cvt_pk_bf16_f32 v223, v190, v191
v_add_f32 v61, v61, v191
v_mfma_f32_32x32x16_bf16 a[48:63], a[132:135], v[216:219], a[48:63]
ds_read_b64_tr_b16 a[148:149], v57 offset:18432
v_exp_f32 v128, v128
v_exp_f32 v129, v129
v_add_f32 v60, v60, v128
v_mfma_f32_32x32x16_bf16 a[32:47], a[132:135], v[208:211], a[32:47]
ds_read_b64_tr_b16 a[150:151], v57 offset:18944
v_exp_f32 v130, v130
v_cvt_pk_bf16_f32 v192, v128, v129
v_add_f32 v60, v60, v129
s_waitcnt lgkmcnt(4)
v_mfma_f32_32x32x16_bf16 a[64:79], a[136:139], v[208:211], a[64:79]
ds_read_b64_tr_b16 a[152:153], v56 offset:22528
v_exp_f32 v131, v131
v_add_f32 v60, v60, v130
v_cvt_pk_bf16_f32 v193, v130, v131
v_add_f32 v60, v60, v131
v_mfma_f32_32x32x16_bf16 a[80:95], a[136:139], v[216:219], a[80:95]
ds_read_b64_tr_b16 a[154:155], v56 offset:23040
v_exp_f32 v132, v132
v_mfma_f32_32x32x16_bf16 a[112:127], a[140:143], v[216:219], a[112:127]
ds_read_b64_tr_b16 a[156:157], v57 offset:22528
v_exp_f32 v133, v133
v_add_f32 v60, v60, v132
v_cvt_pk_bf16_f32 v194, v132, v133
v_add_f32 v60, v60, v133
v_mfma_f32_32x32x16_bf16 a[96:111], a[140:143], v[208:211], a[96:111]
ds_read_b64_tr_b16 a[158:159], v57 offset:23040
v_exp_f32 v134, v134
s_waitcnt lgkmcnt(4)
v_mfma_f32_32x32x16_bf16 a[0:15], a[144:147], v[212:215], a[0:15]
ds_read_b128 a[128:131], v52 offset:16384
v_exp_f32 v135, v135
v_add_f32 v60, v60, v134
v_cvt_pk_bf16_f32 v195, v134, v135
s_add_u32 m0, s87, 0
v_mfma_f32_32x32x16_bf16 a[16:31], a[144:147], v[220:223], a[16:31]
buffer_load_dwordx4 v36, s[44:47], s84 offen lds
v_exp_f32 v144, v144
v_add_f32 v60, v60, v135
v_add_f32 v61, v61, v144
v_mfma_f32_32x32x16_bf16 a[48:63], a[148:151], v[220:223], a[48:63]
ds_read_b128 a[132:135], v53 offset:16384
v_exp_f32 v145, v145
v_mfma_f32_32x32x16_bf16 a[32:47], a[148:151], v[212:215], a[32:47]
buffer_load_dwordx4 v37, s[44:47], s84 offen offset:1024 lds
v_exp_f32 v146, v146
v_cvt_pk_bf16_f32 v200, v144, v145
s_waitcnt lgkmcnt(2)
v_mfma_f32_32x32x16_bf16 a[64:79], a[152:155], v[212:215], a[64:79]
ds_read_b128 a[136:139], v54 offset:16384
v_exp_f32 v147, v147
v_add_f32 v61, v61, v145
v_cvt_pk_bf16_f32 v201, v146, v147
v_add_f32 v61, v61, v146
v_mfma_f32_32x32x16_bf16 a[80:95], a[152:155], v[220:223], a[80:95]
buffer_load_dwordx4 v38, s[44:47], s84 offen offset:2048 lds
v_exp_f32 v148, v148
v_add_f32 v61, v61, v147
v_add_f32 v61, v61, v148
v_mfma_f32_32x32x16_bf16 a[112:127], a[156:159], v[220:223], a[112:127]
ds_read_b128 a[140:143], v55 offset:16384
v_exp_f32 v149, v149
v_mfma_f32_32x32x16_bf16 a[96:111], a[156:159], v[212:215], a[96:111]
buffer_load_dwordx4 v39, s[44:47], s84 offen offset:3072 lds
s_add_u32 s84, s84, 0x2000
s_and_b32 s84, s84, 0x1ffff
v_cvt_pk_bf16_f32 v202, v148, v149
s_waitcnt lgkmcnt(2)
s_waitcnt vmcnt(16)
s_barrier
v_mfma_f32_32x32x16_bf16 v[160:175], a[128:131], v[64:67], v[224:239]
ds_read_b128 a[144:147], v52 offset:20480
v_exp_f32 v150, v150
v_exp_f32 v151, v151
s_add_u32 m0, s35, 0
v_mfma_f32_32x32x16_bf16 v[176:191], a[128:131], v[96:99], v[240:255]
buffer_load_dwordx4 v32, s[40:43], s83 offen lds
v_exp_f32 v136, v136
v_add_f32 v61, v61, v149
v_cvt_pk_bf16_f32 v203, v150, v151
v_mfma_f32_32x32x16_bf16 v[160:175], a[132:135], v[68:71], v[160:175]
ds_read_b128 a[148:151], v53 offset:20480
v_exp_f32 v137, v137
v_add_f32 v61, v61, v150
v_add_f32 v61, v61, v151
v_cvt_pk_bf16_f32 v196, v136, v137
v_mfma_f32_32x32x16_bf16 v[176:191], a[132:135], v[100:103], v[176:191]
buffer_load_dwordx4 v33, s[40:43], s83 offen offset:1024 lds
v_exp_f32 v138, v138
v_add_f32 v60, v60, v136
s_waitcnt lgkmcnt(2)
v_mfma_f32_32x32x16_bf16 v[160:175], a[136:139], v[72:75], v[160:175]
ds_read_b128 a[152:155], v54 offset:20480
v_exp_f32 v139, v139
v_add_f32 v60, v60, v137
v_cvt_pk_bf16_f32 v197, v138, v139
v_add_f32 v60, v60, v138
v_mfma_f32_32x32x16_bf16 v[176:191], a[136:139], v[104:107], v[176:191]
buffer_load_dwordx4 v34, s[40:43], s83 offen offset:2048 lds
v_exp_f32 v140, v140
v_add_f32 v60, v60, v139
v_add_f32 v60, v60, v140
v_mfma_f32_32x32x16_bf16 v[160:175], a[140:143], v[76:79], v[160:175]
ds_read_b128 a[156:159], v55 offset:20480
v_exp_f32 v141, v141
v_mfma_f32_32x32x16_bf16 v[176:191], a[140:143], v[108:111], v[176:191]
buffer_load_dwordx4 v35, s[40:43], s83 offen offset:3072 lds
s_add_u32 s83, s83, 0x2000
s_and_b32 s83, s83, 0x1ffff
v_cvt_pk_bf16_f32 v198, v140, v141
s_waitcnt lgkmcnt(2)
v_mfma_f32_32x32x16_bf16 v[160:175], a[144:147], v[80:83], v[160:175]
ds_read_b64_tr_b16 a[128:129], v58 offset:0
v_exp_f32 v142, v142
v_exp_f32 v143, v143
s_add_u32 m0, s63, 0
v_mfma_f32_32x32x16_bf16 v[176:191], a[144:147], v[112:115], v[176:191]
buffer_load_dwordx4 v2, s[64:67], s18 offen offset:2048 lds
ds_read_b64_tr_b16 a[130:131], v58 offset:512
v_exp_f32 v152, v152
v_add_f32 v60, v60, v141
v_mfma_f32_32x32x16_bf16 v[160:175], a[148:151], v[84:87], v[160:175]
ds_read_b64_tr_b16 a[132:133], v59 offset:0
)ASM"
R"ASM(
s_xor_b32 s34, s34, 0x8000
v_exp_f32 v153, v153
v_cvt_pk_bf16_f32 v199, v142, v143
v_add_f32 v60, v60, v142
v_mfma_f32_32x32x16_bf16 v[176:191], a[148:151], v[116:119], v[176:191]
buffer_load_dwordx4 v2, s[20:23], s18 offen offset:3072 lds
s_add_u32 s18, s18, 0x400
ds_read_b64_tr_b16 a[134:135], v59 offset:512
v_add_f32 v60, v60, v143
s_waitcnt lgkmcnt(4)
v_mfma_f32_32x32x16_bf16 v[160:175], a[152:155], v[88:91], v[160:175]
ds_read_b64_tr_b16 a[136:137], v58 offset:4096
s_xor_b32 s35, s35, 0x8000
v_exp_f32 v154, v154
v_exp_f32 v155, v155
v_mfma_f32_32x32x16_bf16 v[176:191], a[152:155], v[120:123], v[176:191]
ds_read_b64_tr_b16 a[138:139], v58 offset:4608
v_cvt_pk_bf16_f32 v204, v152, v153
v_add_f32 v61, v61, v152
v_exp_f32 v156, v156
v_add_f32 v61, v61, v153
v_mfma_f32_32x32x16_bf16 v[160:175], a[156:159], v[92:95], v[160:175]
ds_read_b64_tr_b16 a[140:141], v59 offset:4096
v_exp_f32 v157, v157
v_cvt_pk_bf16_f32 v205, v154, v155
v_add_f32 v61, v61, v154
v_add_f32 v61, v61, v155
v_mfma_f32_32x32x16_bf16 v[176:191], a[156:159], v[124:127], v[176:191]
ds_read_b64_tr_b16 a[142:143], v59 offset:4608
v_exp_f32 v158, v158
v_cvt_pk_bf16_f32 v206, v156, v157
v_add_f32 v61, v61, v156
s_waitcnt lgkmcnt(4)
v_mfma_f32_32x32x16_bf16 a[0:15], a[128:131], v[192:195], a[0:15]
ds_read_b64_tr_b16 a[144:145], v58 offset:2048
v_xor_b32 v52, 0x8000, v52
v_exp_f32 v159, v159
v_add_f32 v61, v61, v157
v_cvt_pk_bf16_f32 v207, v158, v159
v_mfma_f32_32x32x16_bf16 a[16:31], a[128:131], v[200:203], a[16:31]
ds_read_b64_tr_b16 a[146:147], v58 offset:2560
v_add_f32 v61, v61, v158
v_add_f32 v61, v61, v159
v_mfma_f32_32x32x16_bf16 a[48:63], a[132:135], v[200:203], a[48:63]
ds_read_b64_tr_b16 a[148:149], v59 offset:2048
v_xor_b32 v53, 0x8000, v53
v_exp_f32 v160, v160
v_exp_f32 v161, v161
v_mfma_f32_32x32x16_bf16 a[32:47], a[132:135], v[192:195], a[32:47]
ds_read_b64_tr_b16 a[150:151], v59 offset:2560
v_exp_f32 v162, v162
v_cvt_pk_bf16_f32 v208, v160, v161
v_add_f32 v60, v60, v160
s_waitcnt lgkmcnt(4)
v_mfma_f32_32x32x16_bf16 a[64:79], a[136:139], v[192:195], a[64:79]
ds_read_b64_tr_b16 a[152:153], v58 offset:6144
v_xor_b32 v54, 0x8000, v54
v_exp_f32 v163, v163
v_add_f32 v60, v60, v161
v_cvt_pk_bf16_f32 v209, v162, v163
v_mfma_f32_32x32x16_bf16 a[80:95], a[136:139], v[200:203], a[80:95]
ds_read_b64_tr_b16 a[154:155], v58 offset:6656
v_exp_f32 v164, v164
v_add_f32 v60, v60, v162
v_add_f32 v60, v60, v163
v_add_f32 v60, v60, v164
v_mfma_f32_32x32x16_bf16 a[112:127], a[140:143], v[200:203], a[112:127]
ds_read_b64_tr_b16 a[156:157], v59 offset:6144
v_xor_b32 v55, 0x8000, v55
v_exp_f32 v165, v165
v_mfma_f32_32x32x16_bf16 a[96:111], a[140:143], v[192:195], a[96:111]
ds_read_b64_tr_b16 a[158:159], v59 offset:6656
v_exp_f32 v166, v166
v_cvt_pk_bf16_f32 v210, v164, v165
v_add_f32 v60, v60, v165
s_waitcnt lgkmcnt(4)
v_mfma_f32_32x32x16_bf16 a[0:15], a[144:147], v[196:199], a[0:15]
ds_read_b128 a[128:131], v48 offset:0
v_xor_b32 v56, 0x8000, v56
v_xor_b32 v48, 0x8000, v48
v_exp_f32 v167, v167
s_add_u32 m0, s87, 0x4000
v_mfma_f32_32x32x16_bf16 a[16:31], a[144:147], v[204:207], a[16:31]
buffer_load_dwordx4 v36, s[44:47], s84 offen lds
v_xor_b32 v58, 0x8000, v58
v_exp_f32 v176, v176
v_cvt_pk_bf16_f32 v211, v166, v167
v_mfma_f32_32x32x16_bf16 a[48:63], a[148:151], v[204:207], a[48:63]
ds_read_b128 a[132:135], v49 offset:0
v_xor_b32 v57, 0x8000, v57
v_xor_b32 v49, 0x8000, v49
v_exp_f32 v177, v177
v_add_f32 v60, v60, v166
v_mfma_f32_32x32x16_bf16 a[32:47], a[148:151], v[196:199], a[32:47]
buffer_load_dwordx4 v37, s[44:47], s84 offen offset:1024 lds
s_xor_b32 s87, s87, 0x8000
v_exp_f32 v178, v178
s_waitcnt lgkmcnt(2)
v_mfma_f32_32x32x16_bf16 a[64:79], a[152:155], v[196:199], a[64:79]
ds_read_b128 a[136:139], v50 offset:0
v_xor_b32 v59, 0x8000, v59
v_xor_b32 v50, 0x8000, v50
v_add_f32 v60, v60, v167
v_exp_f32 v179, v179
v_mfma_f32_32x32x16_bf16 a[80:95], a[152:155], v[204:207], a[80:95]
buffer_load_dwordx4 v38, s[44:47], s84 offen offset:2048 lds
s_xor_b32 s96, s96, 0x8000
v_cvt_pk_bf16_f32 v216, v176, v177
v_add_f32 v61, v61, v176
v_add_f32 v61, v61, v177
v_mfma_f32_32x32x16_bf16 a[112:127], a[156:159], v[204:207], a[112:127]
ds_read_b128 a[140:143], v51 offset:0
v_xor_b32 v51, 0x8000, v51
v_exp_f32 v180, v180
v_exp_f32 v181, v181
v_mfma_f32_32x32x16_bf16 a[96:111], a[156:159], v[196:199], a[96:111]
buffer_load_dwordx4 v39, s[44:47], s84 offen offset:3072 lds
s_add_u32 s84, s84, 0x2000
s_and_b32 s84, s84, 0x1ffff
v_cvt_pk_bf16_f32 v217, v178, v179
s_waitcnt lgkmcnt(2)
s_add_u32 s54, s54, -1
s_cbranch_scc1 .Lk1_att
s_waitcnt vmcnt(16)
s_barrier
v_add_u32 v4, 0x20000, v42
v_add_u32 v5, 0x20000, v43
v_add_u32 v6, 0x20000, v44
v_add_u32 v7, 0x20000, v45
v_lshlrev_b32 v21, 2, v1
buffer_load_dword v22, v21, s[12:15], 0 offen offset:1856 sc1
v_mfma_f32_32x32x16_bf16 v[128:143], a[128:131], v[64:67], v[224:239]
ds_read_b128 a[144:147], v52 offset:4096
v_add_f32 v61, v61, v178
v_exp_f32 v182, v182
v_add_f32 v61, v61, v179
s_add_u32 m0, s63, 0
v_mfma_f32_32x32x16_bf16 v[144:159], a[128:131], v[96:99], v[240:255]
s_waitcnt vmcnt(5)
buffer_load_dwordx4 v24, s[48:51], s73 offen lds
v_exp_f32 v183, v183
v_cvt_pk_bf16_f32 v218, v180, v181
v_add_f32 v61, v61, v180
v_mfma_f32_32x32x16_bf16 v[128:143], a[132:135], v[68:71], v[128:143]
ds_read_b128 a[148:151], v53 offset:4096
v_exp_f32 v168, v168
v_add_f32 v61, v61, v181
v_cvt_pk_bf16_f32 v219, v182, v183
v_mfma_f32_32x32x16_bf16 v[144:159], a[132:135], v[100:103], v[144:159]
buffer_load_dwordx4 v25, s[48:51], s73 offen offset:1024 lds
v_exp_f32 v169, v169
v_add_f32 v61, v61, v182
s_waitcnt lgkmcnt(2)
v_mfma_f32_32x32x16_bf16 v[128:143], a[136:139], v[72:75], v[128:143]
ds_read_b128 a[152:155], v54 offset:4096
v_exp_f32 v170, v170
v_add_f32 v61, v61, v183
v_cvt_pk_bf16_f32 v212, v168, v169
v_mfma_f32_32x32x16_bf16 v[144:159], a[136:139], v[104:107], v[144:159]
buffer_load_dwordx4 v26, s[48:51], s73 offen offset:2048 lds
v_exp_f32 v171, v171
v_add_f32 v60, v60, v168
v_add_f32 v60, v60, v169
v_mfma_f32_32x32x16_bf16 v[128:143], a[140:143], v[76:79], v[128:143]
ds_read_b128 a[156:159], v55 offset:4096
v_exp_f32 v172, v172
v_exp_f32 v173, v173
v_mfma_f32_32x32x16_bf16 v[144:159], a[140:143], v[108:111], v[144:159]
buffer_load_dwordx4 v27, s[48:51], s73 offen offset:3072 lds
v_cvt_pk_bf16_f32 v213, v170, v171
v_add_f32 v60, v60, v170
v_add_f32 v60, v60, v171
s_waitcnt lgkmcnt(2)
v_mfma_f32_32x32x16_bf16 v[128:143], a[144:147], v[80:83], v[128:143]
ds_read_b64_tr_b16 a[128:129], v56 offset:16384
v_exp_f32 v174, v174
v_exp_f32 v175, v175
v_cvt_pk_bf16_f32 v214, v172, v173
v_mfma_f32_32x32x16_bf16 v[144:159], a[144:147], v[112:115], v[144:159]
ds_read_b64_tr_b16 a[130:131], v56 offset:16896
v_add_f32 v60, v60, v172
v_exp_f32 v184, v184
v_add_f32 v60, v60, v173
v_cvt_pk_bf16_f32 v215, v174, v175
v_mfma_f32_32x32x16_bf16 v[128:143], a[148:151], v[84:87], v[128:143]
ds_read_b64_tr_b16 a[132:133], v57 offset:16384
v_exp_f32 v185, v185
v_add_f32 v60, v60, v174
v_add_f32 v60, v60, v175
v_cvt_pk_bf16_f32 v220, v184, v185
v_mfma_f32_32x32x16_bf16 v[144:159], a[148:151], v[116:119], v[144:159]
ds_read_b64_tr_b16 a[134:135], v57 offset:16896
v_exp_f32 v186, v186
v_add_f32 v61, v61, v184
v_add_f32 v61, v61, v185
s_waitcnt lgkmcnt(4)
v_mfma_f32_32x32x16_bf16 v[128:143], a[152:155], v[88:91], v[128:143]
ds_read_b64_tr_b16 a[136:137], v56 offset:20480
v_exp_f32 v187, v187
v_add_f32 v61, v61, v186
)ASM"
R"ASM(
v_cvt_pk_bf16_f32 v221, v186, v187
v_add_f32 v61, v61, v187
v_mfma_f32_32x32x16_bf16 v[144:159], a[152:155], v[120:123], v[144:159]
ds_read_b64_tr_b16 a[138:139], v56 offset:20992
v_exp_f32 v188, v188
v_mfma_f32_32x32x16_bf16 v[128:143], a[156:159], v[92:95], v[128:143]
ds_read_b64_tr_b16 a[140:141], v57 offset:20480
v_exp_f32 v189, v189
v_add_f32 v61, v61, v188
v_cvt_pk_bf16_f32 v222, v188, v189
v_add_f32 v61, v61, v189
v_mfma_f32_32x32x16_bf16 v[144:159], a[156:159], v[124:127], v[144:159]
ds_read_b64_tr_b16 a[142:143], v57 offset:20992
v_exp_f32 v190, v190
s_waitcnt lgkmcnt(4)
v_mfma_f32_32x32x16_bf16 a[0:15], a[128:131], v[208:211], a[0:15]
ds_read_b64_tr_b16 a[144:145], v56 offset:18432
v_exp_f32 v191, v191
v_add_f32 v61, v61, v190
v_cvt_pk_bf16_f32 v223, v190, v191
v_add_f32 v61, v61, v191
v_mfma_f32_32x32x16_bf16 a[16:31], a[128:131], v[216:219], a[16:31]
ds_read_b64_tr_b16 a[146:147], v56 offset:18944
v_mfma_f32_32x32x16_bf16 a[48:63], a[132:135], v[216:219], a[48:63]
ds_read_b64_tr_b16 a[148:149], v57 offset:18432
v_exp_f32 v128, v128
v_exp_f32 v129, v129
v_add_f32 v60, v60, v128
v_mfma_f32_32x32x16_bf16 a[32:47], a[132:135], v[208:211], a[32:47]
ds_read_b64_tr_b16 a[150:151], v57 offset:18944
v_exp_f32 v130, v130
v_cvt_pk_bf16_f32 v192, v128, v129
v_add_f32 v60, v60, v129
s_waitcnt lgkmcnt(4)
v_mfma_f32_32x32x16_bf16 a[64:79], a[136:139], v[208:211], a[64:79]
ds_read_b64_tr_b16 a[152:153], v56 offset:22528
v_exp_f32 v131, v131
v_add_f32 v60, v60, v130
v_cvt_pk_bf16_f32 v193, v130, v131
v_add_f32 v60, v60, v131
v_mfma_f32_32x32x16_bf16 a[80:95], a[136:139], v[216:219], a[80:95]
ds_read_b64_tr_b16 a[154:155], v56 offset:23040
v_exp_f32 v132, v132
v_mfma_f32_32x32x16_bf16 a[112:127], a[140:143], v[216:219], a[112:127]
ds_read_b64_tr_b16 a[156:157], v57 offset:22528
v_exp_f32 v133, v133
v_add_f32 v60, v60, v132
v_cvt_pk_bf16_f32 v194, v132, v133
v_add_f32 v60, v60, v133
v_mfma_f32_32x32x16_bf16 a[96:111], a[140:143], v[208:211], a[96:111]
ds_read_b64_tr_b16 a[158:159], v57 offset:23040
v_exp_f32 v134, v134
s_waitcnt lgkmcnt(4)
v_mfma_f32_32x32x16_bf16 a[0:15], a[144:147], v[212:215], a[0:15]
ds_read_b128 a[128:131], v52 offset:16384
v_exp_f32 v135, v135
v_add_f32 v60, v60, v134
v_cvt_pk_bf16_f32 v195, v134, v135
s_add_u32 m0, s63, 0x4000
v_mfma_f32_32x32x16_bf16 a[16:31], a[144:147], v[220:223], a[16:31]
buffer_load_dwordx4 v28, s[48:51], s73 offen lds
v_exp_f32 v144, v144
v_add_f32 v60, v60, v135
v_add_f32 v61, v61, v144
v_mfma_f32_32x32x16_bf16 a[48:63], a[148:151], v[220:223], a[48:63]
ds_read_b128 a[132:135], v53 offset:16384
v_exp_f32 v145, v145
v_mfma_f32_32x32x16_bf16 a[32:47], a[148:151], v[212:215], a[32:47]
buffer_load_dwordx4 v29, s[48:51], s73 offen offset:1024 lds
v_exp_f32 v146, v146
v_cvt_pk_bf16_f32 v200, v144, v145
s_waitcnt lgkmcnt(2)
v_mfma_f32_32x32x16_bf16 a[64:79], a[152:155], v[212:215], a[64:79]
ds_read_b128 a[136:139], v54 offset:16384
v_exp_f32 v147, v147
v_add_f32 v61, v61, v145
v_cvt_pk_bf16_f32 v201, v146, v147
v_mfma_f32_32x32x16_bf16 a[80:95], a[152:155], v[220:223], a[80:95]
buffer_load_dwordx4 v30, s[48:51], s73 offen offset:2048 lds
v_exp_f32 v148, v148
v_add_f32 v61, v61, v146
v_add_f32 v61, v61, v147
v_mfma_f32_32x32x16_bf16 a[112:127], a[156:159], v[220:223], a[112:127]
ds_read_b128 a[140:143], v55 offset:16384
v_exp_f32 v149, v149
v_add_f32 v61, v61, v148
v_cvt_pk_bf16_f32 v202, v148, v149
v_mfma_f32_32x32x16_bf16 a[96:111], a[156:159], v[212:215], a[96:111]
buffer_load_dwordx4 v31, s[48:51], s73 offen offset:3072 lds
v_exp_f32 v150, v150
v_add_f32 v61, v61, v149
s_waitcnt lgkmcnt(2)
s_waitcnt vmcnt(9)
s_barrier
v_mfma_f32_32x32x16_bf16 v[160:175], a[128:131], v[64:67], v[224:239]
ds_read_b128 a[144:147], v52 offset:20480
v_exp_f32 v151, v151
v_add_f32 v61, v61, v150
v_cvt_pk_bf16_f32 v203, v150, v151
s_add_u32 m0, s53, 0x4000
v_mfma_f32_32x32x16_bf16 v[176:191], a[128:131], v[96:99], v[240:255]
s_add_u32 s73, s73, 0x12000
s_cmp_eq_u32 s73, s79
s_cselect_b32 s73, s76, s73
buffer_load_dwordx4 v24, s[48:51], s73 offen lds
v_add_f32 v61, v61, v151
v_mfma_f32_32x32x16_bf16 v[160:175], a[132:135], v[68:71], v[160:175]
ds_read_b128 a[148:151], v53 offset:20480
v_exp_f32 v136, v136
v_exp_f32 v137, v137
v_mfma_f32_32x32x16_bf16 v[176:191], a[132:135], v[100:103], v[176:191]
buffer_load_dwordx4 v25, s[48:51], s73 offen offset:1024 lds
v_exp_f32 v138, v138
v_cvt_pk_bf16_f32 v196, v136, v137
s_waitcnt lgkmcnt(2)
v_mfma_f32_32x32x16_bf16 v[160:175], a[136:139], v[72:75], v[160:175]
ds_read_b128 a[152:155], v54 offset:20480
v_exp_f32 v139, v139
v_add_f32 v60, v60, v136
v_add_f32 v60, v60, v137
v_mfma_f32_32x32x16_bf16 v[176:191], a[136:139], v[104:107], v[176:191]
buffer_load_dwordx4 v26, s[48:51], s73 offen offset:2048 lds
v_exp_f32 v140, v140
v_cvt_pk_bf16_f32 v197, v138, v139
v_add_f32 v60, v60, v138
v_mfma_f32_32x32x16_bf16 v[160:175], a[140:143], v[76:79], v[160:175]
ds_read_b128 a[156:159], v55 offset:20480
v_exp_f32 v141, v141
v_add_f32 v60, v60, v139
v_cvt_pk_bf16_f32 v198, v140, v141
v_mfma_f32_32x32x16_bf16 v[176:191], a[140:143], v[108:111], v[176:191]
buffer_load_dwordx4 v27, s[48:51], s73 offen offset:3072 lds
v_exp_f32 v142, v142
v_add_f32 v60, v60, v140
s_waitcnt lgkmcnt(2)
v_mfma_f32_32x32x16_bf16 v[160:175], a[144:147], v[80:83], v[160:175]
ds_read_b64_tr_b16 a[128:129], v58 offset:0
v_exp_f32 v143, v143
v_add_f32 v60, v60, v141
v_cvt_pk_bf16_f32 v199, v142, v143
v_add_f32 v60, v60, v142
v_mfma_f32_32x32x16_bf16 v[176:191], a[144:147], v[112:115], v[176:191]
ds_read_b64_tr_b16 a[130:131], v58 offset:512
v_exp_f32 v152, v152
v_add_f32 v60, v60, v143
v_add_f32 v61, v61, v152
v_mfma_f32_32x32x16_bf16 v[160:175], a[148:151], v[84:87], v[160:175]
ds_read_b64_tr_b16 a[132:133], v59 offset:0
v_exp_f32 v153, v153
v_mfma_f32_32x32x16_bf16 v[176:191], a[148:151], v[116:119], v[176:191]
ds_read_b64_tr_b16 a[134:135], v59 offset:512
v_exp_f32 v154, v154
v_cvt_pk_bf16_f32 v204, v152, v153
v_add_f32 v61, v61, v153
s_waitcnt lgkmcnt(4)
v_mfma_f32_32x32x16_bf16 v[160:175], a[152:155], v[88:91], v[160:175]
ds_read_b64_tr_b16 a[136:137], v58 offset:4096
v_exp_f32 v155, v155
v_add_f32 v61, v61, v154
v_cvt_pk_bf16_f32 v205, v154, v155
v_add_f32 v61, v61, v155
v_mfma_f32_32x32x16_bf16 v[176:191], a[152:155], v[120:123], v[176:191]
ds_read_b64_tr_b16 a[138:139], v58 offset:4608
v_exp_f32 v156, v156
v_mfma_f32_32x32x16_bf16 v[160:175], a[156:159], v[92:95], v[160:175]
ds_read_b64_tr_b16 a[140:141], v59 offset:4096
v_exp_f32 v157, v157
v_add_f32 v61, v61, v156
v_cvt_pk_bf16_f32 v206, v156, v157
v_add_f32 v61, v61, v157
v_mfma_f32_32x32x16_bf16 v[176:191], a[156:159], v[124:127], v[176:191]
ds_read_b64_tr_b16 a[142:143], v59 offset:4608
v_exp_f32 v158, v158
s_waitcnt lgkmcnt(4)
v_mfma_f32_32x32x16_bf16 a[0:15], a[128:131], v[192:195], a[0:15]
ds_read_b64_tr_b16 a[144:145], v58 offset:2048
v_exp_f32 v159, v159
v_add_f32 v61, v61, v158
v_cvt_pk_bf16_f32 v207, v158, v159
v_add_f32 v61, v61, v159
v_mfma_f32_32x32x16_bf16 a[16:31], a[128:131], v[200:203], a[16:31]
ds_read_b64_tr_b16 a[146:147], v58 offset:2560
v_mfma_f32_32x32x16_bf16 a[48:63], a[132:135], v[200:203], a[48:63]
ds_read_b64_tr_b16 a[148:149], v59 offset:2048
v_exp_f32 v160, v160
v_exp_f32 v161, v161
v_add_f32 v60, v60, v160
v_mfma_f32_32x32x16_bf16 a[32:47], a[132:135], v[192:195], a[32:47]
ds_read_b64_tr_b16 a[150:151], v59 offset:2560
v_exp_f32 v162, v162
v_cvt_pk_bf16_f32 v208, v160, v161
v_add_f32 v60, v60, v161
s_waitcnt lgkmcnt(4)
v_mfma_f32_32x32x16_bf16 a[64:79], a[136:139], v[192:195], a[64:79]
ds_read_b64_tr_b16 a[152:153], v58 offset:6144
v_exp_f32 v163, v163
v_add_f32 v60, v60, v162
v_cvt_pk_bf16_f32 v209, v162, v163
v_add_f32 v60, v60, v163
v_mfma_f32_32x32x16_bf16 a[80:95], a[136:139], v[200:203], a[80:95]
)ASM"
R"ASM(
ds_read_b64_tr_b16 a[154:155], v58 offset:6656
v_exp_f32 v164, v164
v_mfma_f32_32x32x16_bf16 a[112:127], a[140:143], v[200:203], a[112:127]
ds_read_b64_tr_b16 a[156:157], v59 offset:6144
v_exp_f32 v165, v165
v_add_f32 v60, v60, v164
v_cvt_pk_bf16_f32 v210, v164, v165
v_add_f32 v60, v60, v165
v_mfma_f32_32x32x16_bf16 a[96:111], a[140:143], v[192:195], a[96:111]
ds_read_b64_tr_b16 a[158:159], v59 offset:6656
v_exp_f32 v166, v166
s_waitcnt lgkmcnt(4)
v_mfma_f32_32x32x16_bf16 a[0:15], a[144:147], v[196:199], a[0:15]
ds_read_b64_tr_b16 a[128:129], v58 offset:16384
v_exp_f32 v167, v167
v_add_f32 v60, v60, v166
v_cvt_pk_bf16_f32 v211, v166, v167
s_add_u32 m0, s61, 0
v_mfma_f32_32x32x16_bf16 a[16:31], a[144:147], v[204:207], a[16:31]
buffer_load_dwordx4 v40, s[48:51], s97 offen lds
ds_read_b64_tr_b16 a[130:131], v58 offset:16896
v_exp_f32 v176, v176
v_add_f32 v60, v60, v167
v_mfma_f32_32x32x16_bf16 a[48:63], a[148:151], v[204:207], a[48:63]
ds_read_b64_tr_b16 a[132:133], v59 offset:16384
v_exp_f32 v177, v177
v_add_f32 v61, v61, v176
v_cvt_pk_bf16_f32 v216, v176, v177
s_add_u32 m0, s61, 0x1000
v_mfma_f32_32x32x16_bf16 a[32:47], a[148:151], v[196:199], a[32:47]
s_waitcnt vmcnt(0)
buffer_load_dwordx4 v41, s[48:51], s97 offen lds
ds_read_b64_tr_b16 a[134:135], v59 offset:16896
v_exp_f32 v178, v178
s_waitcnt lgkmcnt(4)
v_mfma_f32_32x32x16_bf16 a[64:79], a[152:155], v[196:199], a[64:79]
ds_read_b64_tr_b16 a[136:137], v58 offset:20480
v_exp_f32 v179, v179
v_add_f32 v61, v61, v177
v_cvt_pk_bf16_f32 v217, v178, v179
v_add_f32 v61, v61, v178
v_mfma_f32_32x32x16_bf16 a[80:95], a[152:155], v[204:207], a[80:95]
ds_read_b64_tr_b16 a[138:139], v58 offset:20992
v_exp_f32 v180, v180
v_add_f32 v61, v61, v179
v_add_f32 v61, v61, v180
v_mfma_f32_32x32x16_bf16 a[112:127], a[156:159], v[204:207], a[112:127]
ds_read_b64_tr_b16 a[140:141], v59 offset:20480
v_exp_f32 v181, v181
v_mfma_f32_32x32x16_bf16 a[96:111], a[156:159], v[196:199], a[96:111]
ds_read_b64_tr_b16 a[142:143], v59 offset:20992
v_exp_f32 v182, v182
v_cvt_pk_bf16_f32 v218, v180, v181
v_add_f32 v61, v61, v181
s_waitcnt lgkmcnt(4)
s_barrier
v_cmp_ne_u32 vcc, s11, v22
s_nop 3
s_and_b32 vcc_hi, vcc_hi, 0xf
s_cmp_eq_u64 vcc, 0
s_cbranch_scc1 .Lk1_ft_d
s_mov_b32 s54, 0x20000
.Lk1_ft_p:
buffer_load_dword v22, v21, s[12:15], 0 offen offset:1856 sc1
s_waitcnt vmcnt(0)
v_xor_b32 v22, s11, v22
v_cmp_ne_u32 vcc, 0, v22
s_nop 3
s_and_b32 vcc_lo, vcc_lo, 0xffffffff
s_and_b32 vcc_hi, vcc_hi, 0xf
s_cmp_eq_u64 vcc, 0
s_cbranch_scc1 .Lk1_ft_d
s_sleep 1
s_add_u32 s54, s54, -1
s_cbranch_scc1 .Lk1_ft_p
.Lk1_ft_d:
v_exp_f32 v183, v183
s_nop 0
v_cvt_pk_bf16_f32 v219, v182, v183
v_mfma_f32_32x32x16_bf16 a[0:15], a[128:131], v[208:211], a[0:15]
ds_read_b64_tr_b16 a[144:145], v58 offset:18432
v_exp_f32 v168, v168
v_exp_f32 v169, v169
v_add_f32 v61, v61, v182
v_add_f32 v61, v61, v183
v_mfma_f32_32x32x16_bf16 a[16:31], a[128:131], v[216:219], a[16:31]
ds_read_b64_tr_b16 a[146:147], v58 offset:18944
v_exp_f32 v170, v170
v_exp_f32 v171, v171
v_cvt_pk_bf16_f32 v212, v168, v169
v_add_f32 v60, v60, v168
v_mfma_f32_32x32x16_bf16 a[48:63], a[132:135], v[216:219], a[48:63]
ds_read_b64_tr_b16 a[148:149], v59 offset:18432
v_exp_f32 v172, v172
v_add_f32 v60, v60, v169
v_exp_f32 v173, v173
v_cvt_pk_bf16_f32 v213, v170, v171
v_mfma_f32_32x32x16_bf16 a[32:47], a[132:135], v[208:211], a[32:47]
ds_read_b64_tr_b16 a[150:151], v59 offset:18944
v_add_f32 v60, v60, v170
v_exp_f32 v174, v174
v_add_f32 v60, v60, v171
v_cvt_pk_bf16_f32 v214, v172, v173
s_waitcnt lgkmcnt(4)
v_mfma_f32_32x32x16_bf16 a[64:79], a[136:139], v[208:211], a[64:79]
ds_read_b64_tr_b16 a[152:153], v58 offset:22528
v_exp_f32 v175, v175
v_add_f32 v60, v60, v172
v_exp_f32 v184, v184
v_add_f32 v60, v60, v173
v_mfma_f32_32x32x16_bf16 a[80:95], a[136:139], v[216:219], a[80:95]
ds_read_b64_tr_b16 a[154:155], v58 offset:23040
v_exp_f32 v185, v185
v_cvt_pk_bf16_f32 v215, v174, v175
v_add_f32 v60, v60, v174
v_exp_f32 v186, v186
v_mfma_f32_32x32x16_bf16 a[112:127], a[140:143], v[216:219], a[112:127]
ds_read_b64_tr_b16 a[156:157], v59 offset:22528
v_add_f32 v60, v60, v175
v_exp_f32 v187, v187
v_cvt_pk_bf16_f32 v220, v184, v185
v_add_f32 v61, v61, v184
v_add_f32 v61, v61, v185
v_mfma_f32_32x32x16_bf16 a[96:111], a[140:143], v[208:211], a[96:111]
ds_read_b64_tr_b16 a[158:159], v59 offset:23040
v_exp_f32 v188, v188
v_exp_f32 v189, v189
v_cvt_pk_bf16_f32 v221, v186, v187
v_cvt_pk_bf16_f32 v222, v188, v189
v_exp_f32 v190, v190
v_exp_f32 v191, v191
s_nop 0
v_cvt_pk_bf16_f32 v223, v190, v191
s_waitcnt lgkmcnt(4)
v_mfma_f32_32x32x16_bf16 a[0:15], a[144:147], v[212:215], a[0:15]
v_add_f32 v61, v61, v186
v_add_f32 v61, v61, v187
v_add_f32 v61, v61, v188
v_add_f32 v61, v61, v189
v_add_f32 v61, v61, v190
v_add_f32 v61, v61, v191
v_mfma_f32_32x32x16_bf16 a[16:31], a[144:147], v[220:223], a[16:31]
v_mfma_f32_32x32x16_bf16 a[48:63], a[148:151], v[220:223], a[48:63]
v_mfma_f32_32x32x16_bf16 a[32:47], a[148:151], v[212:215], a[32:47]
s_waitcnt lgkmcnt(0)
v_mfma_f32_32x32x16_bf16 a[64:79], a[152:155], v[212:215], a[64:79]
v_mfma_f32_32x32x16_bf16 a[80:95], a[152:155], v[220:223], a[80:95]
v_mfma_f32_32x32x16_bf16 a[112:127], a[156:159], v[220:223], a[112:127]
v_mfma_f32_32x32x16_bf16 a[96:111], a[156:159], v[212:215], a[96:111]
v_mov_b32 v130, v60
v_mov_b32 v131, v61
s_nop 0
v_permlane32_swap_b32 v60, v130
v_permlane32_swap_b32 v61, v131
v_add_f32 v60, v60, v130
v_add_f32 v61, v61, v131
v_add_f32 v14, v60, v61
v_rcp_f32 v128, v60
v_rcp_f32 v129, v61
s_mov_b32 s58, 0x5f800000
v_cmp_nge_f32 vcc, s58, v14
s_nop 3
s_cmp_lg_u64 vcc, 0
s_cselect_b32 s58, 1, 0
s_andn2_b32 s58, s58, s85
v_mov_b32 v14, s58
s_lshl_b32 s59, s52, 2
s_add_u32 s59, s59, 0xa000
v_mov_b32 v13, s59
ds_write_b32 v13, v14 offset:0
v_accvgpr_read_b32 v136, a0
v_accvgpr_read_b32 v137, a1
v_accvgpr_read_b32 v138, a2
v_accvgpr_read_b32 v139, a3
v_accvgpr_read_b32 v140, a4
v_accvgpr_read_b32 v141, a5
v_accvgpr_read_b32 v142, a6
v_accvgpr_read_b32 v143, a7
v_mul_f32 v136, v136, v128
v_mul_f32 v137, v137, v128
v_mul_f32 v138, v138, v128
v_mul_f32 v139, v139, v128
v_mul_f32 v140, v140, v128
v_mul_f32 v141, v141, v128
v_mul_f32 v142, v142, v128
v_mul_f32 v143, v143, v128
v_cvt_pk_bf16_f32 v144, v136, v137
v_cvt_pk_bf16_f32 v145, v138, v139
v_cvt_pk_bf16_f32 v146, v140, v141
v_cvt_pk_bf16_f32 v147, v142, v143
ds_write_b128 v12, v[144:147] offset:0
v_accvgpr_read_b32 v148, a8
v_accvgpr_read_b32 v149, a9
v_accvgpr_read_b32 v150, a10
v_accvgpr_read_b32 v151, a11
v_accvgpr_read_b32 v152, a12
v_accvgpr_read_b32 v153, a13
v_accvgpr_read_b32 v154, a14
v_accvgpr_read_b32 v155, a15
v_mul_f32 v148, v148, v128
)ASM"
R"ASM(
v_mul_f32 v149, v149, v128
v_mul_f32 v150, v150, v128
v_mul_f32 v151, v151, v128
v_mul_f32 v152, v152, v128
v_mul_f32 v153, v153, v128
v_mul_f32 v154, v154, v128
v_mul_f32 v155, v155, v128
v_cvt_pk_bf16_f32 v156, v148, v149
v_cvt_pk_bf16_f32 v157, v150, v151
v_cvt_pk_bf16_f32 v158, v152, v153
v_cvt_pk_bf16_f32 v159, v154, v155
ds_write_b128 v12, v[156:159] offset:512
v_accvgpr_read_b32 v160, a16
v_accvgpr_read_b32 v161, a17
v_accvgpr_read_b32 v162, a18
v_accvgpr_read_b32 v163, a19
v_accvgpr_read_b32 v164, a20
v_accvgpr_read_b32 v165, a21
v_accvgpr_read_b32 v166, a22
v_accvgpr_read_b32 v167, a23
v_mul_f32 v160, v160, v129
v_mul_f32 v161, v161, v129
v_mul_f32 v162, v162, v129
v_mul_f32 v163, v163, v129
v_mul_f32 v164, v164, v129
v_mul_f32 v165, v165, v129
v_mul_f32 v166, v166, v129
v_mul_f32 v167, v167, v129
v_cvt_pk_bf16_f32 v168, v160, v161
v_cvt_pk_bf16_f32 v169, v162, v163
v_cvt_pk_bf16_f32 v170, v164, v165
v_cvt_pk_bf16_f32 v171, v166, v167
ds_write_b128 v12, v[168:171] offset:2048
v_accvgpr_read_b32 v172, a24
v_accvgpr_read_b32 v173, a25
v_accvgpr_read_b32 v174, a26
v_accvgpr_read_b32 v175, a27
v_accvgpr_read_b32 v176, a28
v_accvgpr_read_b32 v177, a29
v_accvgpr_read_b32 v178, a30
v_accvgpr_read_b32 v179, a31
v_mul_f32 v172, v172, v129
v_mul_f32 v173, v173, v129
v_mul_f32 v174, v174, v129
v_mul_f32 v175, v175, v129
v_mul_f32 v176, v176, v129
v_mul_f32 v177, v177, v129
v_mul_f32 v178, v178, v129
v_mul_f32 v179, v179, v129
v_cvt_pk_bf16_f32 v180, v172, v173
v_cvt_pk_bf16_f32 v181, v174, v175
v_cvt_pk_bf16_f32 v182, v176, v177
v_cvt_pk_bf16_f32 v183, v178, v179
ds_write_b128 v12, v[180:183] offset:2560
v_accvgpr_read_b32 v184, a32
v_accvgpr_read_b32 v185, a33
v_accvgpr_read_b32 v186, a34
v_accvgpr_read_b32 v187, a35
v_accvgpr_read_b32 v188, a36
v_accvgpr_read_b32 v189, a37
v_accvgpr_read_b32 v190, a38
v_accvgpr_read_b32 v191, a39
v_mul_f32 v184, v184, v128
v_mul_f32 v185, v185, v128
v_mul_f32 v186, v186, v128
v_mul_f32 v187, v187, v128
v_mul_f32 v188, v188, v128
v_mul_f32 v189, v189, v128
v_mul_f32 v190, v190, v128
v_mul_f32 v191, v191, v128
v_cvt_pk_bf16_f32 v192, v184, v185
v_cvt_pk_bf16_f32 v193, v186, v187
v_cvt_pk_bf16_f32 v194, v188, v189
v_cvt_pk_bf16_f32 v195, v190, v191
ds_write_b128 v12, v[192:195] offset:4096
v_accvgpr_read_b32 v196, a40
v_accvgpr_read_b32 v197, a41
v_accvgpr_read_b32 v198, a42
v_accvgpr_read_b32 v199, a43
v_accvgpr_read_b32 v200, a44
v_accvgpr_read_b32 v201, a45
v_accvgpr_read_b32 v202, a46
v_accvgpr_read_b32 v203, a47
v_mul_f32 v196, v196, v128
v_mul_f32 v197, v197, v128
v_mul_f32 v198, v198, v128
v_mul_f32 v199, v199, v128
v_mul_f32 v200, v200, v128
v_mul_f32 v201, v201, v128
v_mul_f32 v202, v202, v128
v_mul_f32 v203, v203, v128
v_cvt_pk_bf16_f32 v204, v196, v197
v_cvt_pk_bf16_f32 v205, v198, v199
v_cvt_pk_bf16_f32 v206, v200, v201
v_cvt_pk_bf16_f32 v207, v202, v203
ds_write_b128 v12, v[204:207] offset:4608
v_accvgpr_read_b32 v208, a48
v_accvgpr_read_b32 v209, a49
v_accvgpr_read_b32 v210, a50
v_accvgpr_read_b32 v211, a51
v_accvgpr_read_b32 v212, a52
v_accvgpr_read_b32 v213, a53
v_accvgpr_read_b32 v214, a54
v_accvgpr_read_b32 v215, a55
v_mul_f32 v208, v208, v129
v_mul_f32 v209, v209, v129
v_mul_f32 v210, v210, v129
v_mul_f32 v211, v211, v129
v_mul_f32 v212, v212, v129
v_mul_f32 v213, v213, v129
v_mul_f32 v214, v214, v129
v_mul_f32 v215, v215, v129
v_cvt_pk_bf16_f32 v216, v208, v209
v_cvt_pk_bf16_f32 v217, v210, v211
v_cvt_pk_bf16_f32 v218, v212, v213
v_cvt_pk_bf16_f32 v219, v214, v215
ds_write_b128 v12, v[216:219] offset:6144
v_accvgpr_read_b32 v220, a56
v_accvgpr_read_b32 v221, a57
v_accvgpr_read_b32 v222, a58
v_accvgpr_read_b32 v223, a59
v_accvgpr_read_b32 v224, a60
v_accvgpr_read_b32 v225, a61
v_accvgpr_read_b32 v226, a62
v_accvgpr_read_b32 v227, a63
v_mul_f32 v220, v220, v129
v_mul_f32 v221, v221, v129
v_mul_f32 v222, v222, v129
v_mul_f32 v223, v223, v129
v_mul_f32 v224, v224, v129
v_mul_f32 v225, v225, v129
v_mul_f32 v226, v226, v129
v_mul_f32 v227, v227, v129
v_cvt_pk_bf16_f32 v228, v220, v221
v_cvt_pk_bf16_f32 v229, v222, v223
v_cvt_pk_bf16_f32 v230, v224, v225
v_cvt_pk_bf16_f32 v231, v226, v227
ds_write_b128 v12, v[228:231] offset:6656
v_accvgpr_read_b32 v136, a64
v_accvgpr_read_b32 v137, a65
v_accvgpr_read_b32 v138, a66
v_accvgpr_read_b32 v139, a67
v_accvgpr_read_b32 v140, a68
v_accvgpr_read_b32 v141, a69
v_accvgpr_read_b32 v142, a70
v_accvgpr_read_b32 v143, a71
v_mul_f32 v136, v136, v128
v_mul_f32 v137, v137, v128
v_mul_f32 v138, v138, v128
v_mul_f32 v139, v139, v128
v_mul_f32 v140, v140, v128
v_mul_f32 v141, v141, v128
v_mul_f32 v142, v142, v128
v_mul_f32 v143, v143, v128
v_cvt_pk_bf16_f32 v144, v136, v137
v_cvt_pk_bf16_f32 v145, v138, v139
v_cvt_pk_bf16_f32 v146, v140, v141
v_cvt_pk_bf16_f32 v147, v142, v143
ds_write_b128 v12, v[144:147] offset:8192
v_accvgpr_read_b32 v148, a72
v_accvgpr_read_b32 v149, a73
v_accvgpr_read_b32 v150, a74
v_accvgpr_read_b32 v151, a75
v_accvgpr_read_b32 v152, a76
v_accvgpr_read_b32 v153, a77
v_accvgpr_read_b32 v154, a78
v_accvgpr_read_b32 v155, a79
v_mul_f32 v148, v148, v128
v_mul_f32 v149, v149, v128
v_mul_f32 v150, v150, v128
v_mul_f32 v151, v151, v128
v_mul_f32 v152, v152, v128
v_mul_f32 v153, v153, v128
v_mul_f32 v154, v154, v128
v_mul_f32 v155, v155, v128
v_cvt_pk_bf16_f32 v156, v148, v149
v_cvt_pk_bf16_f32 v157, v150, v151
v_cvt_pk_bf16_f32 v158, v152, v153
v_cvt_pk_bf16_f32 v159, v154, v155
ds_write_b128 v12, v[156:159] offset:8704
v_accvgpr_read_b32 v160, a80
v_accvgpr_read_b32 v161, a81
v_accvgpr_read_b32 v162, a82
v_accvgpr_read_b32 v163, a83
v_accvgpr_read_b32 v164, a84
v_accvgpr_read_b32 v165, a85
v_accvgpr_read_b32 v166, a86
v_accvgpr_read_b32 v167, a87
v_mul_f32 v160, v160, v129
v_mul_f32 v161, v161, v129
v_mul_f32 v162, v162, v129
v_mul_f32 v163, v163, v129
v_mul_f32 v164, v164, v129
v_mul_f32 v165, v165, v129
v_mul_f32 v166, v166, v129
v_mul_f32 v167, v167, v129
v_cvt_pk_bf16_f32 v168, v160, v161
v_cvt_pk_bf16_f32 v169, v162, v163
v_cvt_pk_bf16_f32 v170, v164, v165
v_cvt_pk_bf16_f32 v171, v166, v167
)ASM"
R"ASM(
ds_write_b128 v12, v[168:171] offset:10240
v_accvgpr_read_b32 v172, a88
v_accvgpr_read_b32 v173, a89
v_accvgpr_read_b32 v174, a90
v_accvgpr_read_b32 v175, a91
v_accvgpr_read_b32 v176, a92
v_accvgpr_read_b32 v177, a93
v_accvgpr_read_b32 v178, a94
v_accvgpr_read_b32 v179, a95
v_mul_f32 v172, v172, v129
v_mul_f32 v173, v173, v129
v_mul_f32 v174, v174, v129
v_mul_f32 v175, v175, v129
v_mul_f32 v176, v176, v129
v_mul_f32 v177, v177, v129
v_mul_f32 v178, v178, v129
v_mul_f32 v179, v179, v129
v_cvt_pk_bf16_f32 v180, v172, v173
v_cvt_pk_bf16_f32 v181, v174, v175
v_cvt_pk_bf16_f32 v182, v176, v177
v_cvt_pk_bf16_f32 v183, v178, v179
ds_write_b128 v12, v[180:183] offset:10752
v_accvgpr_read_b32 v184, a96
v_accvgpr_read_b32 v185, a97
v_accvgpr_read_b32 v186, a98
v_accvgpr_read_b32 v187, a99
v_accvgpr_read_b32 v188, a100
v_accvgpr_read_b32 v189, a101
v_accvgpr_read_b32 v190, a102
v_accvgpr_read_b32 v191, a103
v_mul_f32 v184, v184, v128
v_mul_f32 v185, v185, v128
v_mul_f32 v186, v186, v128
v_mul_f32 v187, v187, v128
v_mul_f32 v188, v188, v128
v_mul_f32 v189, v189, v128
v_mul_f32 v190, v190, v128
v_mul_f32 v191, v191, v128
v_cvt_pk_bf16_f32 v192, v184, v185
v_cvt_pk_bf16_f32 v193, v186, v187
v_cvt_pk_bf16_f32 v194, v188, v189
v_cvt_pk_bf16_f32 v195, v190, v191
ds_write_b128 v12, v[192:195] offset:12288
v_accvgpr_read_b32 v196, a104
v_accvgpr_read_b32 v197, a105
v_accvgpr_read_b32 v198, a106
v_accvgpr_read_b32 v199, a107
v_accvgpr_read_b32 v200, a108
v_accvgpr_read_b32 v201, a109
v_accvgpr_read_b32 v202, a110
v_accvgpr_read_b32 v203, a111
v_mul_f32 v196, v196, v128
v_mul_f32 v197, v197, v128
v_mul_f32 v198, v198, v128
v_mul_f32 v199, v199, v128
v_mul_f32 v200, v200, v128
v_mul_f32 v201, v201, v128
v_mul_f32 v202, v202, v128
v_mul_f32 v203, v203, v128
v_cvt_pk_bf16_f32 v204, v196, v197
v_cvt_pk_bf16_f32 v205, v198, v199
v_cvt_pk_bf16_f32 v206, v200, v201
v_cvt_pk_bf16_f32 v207, v202, v203
ds_write_b128 v12, v[204:207] offset:12800
v_accvgpr_read_b32 v208, a112
v_accvgpr_read_b32 v209, a113
v_accvgpr_read_b32 v210, a114
v_accvgpr_read_b32 v211, a115
v_accvgpr_read_b32 v212, a116
v_accvgpr_read_b32 v213, a117
v_accvgpr_read_b32 v214, a118
v_accvgpr_read_b32 v215, a119
v_mul_f32 v208, v208, v129
v_mul_f32 v209, v209, v129
v_mul_f32 v210, v210, v129
v_mul_f32 v211, v211, v129
v_mul_f32 v212, v212, v129
v_mul_f32 v213, v213, v129
v_mul_f32 v214, v214, v129
v_mul_f32 v215, v215, v129
v_cvt_pk_bf16_f32 v216, v208, v209
v_cvt_pk_bf16_f32 v217, v210, v211
v_cvt_pk_bf16_f32 v218, v212, v213
v_cvt_pk_bf16_f32 v219, v214, v215
ds_write_b128 v12, v[216:219] offset:14336
v_accvgpr_read_b32 v220, a120
v_accvgpr_read_b32 v221, a121
v_accvgpr_read_b32 v222, a122
v_accvgpr_read_b32 v223, a123
v_accvgpr_read_b32 v224, a124
v_accvgpr_read_b32 v225, a125
v_accvgpr_read_b32 v226, a126
v_accvgpr_read_b32 v227, a127
v_mul_f32 v220, v220, v129
v_mul_f32 v221, v221, v129
v_mul_f32 v222, v222, v129
v_mul_f32 v223, v223, v129
v_mul_f32 v224, v224, v129
v_mul_f32 v225, v225, v129
v_mul_f32 v226, v226, v129
v_mul_f32 v227, v227, v129
v_cvt_pk_bf16_f32 v228, v220, v221
v_cvt_pk_bf16_f32 v229, v222, v223
v_cvt_pk_bf16_f32 v230, v224, v225
v_cvt_pk_bf16_f32 v231, v226, v227
ds_write_b128 v12, v[228:231] offset:14848
ds_read_b64_tr_b16 a[128:129], v4 offset:0
ds_read_b64_tr_b16 a[130:131], v4 offset:512
ds_read_b64_tr_b16 a[132:133], v5 offset:0
ds_read_b64_tr_b16 a[134:135], v5 offset:512
ds_read_b64_tr_b16 a[136:137], v6 offset:0
ds_read_b64_tr_b16 a[138:139], v6 offset:512
ds_read_b64_tr_b16 a[140:141], v7 offset:0
ds_read_b64_tr_b16 a[142:143], v7 offset:512
s_mov_b32 s55, s86
v_add_u32 v9, s55, v10
v_add_u32 v8, s55, v11
v_mov_b32 v13, 0xa000
s_waitcnt vmcnt(0) lgkmcnt(8)
s_barrier
ds_read_b128 v[128:131], v13 offset:0
s_waitcnt lgkmcnt(0)
v_or3_b32 v14, v128, v129, v130
v_or_b32 v14, v14, v131
s_nop 0
v_readfirstlane_b32 s58, v14
s_nop 3
s_cmp_lg_u32 s58, 0
s_cbranch_scc1 .Lk1_fallback
s_load_dwordx2 s[16:17], %0, 0x8
s_load_dwordx2 s[20:21], %0, 0x30
s_load_dwordx2 s[24:25], %0, 0x58
s_load_dwordx2 s[30:31], %0, 0x60
v_mov_b32 v72, 1
v_lshlrev_b32 v73, 16, v1
s_lshl_b32 s40, s60, 6
s_add_u32 s41, s40, 0x1000
s_add_u32 s42, s40, 0x2000
s_add_u32 s43, s40, 0x3000
s_lshr_b32 s58, s52, 1
s_add_u32 s58, s58, 8
s_lshl_b32 s44, s58, 12
ds_read_b128 a[160:163], v9 offset:0
ds_read_b128 a[164:167], v9 offset:1024
ds_read_b128 a[168:171], v8 offset:0
ds_read_b128 a[172:175], v8 offset:1024
s_waitcnt lgkmcnt(0)
v_mfma_f32_16x16x32_bf16 v[176:179], a[128:131], a[160:163], 0
s_add_u32 s97, s97, 0x12000
ds_read_b64_tr_b16 a[144:145], v46 offset:0
v_mfma_f32_16x16x32_bf16 v[180:183], a[128:131], a[164:167], 0
s_cmp_eq_u32 s97, s79
s_cselect_b32 s97, s76, s97
v_mfma_f32_16x16x32_bf16 v[184:187], a[128:131], a[168:171], 0
s_add_u32 m0, s61, 0x2000
ds_read_b64_tr_b16 a[146:147], v46 offset:512
v_mfma_f32_16x16x32_bf16 v[188:191], a[128:131], a[172:175], 0
buffer_load_dwordx4 v40, s[48:51], s97 offen lds
v_mfma_f32_16x16x32_bf16 v[204:207], a[132:135], a[172:175], 0
s_add_u32 m0, s61, 0x3000
ds_read_b64_tr_b16 a[148:149], v47 offset:0
v_mfma_f32_16x16x32_bf16 v[200:203], a[132:135], a[168:171], 0
s_waitcnt vmcnt(0)
buffer_load_dwordx4 v41, s[48:51], s97 offen lds
v_mfma_f32_16x16x32_bf16 v[196:199], a[132:135], a[164:167], 0
s_add_u32 m0, s53, 0xc000
ds_read_b64_tr_b16 a[150:151], v47 offset:512
v_mfma_f32_16x16x32_bf16 v[192:195], a[132:135], a[160:163], 0
ds_read_b128 a[176:179], v9 offset:4096
v_mfma_f32_16x16x32_bf16 v[208:211], a[136:139], a[160:163], 0
buffer_load_dwordx4 v28, s[48:51], s73 offen lds
v_mfma_f32_16x16x32_bf16 v[212:215], a[136:139], a[164:167], 0
ds_read_b64_tr_b16 a[152:153], v4 offset:16384
v_mfma_f32_16x16x32_bf16 v[216:219], a[136:139], a[168:171], 0
buffer_load_dwordx4 v29, s[48:51], s73 offen offset:1024 lds
v_mfma_f32_16x16x32_bf16 v[220:223], a[136:139], a[172:175], 0
ds_read_b64_tr_b16 a[154:155], v4 offset:16896
ds_read_b128 a[180:183], v9 offset:5120
v_mfma_f32_16x16x32_bf16 v[236:239], a[140:143], a[172:175], 0
buffer_load_dwordx4 v30, s[48:51], s73 offen offset:2048 lds
v_mfma_f32_16x16x32_bf16 v[232:235], a[140:143], a[168:171], 0
ds_read_b64_tr_b16 a[156:157], v5 offset:16384
ds_read_b128 a[184:187], v8 offset:4096
v_mfma_f32_16x16x32_bf16 v[228:231], a[140:143], a[164:167], 0
buffer_load_dwordx4 v31, s[48:51], s73 offen offset:3072 lds
v_mfma_f32_16x16x32_bf16 v[224:227], a[140:143], a[160:163], 0
ds_read_b64_tr_b16 a[158:159], v5 offset:16896
s_waitcnt lgkmcnt(7)
v_mfma_f32_16x16x32_bf16 v[240:243], a[144:147], a[160:163], 0
s_add_u32 s73, s73, 0x12000
ds_read_b64_tr_b16 a[128:129], v6 offset:16384
v_mfma_f32_16x16x32_bf16 v[244:247], a[144:147], a[164:167], 0
s_cmp_eq_u32 s73, s79
s_cselect_b32 s73, s76, s73
v_mfma_f32_16x16x32_bf16 v[252:255], a[148:151], a[164:167], 0
s_add_u32 m0, s63, 0
ds_read_b64_tr_b16 a[130:131], v6 offset:16896
v_mfma_f32_16x16x32_bf16 v[248:251], a[148:151], a[160:163], 0
ds_read_b64_tr_b16 a[132:133], v7 offset:16384
ds_read_b128 a[188:191], v8 offset:5120
)ASM"
R"ASM(
s_waitcnt lgkmcnt(0)
v_mfma_f32_16x16x32_bf16 v[176:179], a[152:155], a[176:179], v[176:179]
buffer_load_dwordx4 v24, s[48:51], s73 offen lds
v_mfma_f32_16x16x32_bf16 v[180:183], a[152:155], a[180:183], v[180:183]
ds_read_b64_tr_b16 a[134:135], v7 offset:16896
s_add_u32 s55, s55, 0x2000
v_mfma_f32_16x16x32_bf16 v[184:187], a[152:155], a[184:187], v[184:187]
ds_read_b64_tr_b16 a[136:137], v46 offset:4096
ds_read_b64_tr_b16 a[138:139], v46 offset:4608
v_mfma_f32_16x16x32_bf16 v[188:191], a[152:155], a[188:191], v[188:191]
s_cmp_eq_u32 s55, 0x20000
s_cselect_b32 s55, 0x10000, s55
v_mfma_f32_16x16x32_bf16 v[204:207], a[156:159], a[188:191], v[204:207]
buffer_load_dwordx4 v25, s[48:51], s73 offen offset:1024 lds
v_mfma_f32_16x16x32_bf16 v[200:203], a[156:159], a[184:187], v[200:203]
ds_read_b64_tr_b16 a[140:141], v47 offset:4096
v_add_u32 v9, s55, v10
v_mfma_f32_16x16x32_bf16 v[196:199], a[156:159], a[180:183], v[196:199]
ds_read_b64_tr_b16 a[142:143], v47 offset:4608
v_add_u32 v8, s55, v11
v_mfma_f32_16x16x32_bf16 v[192:195], a[156:159], a[176:179], v[192:195]
ds_read_b128 a[160:163], v9 offset:0
s_waitcnt lgkmcnt(5)
v_mfma_f32_16x16x32_bf16 v[208:211], a[128:131], a[176:179], v[208:211]
buffer_load_dwordx4 v26, s[48:51], s73 offen offset:2048 lds
v_mfma_f32_16x16x32_bf16 v[212:215], a[128:131], a[180:183], v[212:215]
ds_read_b64_tr_b16 a[144:145], v42 offset:16384
v_mfma_f32_16x16x32_bf16 v[216:219], a[128:131], a[184:187], v[216:219]
buffer_load_dwordx4 v27, s[48:51], s73 offen offset:3072 lds
v_mfma_f32_16x16x32_bf16 v[220:223], a[128:131], a[188:191], v[220:223]
s_add_u32 m0, s63, 0x4000
ds_read_b64_tr_b16 a[146:147], v42 offset:16896
v_mfma_f32_16x16x32_bf16 v[236:239], a[132:135], a[188:191], v[236:239]
ds_read_b64_tr_b16 a[148:149], v43 offset:16384
ds_read_b128 a[164:167], v9 offset:1024
v_mfma_f32_16x16x32_bf16 v[232:235], a[132:135], a[184:187], v[232:235]
ds_read_b64_tr_b16 a[150:151], v43 offset:16896
ds_read_b128 a[168:171], v8 offset:0
v_mfma_f32_16x16x32_bf16 v[228:231], a[132:135], a[180:183], v[228:231]
buffer_load_dwordx4 v28, s[48:51], s73 offen lds
v_mfma_f32_16x16x32_bf16 v[224:227], a[132:135], a[176:179], v[224:227]
ds_read_b128 a[172:175], v8 offset:1024
s_waitcnt lgkmcnt(8)
v_mfma_f32_16x16x32_bf16 v[240:243], a[136:139], a[176:179], v[240:243]
ds_read_b64_tr_b16 a[152:153], v44 offset:16384
ds_read_b64_tr_b16 a[154:155], v44 offset:16896
v_mfma_f32_16x16x32_bf16 v[244:247], a[136:139], a[180:183], v[244:247]
v_mfma_f32_16x16x32_bf16 v[252:255], a[140:143], a[180:183], v[252:255]
buffer_load_dwordx4 v29, s[48:51], s73 offen offset:1024 lds
v_mfma_f32_16x16x32_bf16 v[248:251], a[140:143], a[176:179], v[248:251]
ds_read_b64_tr_b16 a[156:157], v45 offset:16384
s_waitcnt lgkmcnt(3)
s_waitcnt vmcnt(10)
s_barrier
v_mfma_f32_16x16x32_bf16 v[176:179], a[144:147], a[160:163], v[176:179]
s_add_u32 s97, s97, 0x12000
ds_read_b64_tr_b16 a[158:159], v45 offset:16896
v_mfma_f32_16x16x32_bf16 v[180:183], a[144:147], a[164:167], v[180:183]
s_cmp_eq_u32 s97, s79
s_cselect_b32 s97, s76, s97
v_mfma_f32_16x16x32_bf16 v[184:187], a[144:147], a[168:171], v[184:187]
s_add_u32 m0, s61, 0
ds_read_b64_tr_b16 a[128:129], v46 offset:8192
v_mfma_f32_16x16x32_bf16 v[188:191], a[144:147], a[172:175], v[188:191]
buffer_load_dwordx4 v40, s[48:51], s97 offen lds
v_mfma_f32_16x16x32_bf16 v[204:207], a[148:151], a[172:175], v[204:207]
s_add_u32 m0, s61, 0x1000
ds_read_b64_tr_b16 a[130:131], v46 offset:8704
v_mfma_f32_16x16x32_bf16 v[200:203], a[148:151], a[168:171], v[200:203]
ds_read_b64_tr_b16 a[132:133], v47 offset:8192
ds_read_b128 a[176:179], v9 offset:4096
v_mfma_f32_16x16x32_bf16 v[196:199], a[148:151], a[164:167], v[196:199]
ds_read_b64_tr_b16 a[134:135], v47 offset:8704
ds_read_b128 a[180:183], v9 offset:5120
v_mfma_f32_16x16x32_bf16 v[192:195], a[148:151], a[160:163], v[192:195]
ds_read_b128 a[184:187], v8 offset:4096
s_waitcnt lgkmcnt(7)
v_mfma_f32_16x16x32_bf16 v[208:211], a[152:155], a[160:163], v[208:211]
buffer_load_dwordx4 v41, s[48:51], s97 offen lds
v_mfma_f32_16x16x32_bf16 v[212:215], a[152:155], a[164:167], v[212:215]
s_add_u32 m0, s63, 0x4000
s_waitcnt vmcnt(8)
ds_read_b64_tr_b16 a[136:137], v42 offset:49152
v_mfma_f32_16x16x32_bf16 v[216:219], a[152:155], a[168:171], v[216:219]
buffer_load_dwordx4 v30, s[48:51], s73 offen offset:2048 lds
v_mfma_f32_16x16x32_bf16 v[220:223], a[152:155], a[172:175], v[220:223]
ds_read_b64_tr_b16 a[138:139], v42 offset:49664
ds_read_b128 a[188:191], v8 offset:5120
v_mfma_f32_16x16x32_bf16 v[236:239], a[156:159], a[172:175], v[236:239]
buffer_load_dwordx4 v31, s[48:51], s73 offen offset:3072 lds
v_mfma_f32_16x16x32_bf16 v[232:235], a[156:159], a[168:171], v[232:235]
s_add_u32 s73, s73, 0x12000
ds_read_b64_tr_b16 a[140:141], v43 offset:49152
v_mfma_f32_16x16x32_bf16 v[228:231], a[156:159], a[164:167], v[228:231]
s_cmp_eq_u32 s73, s79
s_cselect_b32 s73, s76, s73
v_mfma_f32_16x16x32_bf16 v[224:227], a[156:159], a[160:163], v[224:227]
ds_read_b64_tr_b16 a[142:143], v43 offset:49664
s_waitcnt lgkmcnt(7)
v_mfma_f32_16x16x32_bf16 v[240:243], a[128:131], a[160:163], v[240:243]
s_add_u32 m0, s53, 0x4000
ds_read_b64_tr_b16 a[144:145], v44 offset:49152
v_mfma_f32_16x16x32_bf16 v[244:247], a[128:131], a[164:167], v[244:247]
buffer_load_dwordx4 v24, s[48:51], s73 offen lds
v_mfma_f32_16x16x32_bf16 v[252:255], a[132:135], a[164:167], v[252:255]
ds_read_b64_tr_b16 a[146:147], v44 offset:49664
ds_read_b64_tr_b16 a[148:149], v45 offset:49152
v_mfma_f32_16x16x32_bf16 v[248:251], a[132:135], a[160:163], v[248:251]
ds_read_b64_tr_b16 a[150:151], v45 offset:49664
s_waitcnt lgkmcnt(4)
v_mfma_f32_16x16x32_bf16 v[176:179], a[136:139], a[176:179], v[176:179]
ds_read_b64_tr_b16 a[152:153], v46 offset:12288
s_add_u32 s55, s55, 0x2000
v_mfma_f32_16x16x32_bf16 v[180:183], a[136:139], a[180:183], v[180:183]
buffer_load_dwordx4 v25, s[48:51], s73 offen offset:1024 lds
v_mfma_f32_16x16x32_bf16 v[184:187], a[136:139], a[184:187], v[184:187]
ds_read_b64_tr_b16 a[154:155], v46 offset:12800
v_mfma_f32_16x16x32_bf16 v[188:191], a[136:139], a[188:191], v[188:191]
s_cmp_eq_u32 s55, 0x20000
s_cselect_b32 s55, 0x10000, s55
v_mfma_f32_16x16x32_bf16 v[204:207], a[140:143], a[188:191], v[204:207]
ds_read_b64_tr_b16 a[156:157], v47 offset:12288
v_add_u32 v9, s55, v10
v_mfma_f32_16x16x32_bf16 v[200:203], a[140:143], a[184:187], v[200:203]
buffer_load_dwordx4 v26, s[48:51], s73 offen offset:2048 lds
v_mfma_f32_16x16x32_bf16 v[196:199], a[140:143], a[180:183], v[196:199]
ds_read_b64_tr_b16 a[158:159], v47 offset:12800
v_add_u32 v8, s55, v11
v_mfma_f32_16x16x32_bf16 v[192:195], a[140:143], a[176:179], v[192:195]
ds_read_b128 a[160:163], v9 offset:0
s_waitcnt lgkmcnt(5)
v_mfma_f32_16x16x32_bf16 v[208:211], a[144:147], a[176:179], v[208:211]
s_waitcnt vmcnt(9)
ds_read_b64_tr_b16 a[128:129], v4 offset:0
v_mfma_f32_16x16x32_bf16 v[212:215], a[144:147], a[180:183], v[212:215]
buffer_load_dwordx4 v27, s[48:51], s73 offen offset:3072 lds
v_mfma_f32_16x16x32_bf16 v[216:219], a[144:147], a[184:187], v[216:219]
s_add_u32 m0, s53, 0xc000
ds_read_b64_tr_b16 a[130:131], v4 offset:512
v_mfma_f32_16x16x32_bf16 v[220:223], a[144:147], a[188:191], v[220:223]
ds_read_b128 a[164:167], v9 offset:1024
ds_read_b128 a[168:171], v8 offset:0
v_mfma_f32_16x16x32_bf16 v[236:239], a[148:151], a[188:191], v[236:239]
ds_read_b64_tr_b16 a[132:133], v5 offset:0
ds_read_b128 a[172:175], v8 offset:1024
v_mfma_f32_16x16x32_bf16 v[232:235], a[148:151], a[184:187], v[232:235]
buffer_load_dwordx4 v28, s[48:51], s73 offen lds
v_mfma_f32_16x16x32_bf16 v[228:231], a[148:151], a[180:183], v[228:231]
ds_read_b64_tr_b16 a[134:135], v5 offset:512
v_mfma_f32_16x16x32_bf16 v[224:227], a[148:151], a[176:179], v[224:227]
s_waitcnt lgkmcnt(8)
v_mfma_f32_16x16x32_bf16 v[240:243], a[152:155], a[176:179], v[240:243]
ds_read_b64_tr_b16 a[136:137], v6 offset:0
ds_read_b64_tr_b16 a[138:139], v6 offset:512
v_mfma_f32_16x16x32_bf16 v[244:247], a[152:155], a[180:183], v[244:247]
buffer_load_dwordx4 v29, s[48:51], s73 offen offset:1024 lds
v_mfma_f32_16x16x32_bf16 v[252:255], a[156:159], a[180:183], v[252:255]
ds_read_b64_tr_b16 a[140:141], v7 offset:0
ds_read_b64_tr_b16 a[142:143], v7 offset:512
v_mfma_f32_16x16x32_bf16 v[248:251], a[156:159], a[176:179], v[248:251]
s_waitcnt lgkmcnt(4)
s_mov_b32 s54, 1
.Lk1_O1:
s_waitcnt vmcnt(8)
s_barrier
v_mfma_f32_16x16x32_bf16 v[176:179], a[128:131], a[160:163], v[176:179]
s_add_u32 s97, s97, 0x12000
ds_read_b64_tr_b16 a[144:145], v46 offset:0
v_mfma_f32_16x16x32_bf16 v[180:183], a[128:131], a[164:167], v[180:183]
s_cmp_eq_u32 s97, s79
s_cselect_b32 s97, s76, s97
v_mfma_f32_16x16x32_bf16 v[184:187], a[128:131], a[168:171], v[184:187]
s_add_u32 m0, s61, 0x2000
ds_read_b64_tr_b16 a[146:147], v46 offset:512
v_mfma_f32_16x16x32_bf16 v[188:191], a[128:131], a[172:175], v[188:191]
buffer_load_dwordx4 v40, s[48:51], s97 offen lds
v_mfma_f32_16x16x32_bf16 v[204:207], a[132:135], a[172:175], v[204:207]
s_add_u32 m0, s61, 0x3000
ds_read_b64_tr_b16 a[148:149], v47 offset:0
v_mfma_f32_16x16x32_bf16 v[200:203], a[132:135], a[168:171], v[200:203]
ds_read_b64_tr_b16 a[150:151], v47 offset:512
ds_read_b128 a[176:179], v9 offset:4096
v_mfma_f32_16x16x32_bf16 v[196:199], a[132:135], a[164:167], v[196:199]
ds_read_b128 a[180:183], v9 offset:5120
ds_read_b128 a[184:187], v8 offset:4096
v_mfma_f32_16x16x32_bf16 v[192:195], a[132:135], a[160:163], v[192:195]
ds_read_b128 a[188:191], v8 offset:5120
s_waitcnt lgkmcnt(8)
v_mfma_f32_16x16x32_bf16 v[208:211], a[136:139], a[160:163], v[208:211]
buffer_load_dwordx4 v41, s[48:51], s97 offen lds
v_mfma_f32_16x16x32_bf16 v[212:215], a[136:139], a[164:167], v[212:215]
s_add_u32 m0, s53, 0xc000
s_waitcnt vmcnt(8)
ds_read_b64_tr_b16 a[152:153], v4 offset:16384
v_mfma_f32_16x16x32_bf16 v[216:219], a[136:139], a[168:171], v[216:219]
buffer_load_dwordx4 v30, s[48:51], s73 offen offset:2048 lds
v_mfma_f32_16x16x32_bf16 v[220:223], a[136:139], a[172:175], v[220:223]
ds_read_b64_tr_b16 a[154:155], v4 offset:16896
v_mfma_f32_16x16x32_bf16 v[236:239], a[140:143], a[172:175], v[236:239]
buffer_load_dwordx4 v31, s[48:51], s73 offen offset:3072 lds
)ASM"
R"ASM(
v_mfma_f32_16x16x32_bf16 v[232:235], a[140:143], a[168:171], v[232:235]
s_add_u32 s73, s73, 0x12000
ds_read_b64_tr_b16 a[156:157], v5 offset:16384
v_mfma_f32_16x16x32_bf16 v[228:231], a[140:143], a[164:167], v[228:231]
s_cmp_eq_u32 s73, s79
s_cselect_b32 s73, s76, s73
v_mfma_f32_16x16x32_bf16 v[224:227], a[140:143], a[160:163], v[224:227]
ds_read_b64_tr_b16 a[158:159], v5 offset:16896
s_waitcnt lgkmcnt(8)
v_mfma_f32_16x16x32_bf16 v[240:243], a[144:147], a[160:163], v[240:243]
s_add_u32 m0, s63, 0
ds_read_b64_tr_b16 a[128:129], v6 offset:16384
v_mfma_f32_16x16x32_bf16 v[244:247], a[144:147], a[164:167], v[244:247]
buffer_load_dwordx4 v24, s[48:51], s73 offen lds
v_mfma_f32_16x16x32_bf16 v[252:255], a[148:151], a[164:167], v[252:255]
ds_read_b64_tr_b16 a[130:131], v6 offset:16896
ds_read_b64_tr_b16 a[132:133], v7 offset:16384
v_mfma_f32_16x16x32_bf16 v[248:251], a[148:151], a[160:163], v[248:251]
ds_read_b64_tr_b16 a[134:135], v7 offset:16896
s_waitcnt lgkmcnt(4)
v_mfma_f32_16x16x32_bf16 v[176:179], a[152:155], a[176:179], v[176:179]
ds_read_b64_tr_b16 a[136:137], v46 offset:4096
s_add_u32 s55, s55, 0x2000
v_mfma_f32_16x16x32_bf16 v[180:183], a[152:155], a[180:183], v[180:183]
buffer_load_dwordx4 v25, s[48:51], s73 offen offset:1024 lds
v_mfma_f32_16x16x32_bf16 v[184:187], a[152:155], a[184:187], v[184:187]
ds_read_b64_tr_b16 a[138:139], v46 offset:4608
v_mfma_f32_16x16x32_bf16 v[188:191], a[152:155], a[188:191], v[188:191]
s_cmp_eq_u32 s55, 0x20000
s_cselect_b32 s55, 0x10000, s55
v_mfma_f32_16x16x32_bf16 v[204:207], a[156:159], a[188:191], v[204:207]
ds_read_b64_tr_b16 a[140:141], v47 offset:4096
v_add_u32 v9, s55, v10
v_mfma_f32_16x16x32_bf16 v[200:203], a[156:159], a[184:187], v[200:203]
buffer_load_dwordx4 v26, s[48:51], s73 offen offset:2048 lds
v_mfma_f32_16x16x32_bf16 v[196:199], a[156:159], a[180:183], v[196:199]
ds_read_b64_tr_b16 a[142:143], v47 offset:4608
v_add_u32 v8, s55, v11
v_mfma_f32_16x16x32_bf16 v[192:195], a[156:159], a[176:179], v[192:195]
ds_read_b128 a[160:163], v9 offset:0
s_waitcnt lgkmcnt(5)
v_mfma_f32_16x16x32_bf16 v[208:211], a[128:131], a[176:179], v[208:211]
s_waitcnt vmcnt(9)
ds_read_b64_tr_b16 a[144:145], v42 offset:16384
v_mfma_f32_16x16x32_bf16 v[212:215], a[128:131], a[180:183], v[212:215]
buffer_load_dwordx4 v27, s[48:51], s73 offen offset:3072 lds
v_mfma_f32_16x16x32_bf16 v[216:219], a[128:131], a[184:187], v[216:219]
s_add_u32 m0, s63, 0x4000
ds_read_b64_tr_b16 a[146:147], v42 offset:16896
v_mfma_f32_16x16x32_bf16 v[220:223], a[128:131], a[188:191], v[220:223]
ds_read_b128 a[164:167], v9 offset:1024
ds_read_b128 a[168:171], v8 offset:0
v_mfma_f32_16x16x32_bf16 v[236:239], a[132:135], a[188:191], v[236:239]
ds_read_b64_tr_b16 a[148:149], v43 offset:16384
ds_read_b128 a[172:175], v8 offset:1024
v_mfma_f32_16x16x32_bf16 v[232:235], a[132:135], a[184:187], v[232:235]
buffer_load_dwordx4 v28, s[48:51], s73 offen lds
v_mfma_f32_16x16x32_bf16 v[228:231], a[132:135], a[180:183], v[228:231]
ds_read_b64_tr_b16 a[150:151], v43 offset:16896
v_mfma_f32_16x16x32_bf16 v[224:227], a[132:135], a[176:179], v[224:227]
s_waitcnt lgkmcnt(8)
v_mfma_f32_16x16x32_bf16 v[240:243], a[136:139], a[176:179], v[240:243]
ds_read_b64_tr_b16 a[152:153], v44 offset:16384
ds_read_b64_tr_b16 a[154:155], v44 offset:16896
v_mfma_f32_16x16x32_bf16 v[244:247], a[136:139], a[180:183], v[244:247]
buffer_load_dwordx4 v29, s[48:51], s73 offen offset:1024 lds
v_mfma_f32_16x16x32_bf16 v[252:255], a[140:143], a[180:183], v[252:255]
ds_read_b64_tr_b16 a[156:157], v45 offset:16384
ds_read_b64_tr_b16 a[158:159], v45 offset:16896
v_mfma_f32_16x16x32_bf16 v[248:251], a[140:143], a[176:179], v[248:251]
s_waitcnt lgkmcnt(4)
s_waitcnt vmcnt(8)
s_barrier
v_mfma_f32_16x16x32_bf16 v[176:179], a[144:147], a[160:163], v[176:179]
s_add_u32 s97, s97, 0x12000
ds_read_b64_tr_b16 a[128:129], v46 offset:8192
v_mfma_f32_16x16x32_bf16 v[180:183], a[144:147], a[164:167], v[180:183]
s_cmp_eq_u32 s97, s79
s_cselect_b32 s97, s76, s97
v_mfma_f32_16x16x32_bf16 v[184:187], a[144:147], a[168:171], v[184:187]
s_add_u32 m0, s61, 0
ds_read_b64_tr_b16 a[130:131], v46 offset:8704
v_mfma_f32_16x16x32_bf16 v[188:191], a[144:147], a[172:175], v[188:191]
buffer_load_dwordx4 v40, s[48:51], s97 offen lds
v_mfma_f32_16x16x32_bf16 v[204:207], a[148:151], a[172:175], v[204:207]
s_add_u32 m0, s61, 0x1000
ds_read_b64_tr_b16 a[132:133], v47 offset:8192
v_mfma_f32_16x16x32_bf16 v[200:203], a[148:151], a[168:171], v[200:203]
ds_read_b64_tr_b16 a[134:135], v47 offset:8704
ds_read_b128 a[176:179], v9 offset:4096
v_mfma_f32_16x16x32_bf16 v[196:199], a[148:151], a[164:167], v[196:199]
ds_read_b128 a[180:183], v9 offset:5120
ds_read_b128 a[184:187], v8 offset:4096
v_mfma_f32_16x16x32_bf16 v[192:195], a[148:151], a[160:163], v[192:195]
ds_read_b128 a[188:191], v8 offset:5120
s_waitcnt lgkmcnt(8)
v_mfma_f32_16x16x32_bf16 v[208:211], a[152:155], a[160:163], v[208:211]
buffer_load_dwordx4 v41, s[48:51], s97 offen lds
v_mfma_f32_16x16x32_bf16 v[212:215], a[152:155], a[164:167], v[212:215]
s_add_u32 m0, s63, 0x4000
s_waitcnt vmcnt(8)
ds_read_b64_tr_b16 a[136:137], v42 offset:49152
v_mfma_f32_16x16x32_bf16 v[216:219], a[152:155], a[168:171], v[216:219]
buffer_load_dwordx4 v30, s[48:51], s73 offen offset:2048 lds
v_mfma_f32_16x16x32_bf16 v[220:223], a[152:155], a[172:175], v[220:223]
ds_read_b64_tr_b16 a[138:139], v42 offset:49664
v_mfma_f32_16x16x32_bf16 v[236:239], a[156:159], a[172:175], v[236:239]
buffer_load_dwordx4 v31, s[48:51], s73 offen offset:3072 lds
v_mfma_f32_16x16x32_bf16 v[232:235], a[156:159], a[168:171], v[232:235]
s_add_u32 s73, s73, 0x12000
ds_read_b64_tr_b16 a[140:141], v43 offset:49152
v_mfma_f32_16x16x32_bf16 v[228:231], a[156:159], a[164:167], v[228:231]
s_cmp_eq_u32 s73, s79
s_cselect_b32 s73, s76, s73
v_mfma_f32_16x16x32_bf16 v[224:227], a[156:159], a[160:163], v[224:227]
ds_read_b64_tr_b16 a[142:143], v43 offset:49664
s_waitcnt lgkmcnt(8)
v_mfma_f32_16x16x32_bf16 v[240:243], a[128:131], a[160:163], v[240:243]
s_add_u32 m0, s53, 0x4000
ds_read_b64_tr_b16 a[144:145], v44 offset:49152
v_mfma_f32_16x16x32_bf16 v[244:247], a[128:131], a[164:167], v[244:247]
buffer_load_dwordx4 v24, s[48:51], s73 offen lds
v_mfma_f32_16x16x32_bf16 v[252:255], a[132:135], a[164:167], v[252:255]
ds_read_b64_tr_b16 a[146:147], v44 offset:49664
ds_read_b64_tr_b16 a[148:149], v45 offset:49152
v_mfma_f32_16x16x32_bf16 v[248:251], a[132:135], a[160:163], v[248:251]
ds_read_b64_tr_b16 a[150:151], v45 offset:49664
s_waitcnt lgkmcnt(4)
v_mfma_f32_16x16x32_bf16 v[176:179], a[136:139], a[176:179], v[176:179]
ds_read_b64_tr_b16 a[152:153], v46 offset:12288
s_add_u32 s55, s55, 0x2000
v_mfma_f32_16x16x32_bf16 v[180:183], a[136:139], a[180:183], v[180:183]
buffer_load_dwordx4 v25, s[48:51], s73 offen offset:1024 lds
v_mfma_f32_16x16x32_bf16 v[184:187], a[136:139], a[184:187], v[184:187]
ds_read_b64_tr_b16 a[154:155], v46 offset:12800
v_mfma_f32_16x16x32_bf16 v[188:191], a[136:139], a[188:191], v[188:191]
s_cmp_eq_u32 s55, 0x20000
s_cselect_b32 s55, 0x10000, s55
v_mfma_f32_16x16x32_bf16 v[204:207], a[140:143], a[188:191], v[204:207]
ds_read_b64_tr_b16 a[156:157], v47 offset:12288
v_add_u32 v9, s55, v10
v_mfma_f32_16x16x32_bf16 v[200:203], a[140:143], a[184:187], v[200:203]
buffer_load_dwordx4 v26, s[48:51], s73 offen offset:2048 lds
v_mfma_f32_16x16x32_bf16 v[196:199], a[140:143], a[180:183], v[196:199]
ds_read_b64_tr_b16 a[158:159], v47 offset:12800
v_add_u32 v8, s55, v11
v_mfma_f32_16x16x32_bf16 v[192:195], a[140:143], a[176:179], v[192:195]
ds_read_b128 a[160:163], v9 offset:0
s_waitcnt lgkmcnt(5)
v_mfma_f32_16x16x32_bf16 v[208:211], a[144:147], a[176:179], v[208:211]
s_waitcnt vmcnt(9)
ds_read_b64_tr_b16 a[128:129], v4 offset:0
v_mfma_f32_16x16x32_bf16 v[212:215], a[144:147], a[180:183], v[212:215]
buffer_load_dwordx4 v27, s[48:51], s73 offen offset:3072 lds
v_mfma_f32_16x16x32_bf16 v[216:219], a[144:147], a[184:187], v[216:219]
s_add_u32 m0, s53, 0xc000
ds_read_b64_tr_b16 a[130:131], v4 offset:512
v_mfma_f32_16x16x32_bf16 v[220:223], a[144:147], a[188:191], v[220:223]
ds_read_b128 a[164:167], v9 offset:1024
ds_read_b128 a[168:171], v8 offset:0
v_mfma_f32_16x16x32_bf16 v[236:239], a[148:151], a[188:191], v[236:239]
ds_read_b64_tr_b16 a[132:133], v5 offset:0
ds_read_b128 a[172:175], v8 offset:1024
v_mfma_f32_16x16x32_bf16 v[232:235], a[148:151], a[184:187], v[232:235]
buffer_load_dwordx4 v28, s[48:51], s73 offen lds
v_mfma_f32_16x16x32_bf16 v[228:231], a[148:151], a[180:183], v[228:231]
ds_read_b64_tr_b16 a[134:135], v5 offset:512
v_mfma_f32_16x16x32_bf16 v[224:227], a[148:151], a[176:179], v[224:227]
s_waitcnt lgkmcnt(8)
v_mfma_f32_16x16x32_bf16 v[240:243], a[152:155], a[176:179], v[240:243]
ds_read_b64_tr_b16 a[136:137], v6 offset:0
ds_read_b64_tr_b16 a[138:139], v6 offset:512
v_mfma_f32_16x16x32_bf16 v[244:247], a[152:155], a[180:183], v[244:247]
buffer_load_dwordx4 v29, s[48:51], s73 offen offset:1024 lds
v_mfma_f32_16x16x32_bf16 v[252:255], a[156:159], a[180:183], v[252:255]
ds_read_b64_tr_b16 a[140:141], v7 offset:0
ds_read_b64_tr_b16 a[142:143], v7 offset:512
v_mfma_f32_16x16x32_bf16 v[248:251], a[156:159], a[176:179], v[248:251]
s_waitcnt lgkmcnt(4)
s_add_u32 s54, s54, -1
s_cbranch_scc1 .Lk1_O1
s_waitcnt vmcnt(8)
s_barrier
v_mfma_f32_16x16x32_bf16 v[176:179], a[128:131], a[160:163], v[176:179]
s_add_u32 s97, s97, 0x12000
ds_read_b64_tr_b16 a[144:145], v46 offset:0
v_mfma_f32_16x16x32_bf16 v[180:183], a[128:131], a[164:167], v[180:183]
s_cmp_eq_u32 s97, s79
s_cselect_b32 s97, s76, s97
v_mfma_f32_16x16x32_bf16 v[184:187], a[128:131], a[168:171], v[184:187]
s_add_u32 m0, s61, 0x2000
ds_read_b64_tr_b16 a[146:147], v46 offset:512
v_mfma_f32_16x16x32_bf16 v[188:191], a[128:131], a[172:175], v[188:191]
buffer_load_dwordx4 v40, s[48:51], s97 offen lds
v_mfma_f32_16x16x32_bf16 v[204:207], a[132:135], a[172:175], v[204:207]
s_add_u32 m0, s61, 0x3000
ds_read_b64_tr_b16 a[148:149], v47 offset:0
v_mfma_f32_16x16x32_bf16 v[200:203], a[132:135], a[168:171], v[200:203]
ds_read_b64_tr_b16 a[150:151], v47 offset:512
ds_read_b128 a[176:179], v9 offset:4096
)ASM"
R"ASM(
v_mfma_f32_16x16x32_bf16 v[196:199], a[132:135], a[164:167], v[196:199]
ds_read_b128 a[180:183], v9 offset:5120
ds_read_b128 a[184:187], v8 offset:4096
v_mfma_f32_16x16x32_bf16 v[192:195], a[132:135], a[160:163], v[192:195]
ds_read_b128 a[188:191], v8 offset:5120
s_waitcnt lgkmcnt(8)
v_mfma_f32_16x16x32_bf16 v[208:211], a[136:139], a[160:163], v[208:211]
buffer_load_dwordx4 v41, s[48:51], s97 offen lds
v_mfma_f32_16x16x32_bf16 v[212:215], a[136:139], a[164:167], v[212:215]
s_add_u32 m0, s53, 0xc000
s_waitcnt vmcnt(8)
ds_read_b64_tr_b16 a[152:153], v4 offset:16384
v_mfma_f32_16x16x32_bf16 v[216:219], a[136:139], a[168:171], v[216:219]
buffer_load_dwordx4 v30, s[48:51], s73 offen offset:2048 lds
v_mfma_f32_16x16x32_bf16 v[220:223], a[136:139], a[172:175], v[220:223]
ds_read_b64_tr_b16 a[154:155], v4 offset:16896
v_mfma_f32_16x16x32_bf16 v[236:239], a[140:143], a[172:175], v[236:239]
buffer_load_dwordx4 v31, s[48:51], s73 offen offset:3072 lds
v_mfma_f32_16x16x32_bf16 v[232:235], a[140:143], a[168:171], v[232:235]
s_add_u32 m0, s63, 0
ds_read_b64_tr_b16 a[156:157], v5 offset:16384
v_mfma_f32_16x16x32_bf16 v[228:231], a[140:143], a[164:167], v[228:231]
ds_read_b64_tr_b16 a[158:159], v5 offset:16896
v_mfma_f32_16x16x32_bf16 v[224:227], a[140:143], a[160:163], v[224:227]
s_waitcnt lgkmcnt(8)
v_mfma_f32_16x16x32_bf16 v[240:243], a[144:147], a[160:163], v[240:243]
buffer_load_dwordx4 v24, s[48:51], s74 offen lds
v_mfma_f32_16x16x32_bf16 v[244:247], a[144:147], a[164:167], v[244:247]
ds_read_b64_tr_b16 a[128:129], v6 offset:16384
ds_read_b64_tr_b16 a[130:131], v6 offset:16896
v_mfma_f32_16x16x32_bf16 v[252:255], a[148:151], a[164:167], v[252:255]
ds_read_b64_tr_b16 a[132:133], v7 offset:16384
ds_read_b64_tr_b16 a[134:135], v7 offset:16896
v_mfma_f32_16x16x32_bf16 v[248:251], a[148:151], a[160:163], v[248:251]
s_waitcnt lgkmcnt(4)
v_mfma_f32_16x16x32_bf16 v[176:179], a[152:155], a[176:179], v[176:179]
buffer_load_dwordx4 v25, s[48:51], s74 offen offset:1024 lds
v_mfma_f32_16x16x32_bf16 v[180:183], a[152:155], a[180:183], v[180:183]
ds_read_b64_tr_b16 a[136:137], v46 offset:4096
s_add_u32 s55, s55, 0x2000
v_mfma_f32_16x16x32_bf16 v[184:187], a[152:155], a[184:187], v[184:187]
ds_read_b64_tr_b16 a[138:139], v46 offset:4608
v_mfma_f32_16x16x32_bf16 v[188:191], a[152:155], a[188:191], v[188:191]
s_cmp_eq_u32 s55, 0x20000
s_cselect_b32 s55, 0x10000, s55
v_mfma_f32_16x16x32_bf16 v[204:207], a[156:159], a[188:191], v[204:207]
buffer_load_dwordx4 v26, s[48:51], s74 offen offset:2048 lds
v_mfma_f32_16x16x32_bf16 v[200:203], a[156:159], a[184:187], v[200:203]
ds_read_b64_tr_b16 a[140:141], v47 offset:4096
v_add_u32 v9, s55, v10
v_mfma_f32_16x16x32_bf16 v[196:199], a[156:159], a[180:183], v[196:199]
ds_read_b64_tr_b16 a[142:143], v47 offset:4608
v_add_u32 v8, s55, v11
v_mfma_f32_16x16x32_bf16 v[192:195], a[156:159], a[176:179], v[192:195]
ds_read_b128 a[160:163], v9 offset:0
s_waitcnt lgkmcnt(5)
v_mfma_f32_16x16x32_bf16 v[208:211], a[128:131], a[176:179], v[208:211]
buffer_load_dwordx4 v27, s[48:51], s74 offen offset:3072 lds
v_mfma_f32_16x16x32_bf16 v[212:215], a[128:131], a[180:183], v[212:215]
s_add_u32 m0, s63, 0x4000
s_waitcnt vmcnt(10)
ds_read_b64_tr_b16 a[144:145], v42 offset:16384
v_mfma_f32_16x16x32_bf16 v[216:219], a[128:131], a[184:187], v[216:219]
ds_read_b64_tr_b16 a[146:147], v42 offset:16896
ds_read_b128 a[164:167], v9 offset:1024
v_mfma_f32_16x16x32_bf16 v[220:223], a[128:131], a[188:191], v[220:223]
ds_read_b128 a[168:171], v8 offset:0
ds_read_b128 a[172:175], v8 offset:1024
v_mfma_f32_16x16x32_bf16 v[236:239], a[132:135], a[188:191], v[236:239]
buffer_load_dwordx4 v28, s[48:51], s74 offen lds
v_mfma_f32_16x16x32_bf16 v[232:235], a[132:135], a[184:187], v[232:235]
ds_read_b64_tr_b16 a[148:149], v43 offset:16384
ds_read_b64_tr_b16 a[150:151], v43 offset:16896
v_mfma_f32_16x16x32_bf16 v[228:231], a[132:135], a[180:183], v[228:231]
v_mfma_f32_16x16x32_bf16 v[224:227], a[132:135], a[176:179], v[224:227]
s_waitcnt lgkmcnt(8)
v_mfma_f32_16x16x32_bf16 v[240:243], a[136:139], a[176:179], v[240:243]
buffer_load_dwordx4 v29, s[48:51], s74 offen offset:1024 lds
v_mfma_f32_16x16x32_bf16 v[244:247], a[136:139], a[180:183], v[244:247]
ds_read_b64_tr_b16 a[152:153], v44 offset:16384
ds_read_b64_tr_b16 a[154:155], v44 offset:16896
v_mfma_f32_16x16x32_bf16 v[252:255], a[140:143], a[180:183], v[252:255]
ds_read_b64_tr_b16 a[156:157], v45 offset:16384
ds_read_b64_tr_b16 a[158:159], v45 offset:16896
v_mfma_f32_16x16x32_bf16 v[248:251], a[140:143], a[176:179], v[248:251]
s_waitcnt lgkmcnt(4)
s_waitcnt vmcnt(8)
s_barrier
v_mfma_f32_16x16x32_bf16 v[176:179], a[144:147], a[160:163], v[176:179]
buffer_load_dwordx4 v30, s[48:51], s74 offen offset:2048 lds
v_mfma_f32_16x16x32_bf16 v[180:183], a[144:147], a[164:167], v[180:183]
ds_read_b64_tr_b16 a[128:129], v46 offset:8192
ds_read_b128 a[176:179], v9 offset:4096
v_mfma_f32_16x16x32_bf16 v[184:187], a[144:147], a[168:171], v[184:187]
ds_read_b64_tr_b16 a[130:131], v46 offset:8704
ds_read_b128 a[180:183], v9 offset:5120
v_mfma_f32_16x16x32_bf16 v[188:191], a[144:147], a[172:175], v[188:191]
ds_read_b128 a[184:187], v8 offset:4096
ds_read_b128 a[188:191], v8 offset:5120
v_mfma_f32_16x16x32_bf16 v[204:207], a[148:151], a[172:175], v[204:207]
buffer_load_dwordx4 v31, s[48:51], s74 offen offset:3072 lds
v_mfma_f32_16x16x32_bf16 v[200:203], a[148:151], a[168:171], v[200:203]
ds_read_b64_tr_b16 a[132:133], v47 offset:8192
ds_read_b64_tr_b16 a[134:135], v47 offset:8704
v_mfma_f32_16x16x32_bf16 v[196:199], a[148:151], a[164:167], v[196:199]
v_mfma_f32_16x16x32_bf16 v[192:195], a[148:151], a[160:163], v[192:195]
s_waitcnt lgkmcnt(8)
v_mfma_f32_16x16x32_bf16 v[208:211], a[152:155], a[160:163], v[208:211]
s_add_u32 s74, s74, 0x12000
s_waitcnt vmcnt(8)
ds_read_b64_tr_b16 a[136:137], v42 offset:49152
v_mfma_f32_16x16x32_bf16 v[212:215], a[152:155], a[164:167], v[212:215]
s_cmp_eq_u32 s74, s80
s_cselect_b32 s74, s77, s74
v_mfma_f32_16x16x32_bf16 v[216:219], a[152:155], a[168:171], v[216:219]
s_add_u32 m0, s53, 0x4000
ds_read_b64_tr_b16 a[138:139], v42 offset:49664
v_mfma_f32_16x16x32_bf16 v[220:223], a[152:155], a[172:175], v[220:223]
buffer_load_dwordx4 v24, s[48:51], s74 offen lds
v_mfma_f32_16x16x32_bf16 v[236:239], a[156:159], a[172:175], v[236:239]
ds_read_b64_tr_b16 a[140:141], v43 offset:49152
ds_read_b64_tr_b16 a[142:143], v43 offset:49664
v_mfma_f32_16x16x32_bf16 v[232:235], a[156:159], a[168:171], v[232:235]
v_mfma_f32_16x16x32_bf16 v[228:231], a[156:159], a[164:167], v[228:231]
v_mfma_f32_16x16x32_bf16 v[224:227], a[156:159], a[160:163], v[224:227]
s_waitcnt lgkmcnt(4)
v_mfma_f32_16x16x32_bf16 v[240:243], a[128:131], a[160:163], v[240:243]
buffer_load_dwordx4 v25, s[48:51], s74 offen offset:1024 lds
v_mfma_f32_16x16x32_bf16 v[244:247], a[128:131], a[164:167], v[244:247]
ds_read_b64_tr_b16 a[144:145], v44 offset:49152
ds_read_b64_tr_b16 a[146:147], v44 offset:49664
v_mfma_f32_16x16x32_bf16 v[252:255], a[132:135], a[164:167], v[252:255]
ds_read_b64_tr_b16 a[148:149], v45 offset:49152
ds_read_b64_tr_b16 a[150:151], v45 offset:49664
v_mfma_f32_16x16x32_bf16 v[248:251], a[132:135], a[160:163], v[248:251]
s_waitcnt lgkmcnt(4)
v_mfma_f32_16x16x32_bf16 v[176:179], a[136:139], a[176:179], v[176:179]
buffer_load_dwordx4 v26, s[48:51], s74 offen offset:2048 lds
v_mfma_f32_16x16x32_bf16 v[180:183], a[136:139], a[180:183], v[180:183]
ds_read_b64_tr_b16 a[152:153], v46 offset:12288
s_add_u32 s55, s55, 0x2000
v_mfma_f32_16x16x32_bf16 v[184:187], a[136:139], a[184:187], v[184:187]
ds_read_b64_tr_b16 a[154:155], v46 offset:12800
v_mfma_f32_16x16x32_bf16 v[188:191], a[136:139], a[188:191], v[188:191]
s_cmp_eq_u32 s55, 0x20000
s_cselect_b32 s55, 0x10000, s55
v_mfma_f32_16x16x32_bf16 v[204:207], a[140:143], a[188:191], v[204:207]
buffer_load_dwordx4 v27, s[48:51], s74 offen offset:3072 lds
v_mfma_f32_16x16x32_bf16 v[200:203], a[140:143], a[184:187], v[200:203]
ds_read_b64_tr_b16 a[156:157], v47 offset:12288
v_add_u32 v9, s55, v10
v_mfma_f32_16x16x32_bf16 v[196:199], a[140:143], a[180:183], v[196:199]
ds_read_b64_tr_b16 a[158:159], v47 offset:12800
v_add_u32 v8, s55, v11
v_mfma_f32_16x16x32_bf16 v[192:195], a[140:143], a[176:179], v[192:195]
ds_read_b128 a[160:163], v9 offset:0
s_waitcnt lgkmcnt(5)
v_mfma_f32_16x16x32_bf16 v[208:211], a[144:147], a[176:179], v[208:211]
s_add_u32 m0, s53, 0xc000
s_waitcnt vmcnt(8)
ds_read_b64_tr_b16 a[128:129], v4 offset:0
v_mfma_f32_16x16x32_bf16 v[212:215], a[144:147], a[180:183], v[212:215]
buffer_load_dwordx4 v28, s[48:51], s74 offen lds
v_mfma_f32_16x16x32_bf16 v[216:219], a[144:147], a[184:187], v[216:219]
ds_read_b64_tr_b16 a[130:131], v4 offset:512
ds_read_b128 a[164:167], v9 offset:1024
v_mfma_f32_16x16x32_bf16 v[220:223], a[144:147], a[188:191], v[220:223]
ds_read_b128 a[168:171], v8 offset:0
ds_read_b128 a[172:175], v8 offset:1024
v_mfma_f32_16x16x32_bf16 v[236:239], a[148:151], a[188:191], v[236:239]
ds_read_b64_tr_b16 a[132:133], v5 offset:0
ds_read_b64_tr_b16 a[134:135], v5 offset:512
v_mfma_f32_16x16x32_bf16 v[232:235], a[148:151], a[184:187], v[232:235]
buffer_load_dwordx4 v29, s[48:51], s74 offen offset:1024 lds
v_mfma_f32_16x16x32_bf16 v[228:231], a[148:151], a[180:183], v[228:231]
v_mfma_f32_16x16x32_bf16 v[224:227], a[148:151], a[176:179], v[224:227]
s_waitcnt lgkmcnt(8)
v_mfma_f32_16x16x32_bf16 v[240:243], a[152:155], a[176:179], v[240:243]
ds_read_b64_tr_b16 a[136:137], v6 offset:0
ds_read_b64_tr_b16 a[138:139], v6 offset:512
v_mfma_f32_16x16x32_bf16 v[244:247], a[152:155], a[180:183], v[244:247]
buffer_load_dwordx4 v30, s[48:51], s74 offen offset:2048 lds
v_mfma_f32_16x16x32_bf16 v[252:255], a[156:159], a[180:183], v[252:255]
ds_read_b64_tr_b16 a[140:141], v7 offset:0
ds_read_b64_tr_b16 a[142:143], v7 offset:512
v_mfma_f32_16x16x32_bf16 v[248:251], a[156:159], a[176:179], v[248:251]
s_waitcnt lgkmcnt(9)
v_mfma_f32_16x16x32_bf16 v[112:115], a[128:131], a[160:163], 0
buffer_load_dwordx4 v31, s[48:51], s74 offen offset:3072 lds
s_waitcnt lgkmcnt(8)
v_mfma_f32_16x16x32_bf16 v[116:119], a[128:131], a[164:167], 0
s_waitcnt vmcnt(8)
ds_read_b64_tr_b16 a[144:145], v4 offset:16384
ds_read_b128 a[176:179], v9 offset:4096
s_waitcnt lgkmcnt(9)
v_mfma_f32_16x16x32_bf16 v[120:123], a[128:131], a[168:171], 0
ds_read_b64_tr_b16 a[146:147], v4 offset:16896
ds_read_b128 a[180:183], v9 offset:5120
s_waitcnt lgkmcnt(10)
v_mfma_f32_16x16x32_bf16 v[124:127], a[128:131], a[172:175], 0
)ASM"
R"ASM(
ds_read_b128 a[184:187], v8 offset:4096
ds_read_b128 a[188:191], v8 offset:5120
s_waitcnt lgkmcnt(10)
v_mfma_f32_16x16x32_bf16 v[140:143], a[132:135], a[172:175], 0
ds_read_b64_tr_b16 a[148:149], v5 offset:16384
ds_read_b64_tr_b16 a[150:151], v5 offset:16896
v_mfma_f32_16x16x32_bf16 v[136:139], a[132:135], a[168:171], 0
v_cvt_pk_bf16_f32 v240, v240, v241
v_mfma_f32_16x16x32_bf16 v[132:135], a[132:135], a[164:167], 0
v_mfma_f32_16x16x32_bf16 v[128:131], a[132:135], a[160:163], 0
v_cvt_pk_bf16_f32 v241, v242, v243
s_waitcnt lgkmcnt(8)
v_mfma_f32_16x16x32_bf16 v[144:147], a[136:139], a[160:163], 0
s_add_u32 s74, s74, 0x12000
ds_read_b64_tr_b16 a[152:153], v6 offset:16384
v_mfma_f32_16x16x32_bf16 v[148:151], a[136:139], a[164:167], 0
s_cmp_eq_u32 s74, s80
s_cselect_b32 s74, s77, s74
ds_read_b64_tr_b16 a[154:155], v6 offset:16896
v_mfma_f32_16x16x32_bf16 v[152:155], a[136:139], a[168:171], 0
s_add_u32 m0, s63, 0
v_cvt_pk_bf16_f32 v242, v248, v249
v_mfma_f32_16x16x32_bf16 v[156:159], a[136:139], a[172:175], 0
buffer_load_dwordx4 v24, s[48:51], s74 offen lds
v_mfma_f32_16x16x32_bf16 v[172:175], a[140:143], a[172:175], 0
ds_read_b64_tr_b16 a[156:157], v7 offset:16384
ds_read_b64_tr_b16 a[158:159], v7 offset:16896
v_mfma_f32_16x16x32_bf16 v[168:171], a[140:143], a[168:171], 0
v_cvt_pk_bf16_f32 v243, v250, v251
v_mfma_f32_16x16x32_bf16 v[164:167], a[140:143], a[164:167], 0
buffer_store_dwordx4 v[240:243], v10, s[68:71], s44 offen sc1
v_mfma_f32_16x16x32_bf16 v[160:163], a[140:143], a[160:163], 0
s_waitcnt lgkmcnt(4)
v_mfma_f32_16x16x32_bf16 v[112:115], a[144:147], a[176:179], v[112:115]
buffer_load_dwordx4 v25, s[48:51], s74 offen offset:1024 lds
v_mfma_f32_16x16x32_bf16 v[116:119], a[144:147], a[180:183], v[116:119]
s_waitcnt vmcnt(7)
ds_read_b64_tr_b16 a[128:129], v42 offset:16384
s_add_u32 s55, s55, 0x2000
v_mfma_f32_16x16x32_bf16 v[120:123], a[144:147], a[184:187], v[120:123]
ds_read_b64_tr_b16 a[130:131], v42 offset:16896
v_mfma_f32_16x16x32_bf16 v[124:127], a[144:147], a[188:191], v[124:127]
s_cmp_eq_u32 s55, 0x20000
s_cselect_b32 s55, 0x10000, s55
v_add_u32 v9, s55, v10
v_mfma_f32_16x16x32_bf16 v[140:143], a[148:151], a[188:191], v[140:143]
buffer_load_dwordx4 v26, s[48:51], s74 offen offset:2048 lds
v_mfma_f32_16x16x32_bf16 v[136:139], a[148:151], a[184:187], v[136:139]
ds_read_b64_tr_b16 a[132:133], v43 offset:16384
v_add_u32 v8, s55, v11
ds_read_b128 a[160:163], v9 offset:0
v_mfma_f32_16x16x32_bf16 v[132:135], a[148:151], a[180:183], v[132:135]
ds_read_b64_tr_b16 a[134:135], v43 offset:16896
ds_read_b128 a[164:167], v9 offset:1024
v_mfma_f32_16x16x32_bf16 v[128:131], a[148:151], a[176:179], v[128:131]
ds_read_b128 a[168:171], v8 offset:0
ds_read_b128 a[172:175], v8 offset:1024
s_waitcnt lgkmcnt(8)
v_mfma_f32_16x16x32_bf16 v[144:147], a[152:155], a[176:179], v[144:147]
buffer_load_dwordx4 v27, s[48:51], s74 offen offset:3072 lds
v_mfma_f32_16x16x32_bf16 v[148:151], a[152:155], a[180:183], v[148:151]
s_add_u32 m0, s63, 0x4000
ds_read_b64_tr_b16 a[136:137], v44 offset:16384
ds_read_b64_tr_b16 a[138:139], v44 offset:16896
v_mfma_f32_16x16x32_bf16 v[152:155], a[152:155], a[184:187], v[152:155]
v_cvt_pk_bf16_f32 v244, v244, v245
v_cvt_pk_bf16_f32 v245, v246, v247
v_mfma_f32_16x16x32_bf16 v[156:159], a[152:155], a[188:191], v[156:159]
v_cvt_pk_bf16_f32 v246, v252, v253
v_cvt_pk_bf16_f32 v247, v254, v255
buffer_store_dwordx4 v[244:247], v10, s[68:71], s44 offen offset:1024 sc1
v_mfma_f32_16x16x32_bf16 v[172:175], a[156:159], a[188:191], v[172:175]
buffer_load_dwordx4 v28, s[48:51], s74 offen lds
v_mfma_f32_16x16x32_bf16 v[168:171], a[156:159], a[184:187], v[168:171]
ds_read_b64_tr_b16 a[140:141], v45 offset:16384
ds_read_b64_tr_b16 a[142:143], v45 offset:16896
v_cvt_pk_bf16_f32 v176, v176, v177
v_mfma_f32_16x16x32_bf16 v[164:167], a[156:159], a[180:183], v[164:167]
v_mfma_f32_16x16x32_bf16 v[160:163], a[156:159], a[176:179], v[160:163]
v_cvt_pk_bf16_f32 v177, v178, v179
s_waitcnt lgkmcnt(4)
v_mfma_f32_16x16x32_bf16 v[112:115], a[128:131], a[160:163], v[112:115]
buffer_load_dwordx4 v29, s[48:51], s74 offen offset:1024 lds
v_mfma_f32_16x16x32_bf16 v[116:119], a[128:131], a[164:167], v[116:119]
s_waitcnt vmcnt(8)
ds_read_b64_tr_b16 a[144:145], v42 offset:49152
ds_read_b128 a[176:179], v9 offset:4096
v_mfma_f32_16x16x32_bf16 v[120:123], a[128:131], a[168:171], v[120:123]
ds_read_b64_tr_b16 a[146:147], v42 offset:49664
ds_read_b128 a[180:183], v9 offset:5120
v_mfma_f32_16x16x32_bf16 v[124:127], a[128:131], a[172:175], v[124:127]
ds_read_b128 a[184:187], v8 offset:4096
ds_read_b128 a[188:191], v8 offset:5120
v_cvt_pk_bf16_f32 v178, v192, v193
v_mfma_f32_16x16x32_bf16 v[140:143], a[132:135], a[172:175], v[140:143]
buffer_load_dwordx4 v30, s[48:51], s74 offen offset:2048 lds
v_mfma_f32_16x16x32_bf16 v[136:139], a[132:135], a[168:171], v[136:139]
ds_read_b64_tr_b16 a[148:149], v43 offset:49152
ds_read_b64_tr_b16 a[150:151], v43 offset:49664
v_cvt_pk_bf16_f32 v179, v194, v195
v_mfma_f32_16x16x32_bf16 v[132:135], a[132:135], a[164:167], v[132:135]
buffer_store_dwordx4 v[176:179], v10, s[68:71], s40 offen sc1
v_mfma_f32_16x16x32_bf16 v[128:131], a[132:135], a[160:163], v[128:131]
s_waitcnt lgkmcnt(8)
v_mfma_f32_16x16x32_bf16 v[144:147], a[136:139], a[160:163], v[144:147]
buffer_load_dwordx4 v31, s[48:51], s74 offen offset:3072 lds
v_mfma_f32_16x16x32_bf16 v[148:151], a[136:139], a[164:167], v[148:151]
s_add_u32 s74, s74, 0x12000
ds_read_b64_tr_b16 a[152:153], v44 offset:49152
ds_read_b64_tr_b16 a[154:155], v44 offset:49664
v_mfma_f32_16x16x32_bf16 v[152:155], a[136:139], a[168:171], v[152:155]
s_cmp_eq_u32 s74, s80
s_cselect_b32 s74, s77, s74
v_mfma_f32_16x16x32_bf16 v[156:159], a[136:139], a[172:175], v[156:159]
s_add_u32 m0, s53, 0x4000
v_cvt_pk_bf16_f32 v180, v180, v181
v_cvt_pk_bf16_f32 v181, v182, v183
v_mfma_f32_16x16x32_bf16 v[172:175], a[140:143], a[172:175], v[172:175]
buffer_load_dwordx4 v24, s[48:51], s74 offen lds
v_mfma_f32_16x16x32_bf16 v[168:171], a[140:143], a[168:171], v[168:171]
ds_read_b64_tr_b16 a[156:157], v45 offset:49152
ds_read_b64_tr_b16 a[158:159], v45 offset:49664
v_cvt_pk_bf16_f32 v182, v196, v197
v_mfma_f32_16x16x32_bf16 v[164:167], a[140:143], a[164:167], v[164:167]
v_mfma_f32_16x16x32_bf16 v[160:163], a[140:143], a[160:163], v[160:163]
s_waitcnt lgkmcnt(4)
v_mfma_f32_16x16x32_bf16 v[112:115], a[144:147], a[176:179], v[112:115]
buffer_load_dwordx4 v25, s[48:51], s74 offen offset:1024 lds
v_mfma_f32_16x16x32_bf16 v[116:119], a[144:147], a[180:183], v[116:119]
s_waitcnt vmcnt(8)
ds_read_b64_tr_b16 a[128:129], v4 offset:0
s_add_u32 s55, s55, 0x2000
v_mfma_f32_16x16x32_bf16 v[120:123], a[144:147], a[184:187], v[120:123]
ds_read_b64_tr_b16 a[130:131], v4 offset:512
v_mfma_f32_16x16x32_bf16 v[124:127], a[144:147], a[188:191], v[124:127]
s_cmp_eq_u32 s55, 0x20000
s_cselect_b32 s55, 0x10000, s55
v_add_u32 v9, s55, v10
v_mfma_f32_16x16x32_bf16 v[140:143], a[148:151], a[188:191], v[140:143]
buffer_load_dwordx4 v26, s[48:51], s74 offen offset:2048 lds
v_mfma_f32_16x16x32_bf16 v[136:139], a[148:151], a[184:187], v[136:139]
ds_read_b64_tr_b16 a[132:133], v5 offset:0
v_add_u32 v8, s55, v11
ds_read_b128 a[160:163], v9 offset:0
v_mfma_f32_16x16x32_bf16 v[132:135], a[148:151], a[180:183], v[132:135]
ds_read_b64_tr_b16 a[134:135], v5 offset:512
ds_read_b128 a[164:167], v9 offset:1024
v_mfma_f32_16x16x32_bf16 v[128:131], a[148:151], a[176:179], v[128:131]
ds_read_b128 a[168:171], v8 offset:0
ds_read_b128 a[172:175], v8 offset:1024
s_waitcnt lgkmcnt(8)
v_mfma_f32_16x16x32_bf16 v[144:147], a[152:155], a[176:179], v[144:147]
buffer_load_dwordx4 v27, s[48:51], s74 offen offset:3072 lds
v_mfma_f32_16x16x32_bf16 v[148:151], a[152:155], a[180:183], v[148:151]
s_add_u32 m0, s53, 0xc000
ds_read_b64_tr_b16 a[136:137], v6 offset:0
ds_read_b64_tr_b16 a[138:139], v6 offset:512
v_mfma_f32_16x16x32_bf16 v[152:155], a[152:155], a[184:187], v[152:155]
v_cvt_pk_bf16_f32 v183, v198, v199
buffer_store_dwordx4 v[180:183], v10, s[68:71], s40 offen offset:1024 sc1
v_mfma_f32_16x16x32_bf16 v[156:159], a[152:155], a[188:191], v[156:159]
v_cvt_pk_bf16_f32 v184, v184, v185
v_cvt_pk_bf16_f32 v185, v186, v187
v_cvt_pk_bf16_f32 v186, v200, v201
v_mfma_f32_16x16x32_bf16 v[172:175], a[156:159], a[188:191], v[172:175]
buffer_load_dwordx4 v28, s[48:51], s74 offen lds
v_mfma_f32_16x16x32_bf16 v[168:171], a[156:159], a[184:187], v[168:171]
ds_read_b64_tr_b16 a[140:141], v7 offset:0
ds_read_b64_tr_b16 a[142:143], v7 offset:512
v_cvt_pk_bf16_f32 v187, v202, v203
v_mfma_f32_16x16x32_bf16 v[164:167], a[156:159], a[180:183], v[164:167]
buffer_store_dwordx4 v[184:187], v11, s[68:71], s40 offen sc1
v_mfma_f32_16x16x32_bf16 v[160:163], a[156:159], a[176:179], v[160:163]
s_waitcnt lgkmcnt(4)
v_mfma_f32_16x16x32_bf16 v[112:115], a[128:131], a[160:163], v[112:115]
buffer_load_dwordx4 v29, s[48:51], s74 offen offset:1024 lds
v_mfma_f32_16x16x32_bf16 v[116:119], a[128:131], a[164:167], v[116:119]
s_waitcnt vmcnt(8)
ds_read_b64_tr_b16 a[144:145], v4 offset:16384
ds_read_b128 a[176:179], v9 offset:4096
v_mfma_f32_16x16x32_bf16 v[120:123], a[128:131], a[168:171], v[120:123]
ds_read_b64_tr_b16 a[146:147], v4 offset:16896
ds_read_b128 a[180:183], v9 offset:5120
v_mfma_f32_16x16x32_bf16 v[124:127], a[128:131], a[172:175], v[124:127]
ds_read_b128 a[184:187], v8 offset:4096
ds_read_b128 a[188:191], v8 offset:5120
v_cvt_pk_bf16_f32 v188, v188, v189
v_mfma_f32_16x16x32_bf16 v[140:143], a[132:135], a[172:175], v[140:143]
buffer_load_dwordx4 v30, s[48:51], s74 offen offset:2048 lds
v_mfma_f32_16x16x32_bf16 v[136:139], a[132:135], a[168:171], v[136:139]
ds_read_b64_tr_b16 a[148:149], v5 offset:16384
ds_read_b64_tr_b16 a[150:151], v5 offset:16896
v_cvt_pk_bf16_f32 v189, v190, v191
v_mfma_f32_16x16x32_bf16 v[132:135], a[132:135], a[164:167], v[132:135]
v_cvt_pk_bf16_f32 v190, v204, v205
v_mfma_f32_16x16x32_bf16 v[128:131], a[132:135], a[160:163], v[128:131]
s_waitcnt lgkmcnt(8)
v_mfma_f32_16x16x32_bf16 v[144:147], a[136:139], a[160:163], v[144:147]
buffer_load_dwordx4 v31, s[48:51], s74 offen offset:3072 lds
v_mfma_f32_16x16x32_bf16 v[148:151], a[136:139], a[164:167], v[148:151]
)ASM"
R"ASM(
s_add_u32 s74, s74, 0x12000
ds_read_b64_tr_b16 a[152:153], v6 offset:16384
ds_read_b64_tr_b16 a[154:155], v6 offset:16896
v_mfma_f32_16x16x32_bf16 v[152:155], a[136:139], a[168:171], v[152:155]
s_cmp_eq_u32 s74, s80
s_cselect_b32 s74, s77, s74
v_mfma_f32_16x16x32_bf16 v[156:159], a[136:139], a[172:175], v[156:159]
s_add_u32 m0, s63, 0
v_cvt_pk_bf16_f32 v191, v206, v207
buffer_store_dwordx4 v[188:191], v11, s[68:71], s40 offen offset:1024 sc1
v_mfma_f32_16x16x32_bf16 v[172:175], a[140:143], a[172:175], v[172:175]
buffer_load_dwordx4 v24, s[48:51], s74 offen lds
v_mfma_f32_16x16x32_bf16 v[168:171], a[140:143], a[168:171], v[168:171]
ds_read_b64_tr_b16 a[156:157], v7 offset:16384
ds_read_b64_tr_b16 a[158:159], v7 offset:16896
v_cvt_pk_bf16_f32 v208, v208, v209
v_mfma_f32_16x16x32_bf16 v[164:167], a[140:143], a[164:167], v[164:167]
v_mfma_f32_16x16x32_bf16 v[160:163], a[140:143], a[160:163], v[160:163]
v_cvt_pk_bf16_f32 v209, v210, v211
s_waitcnt lgkmcnt(4)
v_mfma_f32_16x16x32_bf16 v[112:115], a[144:147], a[176:179], v[112:115]
buffer_load_dwordx4 v25, s[48:51], s74 offen offset:1024 lds
v_mfma_f32_16x16x32_bf16 v[116:119], a[144:147], a[180:183], v[116:119]
s_waitcnt vmcnt(9)
ds_read_b64_tr_b16 a[128:129], v42 offset:16384
s_add_u32 s55, s55, 0x2000
v_mfma_f32_16x16x32_bf16 v[120:123], a[144:147], a[184:187], v[120:123]
ds_read_b64_tr_b16 a[130:131], v42 offset:16896
v_mfma_f32_16x16x32_bf16 v[124:127], a[144:147], a[188:191], v[124:127]
s_cmp_eq_u32 s55, 0x20000
s_cselect_b32 s55, 0x10000, s55
v_add_u32 v9, s55, v10
v_mfma_f32_16x16x32_bf16 v[140:143], a[148:151], a[188:191], v[140:143]
buffer_load_dwordx4 v26, s[48:51], s74 offen offset:2048 lds
v_mfma_f32_16x16x32_bf16 v[136:139], a[148:151], a[184:187], v[136:139]
ds_read_b64_tr_b16 a[132:133], v43 offset:16384
v_add_u32 v8, s55, v11
ds_read_b128 a[160:163], v9 offset:0
v_mfma_f32_16x16x32_bf16 v[132:135], a[148:151], a[180:183], v[132:135]
ds_read_b64_tr_b16 a[134:135], v43 offset:16896
ds_read_b128 a[164:167], v9 offset:1024
v_mfma_f32_16x16x32_bf16 v[128:131], a[148:151], a[176:179], v[128:131]
ds_read_b128 a[168:171], v8 offset:0
ds_read_b128 a[172:175], v8 offset:1024
s_waitcnt lgkmcnt(8)
v_mfma_f32_16x16x32_bf16 v[144:147], a[152:155], a[176:179], v[144:147]
buffer_load_dwordx4 v27, s[48:51], s74 offen offset:3072 lds
v_mfma_f32_16x16x32_bf16 v[148:151], a[152:155], a[180:183], v[148:151]
s_add_u32 m0, s63, 0x4000
ds_read_b64_tr_b16 a[136:137], v44 offset:16384
ds_read_b64_tr_b16 a[138:139], v44 offset:16896
v_mfma_f32_16x16x32_bf16 v[152:155], a[152:155], a[184:187], v[152:155]
v_cvt_pk_bf16_f32 v210, v224, v225
v_cvt_pk_bf16_f32 v211, v226, v227
v_mfma_f32_16x16x32_bf16 v[156:159], a[152:155], a[188:191], v[156:159]
buffer_store_dwordx4 v[208:211], v10, s[68:71], s41 offen sc1
v_cvt_pk_bf16_f32 v212, v212, v213
v_mfma_f32_16x16x32_bf16 v[172:175], a[156:159], a[188:191], v[172:175]
buffer_load_dwordx4 v28, s[48:51], s74 offen lds
v_mfma_f32_16x16x32_bf16 v[168:171], a[156:159], a[184:187], v[168:171]
ds_read_b64_tr_b16 a[140:141], v45 offset:16384
ds_read_b64_tr_b16 a[142:143], v45 offset:16896
v_cvt_pk_bf16_f32 v213, v214, v215
v_mfma_f32_16x16x32_bf16 v[164:167], a[156:159], a[180:183], v[164:167]
v_cvt_pk_bf16_f32 v214, v228, v229
v_mfma_f32_16x16x32_bf16 v[160:163], a[156:159], a[176:179], v[160:163]
s_waitcnt lgkmcnt(4)
v_mfma_f32_16x16x32_bf16 v[112:115], a[128:131], a[160:163], v[112:115]
buffer_load_dwordx4 v29, s[48:51], s74 offen offset:1024 lds
v_mfma_f32_16x16x32_bf16 v[116:119], a[128:131], a[164:167], v[116:119]
s_waitcnt vmcnt(8)
ds_read_b64_tr_b16 a[144:145], v42 offset:49152
ds_read_b128 a[176:179], v9 offset:4096
v_mfma_f32_16x16x32_bf16 v[120:123], a[128:131], a[168:171], v[120:123]
ds_read_b64_tr_b16 a[146:147], v42 offset:49664
ds_read_b128 a[180:183], v9 offset:5120
v_mfma_f32_16x16x32_bf16 v[124:127], a[128:131], a[172:175], v[124:127]
ds_read_b128 a[184:187], v8 offset:4096
ds_read_b128 a[188:191], v8 offset:5120
v_cvt_pk_bf16_f32 v215, v230, v231
v_mfma_f32_16x16x32_bf16 v[140:143], a[132:135], a[172:175], v[140:143]
buffer_load_dwordx4 v30, s[48:51], s74 offen offset:2048 lds
v_mfma_f32_16x16x32_bf16 v[136:139], a[132:135], a[168:171], v[136:139]
ds_read_b64_tr_b16 a[148:149], v43 offset:49152
ds_read_b64_tr_b16 a[150:151], v43 offset:49664
buffer_store_dwordx4 v[212:215], v10, s[68:71], s41 offen offset:1024 sc1
v_mfma_f32_16x16x32_bf16 v[132:135], a[132:135], a[164:167], v[132:135]
v_cvt_pk_bf16_f32 v216, v216, v217
v_mfma_f32_16x16x32_bf16 v[128:131], a[132:135], a[160:163], v[128:131]
v_cvt_pk_bf16_f32 v217, v218, v219
s_waitcnt lgkmcnt(8)
v_mfma_f32_16x16x32_bf16 v[144:147], a[136:139], a[160:163], v[144:147]
buffer_load_dwordx4 v31, s[48:51], s74 offen offset:3072 lds
v_mfma_f32_16x16x32_bf16 v[148:151], a[136:139], a[164:167], v[148:151]
s_add_u32 s74, s74, 0x12000
ds_read_b64_tr_b16 a[152:153], v44 offset:49152
ds_read_b64_tr_b16 a[154:155], v44 offset:49664
v_mfma_f32_16x16x32_bf16 v[152:155], a[136:139], a[168:171], v[152:155]
s_cmp_eq_u32 s74, s80
s_cselect_b32 s74, s77, s74
v_mfma_f32_16x16x32_bf16 v[156:159], a[136:139], a[172:175], v[156:159]
s_add_u32 m0, s53, 0x4000
v_cvt_pk_bf16_f32 v218, v232, v233
v_mfma_f32_16x16x32_bf16 v[172:175], a[140:143], a[172:175], v[172:175]
buffer_load_dwordx4 v24, s[48:51], s74 offen lds
v_mfma_f32_16x16x32_bf16 v[168:171], a[140:143], a[168:171], v[168:171]
ds_read_b64_tr_b16 a[156:157], v45 offset:49152
ds_read_b64_tr_b16 a[158:159], v45 offset:49664
v_cvt_pk_bf16_f32 v219, v234, v235
v_mfma_f32_16x16x32_bf16 v[164:167], a[140:143], a[164:167], v[164:167]
buffer_store_dwordx4 v[216:219], v11, s[68:71], s41 offen sc1
v_mfma_f32_16x16x32_bf16 v[160:163], a[140:143], a[160:163], v[160:163]
s_waitcnt lgkmcnt(4)
v_mfma_f32_16x16x32_bf16 v[112:115], a[144:147], a[176:179], v[112:115]
buffer_load_dwordx4 v25, s[48:51], s74 offen offset:1024 lds
v_mfma_f32_16x16x32_bf16 v[116:119], a[144:147], a[180:183], v[116:119]
s_waitcnt vmcnt(9)
ds_read_b64_tr_b16 a[128:129], v4 offset:0
s_add_u32 s55, s55, 0x2000
v_mfma_f32_16x16x32_bf16 v[120:123], a[144:147], a[184:187], v[120:123]
ds_read_b64_tr_b16 a[130:131], v4 offset:512
v_mfma_f32_16x16x32_bf16 v[124:127], a[144:147], a[188:191], v[124:127]
s_cmp_eq_u32 s55, 0x20000
s_cselect_b32 s55, 0x10000, s55
v_add_u32 v9, s55, v10
v_mfma_f32_16x16x32_bf16 v[140:143], a[148:151], a[188:191], v[140:143]
buffer_load_dwordx4 v26, s[48:51], s74 offen offset:2048 lds
v_mfma_f32_16x16x32_bf16 v[136:139], a[148:151], a[184:187], v[136:139]
ds_read_b64_tr_b16 a[132:133], v5 offset:0
v_add_u32 v8, s55, v11
ds_read_b128 a[160:163], v9 offset:0
v_mfma_f32_16x16x32_bf16 v[132:135], a[148:151], a[180:183], v[132:135]
ds_read_b64_tr_b16 a[134:135], v5 offset:512
ds_read_b128 a[164:167], v9 offset:1024
v_mfma_f32_16x16x32_bf16 v[128:131], a[148:151], a[176:179], v[128:131]
ds_read_b128 a[168:171], v8 offset:0
ds_read_b128 a[172:175], v8 offset:1024
s_waitcnt lgkmcnt(8)
v_mfma_f32_16x16x32_bf16 v[144:147], a[152:155], a[176:179], v[144:147]
buffer_load_dwordx4 v27, s[48:51], s74 offen offset:3072 lds
v_mfma_f32_16x16x32_bf16 v[148:151], a[152:155], a[180:183], v[148:151]
s_add_u32 m0, s53, 0xc000
ds_read_b64_tr_b16 a[136:137], v6 offset:0
ds_read_b64_tr_b16 a[138:139], v6 offset:512
v_mfma_f32_16x16x32_bf16 v[152:155], a[152:155], a[184:187], v[152:155]
v_cvt_pk_bf16_f32 v220, v220, v221
v_cvt_pk_bf16_f32 v221, v222, v223
v_mfma_f32_16x16x32_bf16 v[156:159], a[152:155], a[188:191], v[156:159]
v_cvt_pk_bf16_f32 v222, v236, v237
v_cvt_pk_bf16_f32 v223, v238, v239
buffer_store_dwordx4 v[220:223], v11, s[68:71], s41 offen offset:1024 sc1
v_mfma_f32_16x16x32_bf16 v[172:175], a[156:159], a[188:191], v[172:175]
buffer_load_dwordx4 v28, s[48:51], s74 offen lds
v_mfma_f32_16x16x32_bf16 v[168:171], a[156:159], a[184:187], v[168:171]
ds_read_b64_tr_b16 a[140:141], v7 offset:0
ds_read_b64_tr_b16 a[142:143], v7 offset:512
v_mfma_f32_16x16x32_bf16 v[164:167], a[156:159], a[180:183], v[164:167]
v_mfma_f32_16x16x32_bf16 v[160:163], a[156:159], a[176:179], v[160:163]
s_waitcnt lgkmcnt(4)
v_mfma_f32_16x16x32_bf16 v[112:115], a[128:131], a[160:163], v[112:115]
buffer_load_dwordx4 v29, s[48:51], s74 offen offset:1024 lds
v_mfma_f32_16x16x32_bf16 v[116:119], a[128:131], a[164:167], v[116:119]
s_waitcnt vmcnt(8)
ds_read_b64_tr_b16 a[144:145], v4 offset:16384
ds_read_b128 a[176:179], v9 offset:4096
v_mfma_f32_16x16x32_bf16 v[120:123], a[128:131], a[168:171], v[120:123]
ds_read_b64_tr_b16 a[146:147], v4 offset:16896
ds_read_b128 a[180:183], v9 offset:5120
v_mfma_f32_16x16x32_bf16 v[124:127], a[128:131], a[172:175], v[124:127]
ds_read_b128 a[184:187], v8 offset:4096
ds_read_b128 a[188:191], v8 offset:5120
v_mfma_f32_16x16x32_bf16 v[140:143], a[132:135], a[172:175], v[140:143]
buffer_load_dwordx4 v30, s[48:51], s74 offen offset:2048 lds
v_mfma_f32_16x16x32_bf16 v[136:139], a[132:135], a[168:171], v[136:139]
ds_read_b64_tr_b16 a[148:149], v5 offset:16384
ds_read_b64_tr_b16 a[150:151], v5 offset:16896
v_mfma_f32_16x16x32_bf16 v[132:135], a[132:135], a[164:167], v[132:135]
v_mfma_f32_16x16x32_bf16 v[128:131], a[132:135], a[160:163], v[128:131]
s_waitcnt lgkmcnt(8)
v_mfma_f32_16x16x32_bf16 v[144:147], a[136:139], a[160:163], v[144:147]
buffer_load_dwordx4 v31, s[48:51], s74 offen offset:3072 lds
v_mfma_f32_16x16x32_bf16 v[148:151], a[136:139], a[164:167], v[148:151]
s_add_u32 s74, s74, 0x12000
ds_read_b64_tr_b16 a[152:153], v6 offset:16384
ds_read_b64_tr_b16 a[154:155], v6 offset:16896
v_mfma_f32_16x16x32_bf16 v[152:155], a[136:139], a[168:171], v[152:155]
s_cmp_eq_u32 s74, s80
s_cselect_b32 s74, s77, s74
v_mfma_f32_16x16x32_bf16 v[156:159], a[136:139], a[172:175], v[156:159]
s_add_u32 m0, s63, 0
v_mfma_f32_16x16x32_bf16 v[172:175], a[140:143], a[172:175], v[172:175]
buffer_load_dwordx4 v24, s[48:51], s74 offen lds
v_mfma_f32_16x16x32_bf16 v[168:171], a[140:143], a[168:171], v[168:171]
ds_read_b64_tr_b16 a[156:157], v7 offset:16384
ds_read_b64_tr_b16 a[158:159], v7 offset:16896
v_mfma_f32_16x16x32_bf16 v[164:167], a[140:143], a[164:167], v[164:167]
v_mfma_f32_16x16x32_bf16 v[160:163], a[140:143], a[160:163], v[160:163]
s_waitcnt lgkmcnt(4)
v_mfma_f32_16x16x32_bf16 v[112:115], a[144:147], a[176:179], v[112:115]
buffer_load_dwordx4 v25, s[48:51], s74 offen offset:1024 lds
)ASM"
R"ASM(
v_mfma_f32_16x16x32_bf16 v[116:119], a[144:147], a[180:183], v[116:119]
s_waitcnt vmcnt(7)
ds_read_b64_tr_b16 a[128:129], v42 offset:16384
s_add_u32 s55, s55, 0x2000
v_mfma_f32_16x16x32_bf16 v[120:123], a[144:147], a[184:187], v[120:123]
ds_read_b64_tr_b16 a[130:131], v42 offset:16896
v_mfma_f32_16x16x32_bf16 v[124:127], a[144:147], a[188:191], v[124:127]
s_cmp_eq_u32 s55, 0x20000
s_cselect_b32 s55, 0x10000, s55
v_add_u32 v9, s55, v10
v_mfma_f32_16x16x32_bf16 v[140:143], a[148:151], a[188:191], v[140:143]
buffer_load_dwordx4 v26, s[48:51], s74 offen offset:2048 lds
v_mfma_f32_16x16x32_bf16 v[136:139], a[148:151], a[184:187], v[136:139]
ds_read_b64_tr_b16 a[132:133], v43 offset:16384
v_add_u32 v8, s55, v11
ds_read_b128 a[160:163], v9 offset:0
v_mfma_f32_16x16x32_bf16 v[132:135], a[148:151], a[180:183], v[132:135]
ds_read_b64_tr_b16 a[134:135], v43 offset:16896
ds_read_b128 a[164:167], v9 offset:1024
v_mfma_f32_16x16x32_bf16 v[128:131], a[148:151], a[176:179], v[128:131]
ds_read_b128 a[168:171], v8 offset:0
ds_read_b128 a[172:175], v8 offset:1024
s_waitcnt lgkmcnt(8)
v_mfma_f32_16x16x32_bf16 v[144:147], a[152:155], a[176:179], v[144:147]
buffer_load_dwordx4 v27, s[48:51], s74 offen offset:3072 lds
v_mfma_f32_16x16x32_bf16 v[148:151], a[152:155], a[180:183], v[148:151]
s_add_u32 m0, s63, 0x4000
ds_read_b64_tr_b16 a[136:137], v44 offset:16384
ds_read_b64_tr_b16 a[138:139], v44 offset:16896
v_mfma_f32_16x16x32_bf16 v[152:155], a[152:155], a[184:187], v[152:155]
v_mfma_f32_16x16x32_bf16 v[156:159], a[152:155], a[188:191], v[156:159]
v_mfma_f32_16x16x32_bf16 v[172:175], a[156:159], a[188:191], v[172:175]
buffer_load_dwordx4 v28, s[48:51], s74 offen lds
v_mfma_f32_16x16x32_bf16 v[168:171], a[156:159], a[184:187], v[168:171]
ds_read_b64_tr_b16 a[140:141], v45 offset:16384
ds_read_b64_tr_b16 a[142:143], v45 offset:16896
v_mfma_f32_16x16x32_bf16 v[164:167], a[156:159], a[180:183], v[164:167]
v_mfma_f32_16x16x32_bf16 v[160:163], a[156:159], a[176:179], v[160:163]
s_waitcnt lgkmcnt(4)
v_mfma_f32_16x16x32_bf16 v[112:115], a[128:131], a[160:163], v[112:115]
buffer_load_dwordx4 v29, s[48:51], s74 offen offset:1024 lds
v_mfma_f32_16x16x32_bf16 v[116:119], a[128:131], a[164:167], v[116:119]
s_waitcnt vmcnt(6)
ds_read_b64_tr_b16 a[144:145], v42 offset:49152
ds_read_b128 a[176:179], v9 offset:4096
v_mfma_f32_16x16x32_bf16 v[120:123], a[128:131], a[168:171], v[120:123]
ds_read_b64_tr_b16 a[146:147], v42 offset:49664
ds_read_b128 a[180:183], v9 offset:5120
v_mfma_f32_16x16x32_bf16 v[124:127], a[128:131], a[172:175], v[124:127]
ds_read_b128 a[184:187], v8 offset:4096
ds_read_b128 a[188:191], v8 offset:5120
v_mfma_f32_16x16x32_bf16 v[140:143], a[132:135], a[172:175], v[140:143]
buffer_load_dwordx4 v30, s[48:51], s74 offen offset:2048 lds
v_mfma_f32_16x16x32_bf16 v[136:139], a[132:135], a[168:171], v[136:139]
ds_read_b64_tr_b16 a[148:149], v43 offset:49152
ds_read_b64_tr_b16 a[150:151], v43 offset:49664
v_mfma_f32_16x16x32_bf16 v[132:135], a[132:135], a[164:167], v[132:135]
v_mfma_f32_16x16x32_bf16 v[128:131], a[132:135], a[160:163], v[128:131]
s_waitcnt lgkmcnt(8)
v_mfma_f32_16x16x32_bf16 v[144:147], a[136:139], a[160:163], v[144:147]
buffer_load_dwordx4 v31, s[48:51], s74 offen offset:3072 lds
v_mfma_f32_16x16x32_bf16 v[148:151], a[136:139], a[164:167], v[148:151]
s_add_u32 s74, s74, 0x12000
ds_read_b64_tr_b16 a[152:153], v44 offset:49152
ds_read_b64_tr_b16 a[154:155], v44 offset:49664
v_mfma_f32_16x16x32_bf16 v[152:155], a[136:139], a[168:171], v[152:155]
s_cmp_eq_u32 s74, s80
s_cselect_b32 s74, s77, s74
v_mfma_f32_16x16x32_bf16 v[156:159], a[136:139], a[172:175], v[156:159]
s_add_u32 m0, s53, 0x4000
v_mfma_f32_16x16x32_bf16 v[172:175], a[140:143], a[172:175], v[172:175]
buffer_load_dwordx4 v24, s[48:51], s74 offen lds
v_mfma_f32_16x16x32_bf16 v[168:171], a[140:143], a[168:171], v[168:171]
ds_read_b64_tr_b16 a[156:157], v45 offset:49152
ds_read_b64_tr_b16 a[158:159], v45 offset:49664
v_mfma_f32_16x16x32_bf16 v[164:167], a[140:143], a[164:167], v[164:167]
v_mfma_f32_16x16x32_bf16 v[160:163], a[140:143], a[160:163], v[160:163]
s_waitcnt lgkmcnt(4)
v_mfma_f32_16x16x32_bf16 v[112:115], a[144:147], a[176:179], v[112:115]
buffer_load_dwordx4 v25, s[48:51], s74 offen offset:1024 lds
v_mfma_f32_16x16x32_bf16 v[116:119], a[144:147], a[180:183], v[116:119]
s_waitcnt vmcnt(6)
ds_read_b64_tr_b16 a[128:129], v4 offset:0
s_add_u32 s55, s55, 0x2000
v_mfma_f32_16x16x32_bf16 v[120:123], a[144:147], a[184:187], v[120:123]
ds_read_b64_tr_b16 a[130:131], v4 offset:512
v_mfma_f32_16x16x32_bf16 v[124:127], a[144:147], a[188:191], v[124:127]
s_cmp_eq_u32 s55, 0x20000
s_cselect_b32 s55, 0x10000, s55
v_add_u32 v9, s55, v10
v_mfma_f32_16x16x32_bf16 v[140:143], a[148:151], a[188:191], v[140:143]
buffer_load_dwordx4 v26, s[48:51], s74 offen offset:2048 lds
v_mfma_f32_16x16x32_bf16 v[136:139], a[148:151], a[184:187], v[136:139]
ds_read_b64_tr_b16 a[132:133], v5 offset:0
v_add_u32 v8, s55, v11
ds_read_b128 a[160:163], v9 offset:0
v_mfma_f32_16x16x32_bf16 v[132:135], a[148:151], a[180:183], v[132:135]
ds_read_b64_tr_b16 a[134:135], v5 offset:512
ds_read_b128 a[164:167], v9 offset:1024
v_mfma_f32_16x16x32_bf16 v[128:131], a[148:151], a[176:179], v[128:131]
ds_read_b128 a[168:171], v8 offset:0
ds_read_b128 a[172:175], v8 offset:1024
s_waitcnt lgkmcnt(8)
v_mfma_f32_16x16x32_bf16 v[144:147], a[152:155], a[176:179], v[144:147]
buffer_load_dwordx4 v27, s[48:51], s74 offen offset:3072 lds
v_mfma_f32_16x16x32_bf16 v[148:151], a[152:155], a[180:183], v[148:151]
s_add_u32 m0, s53, 0xc000
ds_read_b64_tr_b16 a[136:137], v6 offset:0
ds_read_b64_tr_b16 a[138:139], v6 offset:512
v_mfma_f32_16x16x32_bf16 v[152:155], a[152:155], a[184:187], v[152:155]
v_mfma_f32_16x16x32_bf16 v[156:159], a[152:155], a[188:191], v[156:159]
v_mfma_f32_16x16x32_bf16 v[172:175], a[156:159], a[188:191], v[172:175]
buffer_load_dwordx4 v28, s[48:51], s74 offen lds
v_mfma_f32_16x16x32_bf16 v[168:171], a[156:159], a[184:187], v[168:171]
ds_read_b64_tr_b16 a[140:141], v7 offset:0
ds_read_b64_tr_b16 a[142:143], v7 offset:512
v_mfma_f32_16x16x32_bf16 v[164:167], a[156:159], a[180:183], v[164:167]
v_mfma_f32_16x16x32_bf16 v[160:163], a[156:159], a[176:179], v[160:163]
s_waitcnt lgkmcnt(4)
v_mfma_f32_16x16x32_bf16 v[112:115], a[128:131], a[160:163], v[112:115]
buffer_load_dwordx4 v29, s[48:51], s74 offen offset:1024 lds
v_mfma_f32_16x16x32_bf16 v[116:119], a[128:131], a[164:167], v[116:119]
s_waitcnt vmcnt(6)
ds_read_b64_tr_b16 a[144:145], v4 offset:16384
ds_read_b128 a[176:179], v9 offset:4096
v_mfma_f32_16x16x32_bf16 v[120:123], a[128:131], a[168:171], v[120:123]
ds_read_b64_tr_b16 a[146:147], v4 offset:16896
ds_read_b128 a[180:183], v9 offset:5120
v_mfma_f32_16x16x32_bf16 v[124:127], a[128:131], a[172:175], v[124:127]
ds_read_b128 a[184:187], v8 offset:4096
ds_read_b128 a[188:191], v8 offset:5120
v_mfma_f32_16x16x32_bf16 v[140:143], a[132:135], a[172:175], v[140:143]
buffer_load_dwordx4 v30, s[48:51], s74 offen offset:2048 lds
v_mfma_f32_16x16x32_bf16 v[136:139], a[132:135], a[168:171], v[136:139]
ds_read_b64_tr_b16 a[148:149], v5 offset:16384
ds_read_b64_tr_b16 a[150:151], v5 offset:16896
v_mfma_f32_16x16x32_bf16 v[132:135], a[132:135], a[164:167], v[132:135]
v_mfma_f32_16x16x32_bf16 v[128:131], a[132:135], a[160:163], v[128:131]
s_waitcnt lgkmcnt(8)
v_mfma_f32_16x16x32_bf16 v[144:147], a[136:139], a[160:163], v[144:147]
buffer_load_dwordx4 v31, s[48:51], s74 offen offset:3072 lds
v_mfma_f32_16x16x32_bf16 v[148:151], a[136:139], a[164:167], v[148:151]
ds_read_b64_tr_b16 a[152:153], v6 offset:16384
ds_read_b64_tr_b16 a[154:155], v6 offset:16896
v_mfma_f32_16x16x32_bf16 v[152:155], a[136:139], a[168:171], v[152:155]
v_mfma_f32_16x16x32_bf16 v[156:159], a[136:139], a[172:175], v[156:159]
v_mfma_f32_16x16x32_bf16 v[172:175], a[140:143], a[172:175], v[172:175]
ds_read_b64_tr_b16 a[156:157], v7 offset:16384
ds_read_b64_tr_b16 a[158:159], v7 offset:16896
v_mfma_f32_16x16x32_bf16 v[168:171], a[140:143], a[168:171], v[168:171]
v_mfma_f32_16x16x32_bf16 v[164:167], a[140:143], a[164:167], v[164:167]
v_mfma_f32_16x16x32_bf16 v[160:163], a[140:143], a[160:163], v[160:163]
s_waitcnt lgkmcnt(4)
v_mfma_f32_16x16x32_bf16 v[112:115], a[144:147], a[176:179], v[112:115]
s_waitcnt vmcnt(4)
ds_read_b64_tr_b16 a[128:129], v42 offset:16384
v_mfma_f32_16x16x32_bf16 v[116:119], a[144:147], a[180:183], v[116:119]
ds_read_b64_tr_b16 a[130:131], v42 offset:16896
s_add_u32 s55, s55, 0x2000
v_mfma_f32_16x16x32_bf16 v[120:123], a[144:147], a[184:187], v[120:123]
s_cmp_eq_u32 s55, 0x20000
s_cselect_b32 s55, 0x10000, s55
v_mfma_f32_16x16x32_bf16 v[124:127], a[144:147], a[188:191], v[124:127]
v_add_u32 v9, s55, v10
v_add_u32 v8, s55, v11
ds_read_b128 a[160:163], v9 offset:0
v_mfma_f32_16x16x32_bf16 v[140:143], a[148:151], a[188:191], v[140:143]
ds_read_b64_tr_b16 a[132:133], v43 offset:16384
ds_read_b128 a[164:167], v9 offset:1024
v_mfma_f32_16x16x32_bf16 v[136:139], a[148:151], a[184:187], v[136:139]
ds_read_b64_tr_b16 a[134:135], v43 offset:16896
ds_read_b128 a[168:171], v8 offset:0
ds_read_b128 a[172:175], v8 offset:1024
v_mfma_f32_16x16x32_bf16 v[132:135], a[148:151], a[180:183], v[132:135]
v_mfma_f32_16x16x32_bf16 v[128:131], a[148:151], a[176:179], v[128:131]
s_waitcnt lgkmcnt(8)
v_mfma_f32_16x16x32_bf16 v[144:147], a[152:155], a[176:179], v[144:147]
ds_read_b64_tr_b16 a[136:137], v44 offset:16384
ds_read_b64_tr_b16 a[138:139], v44 offset:16896
v_mfma_f32_16x16x32_bf16 v[148:151], a[152:155], a[180:183], v[148:151]
v_mfma_f32_16x16x32_bf16 v[152:155], a[152:155], a[184:187], v[152:155]
v_mfma_f32_16x16x32_bf16 v[156:159], a[152:155], a[188:191], v[156:159]
v_mfma_f32_16x16x32_bf16 v[172:175], a[156:159], a[188:191], v[172:175]
ds_read_b64_tr_b16 a[140:141], v45 offset:16384
s_waitcnt vmcnt(0)
buffer_atomic_add v72, v73, s[12:15], 0 offen offset:0 sc1
v_mfma_f32_16x16x32_bf16 v[168:171], a[156:159], a[184:187], v[168:171]
ds_read_b64_tr_b16 a[142:143], v45 offset:16896
v_mfma_f32_16x16x32_bf16 v[164:167], a[156:159], a[180:183], v[164:167]
v_mfma_f32_16x16x32_bf16 v[160:163], a[156:159], a[176:179], v[160:163]
s_waitcnt lgkmcnt(4)
v_mfma_f32_16x16x32_bf16 v[112:115], a[128:131], a[160:163], v[112:115]
ds_read_b64_tr_b16 a[144:145], v42 offset:49152
v_mfma_f32_16x16x32_bf16 v[116:119], a[128:131], a[164:167], v[116:119]
ds_read_b64_tr_b16 a[146:147], v42 offset:49664
ds_read_b128 a[176:179], v9 offset:4096
ds_read_b128 a[180:183], v9 offset:5120
v_mfma_f32_16x16x32_bf16 v[120:123], a[128:131], a[168:171], v[120:123]
ds_read_b128 a[184:187], v8 offset:4096
ds_read_b128 a[188:191], v8 offset:5120
)ASM"
R"ASM(
v_mfma_f32_16x16x32_bf16 v[124:127], a[128:131], a[172:175], v[124:127]
v_mfma_f32_16x16x32_bf16 v[140:143], a[132:135], a[172:175], v[140:143]
ds_read_b64_tr_b16 a[148:149], v43 offset:49152
ds_read_b64_tr_b16 a[150:151], v43 offset:49664
v_mfma_f32_16x16x32_bf16 v[136:139], a[132:135], a[168:171], v[136:139]
v_mfma_f32_16x16x32_bf16 v[132:135], a[132:135], a[164:167], v[132:135]
v_mfma_f32_16x16x32_bf16 v[128:131], a[132:135], a[160:163], v[128:131]
s_waitcnt lgkmcnt(8)
v_mfma_f32_16x16x32_bf16 v[144:147], a[136:139], a[160:163], v[144:147]
ds_read_b64_tr_b16 a[152:153], v44 offset:49152
ds_read_b64_tr_b16 a[154:155], v44 offset:49664
v_mfma_f32_16x16x32_bf16 v[148:151], a[136:139], a[164:167], v[148:151]
v_mfma_f32_16x16x32_bf16 v[152:155], a[136:139], a[168:171], v[152:155]
v_mfma_f32_16x16x32_bf16 v[156:159], a[136:139], a[172:175], v[156:159]
v_mfma_f32_16x16x32_bf16 v[172:175], a[140:143], a[172:175], v[172:175]
ds_read_b64_tr_b16 a[156:157], v45 offset:49152
ds_read_b64_tr_b16 a[158:159], v45 offset:49664
v_mfma_f32_16x16x32_bf16 v[168:171], a[140:143], a[168:171], v[168:171]
v_mfma_f32_16x16x32_bf16 v[164:167], a[140:143], a[164:167], v[164:167]
v_mfma_f32_16x16x32_bf16 v[160:163], a[140:143], a[160:163], v[160:163]
s_waitcnt lgkmcnt(4)
v_mfma_f32_16x16x32_bf16 v[112:115], a[144:147], a[176:179], v[112:115]
v_mfma_f32_16x16x32_bf16 v[116:119], a[144:147], a[180:183], v[116:119]
v_mfma_f32_16x16x32_bf16 v[120:123], a[144:147], a[184:187], v[120:123]
v_mfma_f32_16x16x32_bf16 v[124:127], a[144:147], a[188:191], v[124:127]
v_mfma_f32_16x16x32_bf16 v[140:143], a[148:151], a[188:191], v[140:143]
v_mfma_f32_16x16x32_bf16 v[136:139], a[148:151], a[184:187], v[136:139]
v_mfma_f32_16x16x32_bf16 v[132:135], a[148:151], a[180:183], v[132:135]
v_mfma_f32_16x16x32_bf16 v[128:131], a[148:151], a[176:179], v[128:131]
s_waitcnt lgkmcnt(0)
v_mfma_f32_16x16x32_bf16 v[144:147], a[152:155], a[176:179], v[144:147]
v_mfma_f32_16x16x32_bf16 v[148:151], a[152:155], a[180:183], v[148:151]
v_mfma_f32_16x16x32_bf16 v[152:155], a[152:155], a[184:187], v[152:155]
v_mfma_f32_16x16x32_bf16 v[156:159], a[152:155], a[188:191], v[156:159]
v_mfma_f32_16x16x32_bf16 v[172:175], a[156:159], a[188:191], v[172:175]
v_mfma_f32_16x16x32_bf16 v[168:171], a[156:159], a[184:187], v[168:171]
v_mfma_f32_16x16x32_bf16 v[164:167], a[156:159], a[180:183], v[164:167]
v_mfma_f32_16x16x32_bf16 v[160:163], a[156:159], a[176:179], v[160:163]
v_cvt_pk_bf16_f32 v112, v112, v113
v_cvt_pk_bf16_f32 v113, v114, v115
v_cvt_pk_bf16_f32 v114, v128, v129
v_cvt_pk_bf16_f32 v115, v130, v131
buffer_store_dwordx4 v[112:115], v10, s[68:71], s42 offen sc1
v_cvt_pk_bf16_f32 v116, v116, v117
v_cvt_pk_bf16_f32 v117, v118, v119
v_cvt_pk_bf16_f32 v118, v132, v133
v_cvt_pk_bf16_f32 v119, v134, v135
buffer_store_dwordx4 v[116:119], v10, s[68:71], s42 offen offset:1024 sc1
v_cvt_pk_bf16_f32 v120, v120, v121
v_cvt_pk_bf16_f32 v121, v122, v123
v_cvt_pk_bf16_f32 v122, v136, v137
v_cvt_pk_bf16_f32 v123, v138, v139
buffer_store_dwordx4 v[120:123], v11, s[68:71], s42 offen sc1
v_cvt_pk_bf16_f32 v124, v124, v125
v_cvt_pk_bf16_f32 v125, v126, v127
v_cvt_pk_bf16_f32 v126, v140, v141
v_cvt_pk_bf16_f32 v127, v142, v143
buffer_store_dwordx4 v[124:127], v11, s[68:71], s42 offen offset:1024 sc1
v_cvt_pk_bf16_f32 v144, v144, v145
v_cvt_pk_bf16_f32 v145, v146, v147
v_cvt_pk_bf16_f32 v146, v160, v161
v_cvt_pk_bf16_f32 v147, v162, v163
buffer_store_dwordx4 v[144:147], v10, s[68:71], s43 offen sc1
v_cvt_pk_bf16_f32 v148, v148, v149
v_cvt_pk_bf16_f32 v149, v150, v151
v_cvt_pk_bf16_f32 v150, v164, v165
v_cvt_pk_bf16_f32 v151, v166, v167
buffer_store_dwordx4 v[148:151], v10, s[68:71], s43 offen offset:1024 sc1
v_cvt_pk_bf16_f32 v152, v152, v153
v_cvt_pk_bf16_f32 v153, v154, v155
v_cvt_pk_bf16_f32 v154, v168, v169
v_cvt_pk_bf16_f32 v155, v170, v171
buffer_store_dwordx4 v[152:155], v11, s[68:71], s43 offen sc1
v_cvt_pk_bf16_f32 v156, v156, v157
v_cvt_pk_bf16_f32 v157, v158, v159
v_cvt_pk_bf16_f32 v158, v172, v173
v_cvt_pk_bf16_f32 v159, v174, v175
buffer_store_dwordx4 v[156:159], v11, s[68:71], s43 offen offset:1024 sc1
s_branch .Ls1_entry
.Lk1_fallback:
s_waitcnt vmcnt(0) lgkmcnt(0)
s_barrier
v_lshrrev_b32 v13, 3, v1
v_lshlrev_b32 v13, 8, v13
v_add_u32 v13, 0x1000, v13
v_and_b32 v14, 7, v1
v_lshrrev_b32 v15, 4, v1
v_xor_b32 v15, v15, v14
v_lshl_add_u32 v32, v15, 4, v13
v_xor_b32 v33, 64, v32
v_add_u32 v33, 0x400, v33
v_add_u32 v34, 0x800, v32
v_add_u32 v35, 0x800, v33
v_bfe_u32 v15, v1, 4, 1
v_lshlrev_b32 v15, 2, v15
v_xor_b32 v15, v15, v14
v_lshl_add_u32 v36, v15, 4, v13
v_add_u32 v37, 0x400, v36
v_add_u32 v38, 0x800, v36
v_add_u32 v39, 0xc00, v36
s_mov_b32 s83, 0
s_mov_b32 s54, 15
.Lk1_fb_loop:
s_mov_b32 m0, s34
s_nop 0
buffer_load_dwordx4 v32, s[40:43], s83 offen offset:0 lds
s_mov_b32 m0, s34
s_nop 0
buffer_load_dwordx4 v33, s[40:43], s83 offen offset:1024 lds
s_mov_b32 m0, s34
s_nop 0
buffer_load_dwordx4 v34, s[40:43], s83 offen offset:2048 lds
s_mov_b32 m0, s34
s_nop 0
buffer_load_dwordx4 v35, s[40:43], s83 offen offset:3072 lds
s_add_u32 s83, s83, 0x2000
s_waitcnt vmcnt(0)
s_barrier
ds_read_b128 a[128:131], v48 offset:0
ds_read_b128 a[132:135], v49 offset:0
ds_read_b128 a[136:139], v50 offset:0
ds_read_b128 a[140:143], v51 offset:0
ds_read_b128 a[144:147], v48 offset:4096
ds_read_b128 a[148:151], v49 offset:4096
ds_read_b128 a[152:155], v50 offset:4096
ds_read_b128 a[156:159], v51 offset:4096
s_waitcnt lgkmcnt(0)
s_barrier
v_mfma_f32_32x32x16_bf16 v[128:143], a[128:131], v[64:67], 0
v_mfma_f32_32x32x16_bf16 v[144:159], a[128:131], v[96:99], 0
v_mfma_f32_32x32x16_bf16 v[128:143], a[132:135], v[68:71], v[128:143]
v_mfma_f32_32x32x16_bf16 v[144:159], a[132:135], v[100:103], v[144:159]
v_mfma_f32_32x32x16_bf16 v[128:143], a[136:139], v[72:75], v[128:143]
v_mfma_f32_32x32x16_bf16 v[144:159], a[136:139], v[104:107], v[144:159]
v_mfma_f32_32x32x16_bf16 v[128:143], a[140:143], v[76:79], v[128:143]
v_mfma_f32_32x32x16_bf16 v[144:159], a[140:143], v[108:111], v[144:159]
v_mfma_f32_32x32x16_bf16 v[128:143], a[144:147], v[80:83], v[128:143]
v_mfma_f32_32x32x16_bf16 v[144:159], a[144:147], v[112:115], v[144:159]
v_mfma_f32_32x32x16_bf16 v[128:143], a[148:151], v[84:87], v[128:143]
v_mfma_f32_32x32x16_bf16 v[144:159], a[148:151], v[116:119], v[144:159]
v_mfma_f32_32x32x16_bf16 v[128:143], a[152:155], v[88:91], v[128:143]
v_mfma_f32_32x32x16_bf16 v[144:159], a[152:155], v[120:123], v[144:159]
v_mfma_f32_32x32x16_bf16 v[128:143], a[156:159], v[92:95], v[128:143]
v_mfma_f32_32x32x16_bf16 v[144:159], a[156:159], v[124:127], v[144:159]
s_nop 15
s_nop 3
v_max3_f32 v62, v128, v129, v62
v_max3_f32 v63, v144, v145, v63
v_max3_f32 v62, v130, v131, v62
v_max3_f32 v63, v146, v147, v63
v_max3_f32 v62, v132, v133, v62
v_max3_f32 v63, v148, v149, v63
v_max3_f32 v62, v134, v135, v62
v_max3_f32 v63, v150, v151, v63
v_max3_f32 v62, v136, v137, v62
v_max3_f32 v63, v152, v153, v63
v_max3_f32 v62, v138, v139, v62
v_max3_f32 v63, v154, v155, v63
v_max3_f32 v62, v140, v141, v62
v_max3_f32 v63, v156, v157, v63
v_max3_f32 v62, v142, v143, v62
v_max3_f32 v63, v158, v159, v63
s_nop 7
s_add_u32 s54, s54, -1
s_cbranch_scc1 .Lk1_fb_loop
s_mov_b32 s73, s97
s_mov_b32 s85, 1
s_mov_b32 s83, s16
s_mov_b32 s84, s16
s_add_u32 m0, s34, 0x0
s_nop 0
buffer_load_dwordx4 v32, s[40:43], s83 offen offset:0 lds
s_add_u32 m0, s34, 0x0
s_nop 0
buffer_load_dwordx4 v33, s[40:43], s83 offen offset:1024 lds
s_add_u32 m0, s34, 0x0
s_nop 0
buffer_load_dwordx4 v34, s[40:43], s83 offen offset:2048 lds
s_add_u32 m0, s34, 0x0
s_nop 0
buffer_load_dwordx4 v35, s[40:43], s83 offen offset:3072 lds
s_add_u32 s83, s83, 0x2000
s_and_b32 s83, s83, 0x1ffff
s_add_u32 m0, s34, 0x4000
s_nop 0
buffer_load_dwordx4 v32, s[40:43], s83 offen offset:0 lds
s_add_u32 m0, s34, 0x4000
s_nop 0
buffer_load_dwordx4 v33, s[40:43], s83 offen offset:1024 lds
s_add_u32 m0, s34, 0x4000
s_nop 0
buffer_load_dwordx4 v34, s[40:43], s83 offen offset:2048 lds
s_add_u32 m0, s34, 0x4000
s_nop 0
buffer_load_dwordx4 v35, s[40:43], s83 offen offset:3072 lds
s_add_u32 s83, s83, 0x2000
s_and_b32 s83, s83, 0x1ffff
s_add_u32 m0, s35, 0x0
s_nop 0
buffer_load_dwordx4 v32, s[40:43], s83 offen offset:0 lds
)ASM"
R"ASM(
s_add_u32 m0, s35, 0x0
s_nop 0
buffer_load_dwordx4 v33, s[40:43], s83 offen offset:1024 lds
s_add_u32 m0, s35, 0x0
s_nop 0
buffer_load_dwordx4 v34, s[40:43], s83 offen offset:2048 lds
s_add_u32 m0, s35, 0x0
s_nop 0
buffer_load_dwordx4 v35, s[40:43], s83 offen offset:3072 lds
s_add_u32 s83, s83, 0x2000
s_and_b32 s83, s83, 0x1ffff
s_add_u32 m0, s35, 0x4000
s_nop 0
buffer_load_dwordx4 v32, s[40:43], s83 offen offset:0 lds
s_add_u32 m0, s35, 0x4000
s_nop 0
buffer_load_dwordx4 v33, s[40:43], s83 offen offset:1024 lds
s_add_u32 m0, s35, 0x4000
s_nop 0
buffer_load_dwordx4 v34, s[40:43], s83 offen offset:2048 lds
s_add_u32 m0, s35, 0x4000
s_nop 0
buffer_load_dwordx4 v35, s[40:43], s83 offen offset:3072 lds
s_add_u32 s83, s83, 0x2000
s_and_b32 s83, s83, 0x1ffff
s_add_u32 m0, s87, 0x0
s_nop 0
buffer_load_dwordx4 v36, s[44:47], s84 offen offset:0 lds
s_add_u32 m0, s87, 0x0
s_nop 0
buffer_load_dwordx4 v37, s[44:47], s84 offen offset:1024 lds
s_add_u32 m0, s87, 0x0
s_nop 0
buffer_load_dwordx4 v38, s[44:47], s84 offen offset:2048 lds
s_add_u32 m0, s87, 0x0
s_nop 0
buffer_load_dwordx4 v39, s[44:47], s84 offen offset:3072 lds
s_add_u32 s84, s84, 0x2000
s_and_b32 s84, s84, 0x1ffff
s_add_u32 m0, s87, 0x4000
s_nop 0
buffer_load_dwordx4 v36, s[44:47], s84 offen offset:0 lds
s_add_u32 m0, s87, 0x4000
s_nop 0
buffer_load_dwordx4 v37, s[44:47], s84 offen offset:1024 lds
s_add_u32 m0, s87, 0x4000
s_nop 0
buffer_load_dwordx4 v38, s[44:47], s84 offen offset:2048 lds
s_add_u32 m0, s87, 0x4000
s_nop 0
buffer_load_dwordx4 v39, s[44:47], s84 offen offset:3072 lds
s_add_u32 s84, s84, 0x2000
s_and_b32 s84, s84, 0x1ffff
s_waitcnt vmcnt(0)
s_branch .Lk1_attentry
.Ls1_entry:
s_lshl_b32 s29, s52, 1
s_cmp_ge_u32 s52, 2
s_addc_u32 s29, s29, 0
v_bfe_u32 v13, v1, 3, 2
v_and_b32 v38, 7, v1
v_lshrrev_b32 v39, 5, v1
v_lshlrev_b32 v19, 4, v13
v_lshl_add_u32 v19, v39, 7, v19
s_mov_b32 s58, 0x480
v_lshrrev_b32 v53, 1, v19
v_mad_u32_u24 v53, v38, s58, v53
v_lshlrev_b32 v50, 4, v38
v_lshl_add_u32 v50, v13, 8, v50
v_lshl_add_u32 v50, v39, 12, v50
s_lshl_b32 s58, s29, 8
v_add_u32 v54, s58, v19
v_add_u32 v55, 0x100, v54
v_add_u32 v19, 0x400, v19
s_lshl_b32 s58, s29, 7
v_add_u32 v51, s58, v53
v_add_u32 v52, 0x80, v51
s_cmp_eq_u32 s52, 0
s_cselect_b32 s58, 0, 0x40000000
s_add_u32 s58, s58, 0x200
v_add_u32 v53, s58, v53
s_lshl_b32 s58, s29, 13
v_add_u32 v48, s58, v50
v_add_u32 v49, 0x2000, v48
s_cmp_eq_u32 s52, 0
s_cselect_b32 s58, 0, 0x40000000
s_add_u32 s58, s58, 0x8000
v_add_u32 v50, s58, v50
s_lshl_b32 s58, s52, 2
s_add_u32 s58, s58, 0x26000
v_lshl_add_u32 v253, v38, 4, s58
v_lshrrev_b32 v39, 5, v1
v_lshl_add_u32 v253, v39, 7, v253
s_mov_b32 s58, 0x26000
v_lshl_add_u32 v254, v38, 4, s58
v_mov_b32 v251, 0x3ae38e39
s_mov_b32 s58, 0
s_add_u32 s16, s16, s58
s_addc_u32 s17, s17, 0
s_and_b32 s17, s17, 0xffff
s_sub_u32 s18, 0x3600, s58
s_mov_b32 s19, 0x20000
s_lshl_b32 s58, s57, 3
s_add_u32 s58, s58, s56
s_mul_i32 s58, s58, 0x2400
s_add_u32 s24, s24, s58
s_addc_u32 s25, s25, 0
s_and_b32 s25, s25, 0xffff
s_sub_u32 s26, 0x240000, s58
s_mov_b32 s27, 0x20000
s_mul_i32 s58, s57, 0x12000
s_lshr_b32 s59, s56, 1
s_lshl_b32 s59, s59, 10
s_add_u32 s58, s58, s59
s_and_b32 s59, s56, 1
s_lshl_b32 s59, s59, 7
s_add_u32 s58, s58, s59
s_add_u32 s20, s20, s58
s_addc_u32 s21, s21, 0
s_and_b32 s21, s21, 0xffff
s_sub_u32 s22, 0x1200000, s58
s_mov_b32 s23, 0x20000
s_add_u32 s30, s30, s58
s_addc_u32 s31, s31, 0
s_sub_u32 s29, 0x240000, s58
v_mov_b32 v255, 0
buffer_load_dwordx2 v[76:77], v51, s[24:27], 0 offen
buffer_load_dwordx2 v[78:79], v51, s[24:27], 0 offen offset:32
buffer_load_dwordx2 v[80:81], v52, s[24:27], 0 offen
buffer_load_dwordx2 v[82:83], v52, s[24:27], 0 offen offset:32
buffer_load_dwordx2 v[84:85], v53, s[24:27], 0 offen
buffer_load_dwordx2 v[86:87], v53, s[24:27], 0 offen offset:32
s_mov_b32 s28, 0x1200
buffer_load_dwordx4 v[176:179], v54, s[16:19], s28 offen sc1
buffer_load_dwordx4 v[180:183], v54, s[16:19], s28 offen offset:64 sc1
buffer_load_dwordx4 v[184:187], v55, s[16:19], s28 offen sc1
buffer_load_dwordx4 v[188:191], v55, s[16:19], s28 offen offset:64 sc1
buffer_load_dwordx4 v[192:195], v19, s[16:19], s28 offen sc1
buffer_load_dwordx4 v[196:199], v19, s[16:19], s28 offen offset:64 sc1
s_mov_b32 s54, 0x20000
.Ls1_a_p:
buffer_load_dword v13, v255, s[12:15], 0 offen sc1
s_waitcnt vmcnt(0)
v_readfirstlane_b32 s58, v13
s_nop 3
s_sub_u32 s58, s58, s4
s_cmp_ge_u32 s58, 32
s_cbranch_scc1 .Ls1_a_d
s_sleep 1
s_add_u32 s54, s54, -1
s_cbranch_scc1 .Ls1_a_p
.Ls1_a_d:
buffer_atomic_add v72, v73, s[12:15], 0 offen offset:256 sc1
s_mov_b32 s28, 0
buffer_load_dwordx4 v[200:203], v48, s[20:23], s28 offen sc1
s_add_u32 s28, s28, 0x240000
buffer_load_dwordx4 v[204:207], v48, s[20:23], s28 offen sc1
s_add_u32 s28, s28, 0x240000
buffer_load_dwordx4 v[208:211], v48, s[20:23], s28 offen sc1
s_add_u32 s28, s28, 0x240000
buffer_load_dwordx4 v[212:215], v48, s[20:23], s28 offen sc1
s_add_u32 s28, s28, 0x240000
buffer_load_dwordx4 v[216:219], v48, s[20:23], s28 offen sc1
s_add_u32 s28, s28, 0x240000
buffer_load_dwordx4 v[220:223], v48, s[20:23], s28 offen sc1
s_add_u32 s28, s28, 0x240000
buffer_load_dwordx4 v[224:227], v48, s[20:23], s28 offen sc1
s_add_u32 s28, s28, 0x240000
buffer_load_dwordx4 v[228:231], v48, s[20:23], s28 offen sc1
s_mov_b32 s28, 0
buffer_load_dwordx4 v[232:235], v50, s[20:23], s28 offen sc1
s_add_u32 s28, s28, 0x240000
buffer_load_dwordx4 v[236:239], v50, s[20:23], s28 offen sc1
s_add_u32 s28, s28, 0x240000
buffer_load_dwordx4 v[88:91], v50, s[20:23], s28 offen sc1
s_add_u32 s28, s28, 0x240000
buffer_load_dwordx4 v[92:95], v50, s[20:23], s28 offen sc1
s_add_u32 s28, s28, 0x240000
buffer_load_dwordx4 v[96:99], v50, s[20:23], s28 offen sc1
s_add_u32 s28, s28, 0x240000
buffer_load_dwordx4 v[100:103], v50, s[20:23], s28 offen sc1
s_add_u32 s28, s28, 0x240000
buffer_load_dwordx4 v[40:43], v50, s[20:23], s28 offen sc1
s_add_u32 s28, s28, 0x240000
buffer_load_dwordx4 v[44:47], v50, s[20:23], s28 offen sc1
.Ls1_join:
s_mov_b32 s24, s30
s_and_b32 s25, s31, 0xffff
s_mov_b32 s26, s29
s_branch .Lk2_pre
.Ls1_part2:
s_waitcnt vmcnt(7)
v_lshlrev_b32 v104, 16, v232
v_and_b32 v105, 0xffff0000, v232
v_lshlrev_b32 v106, 16, v233
v_and_b32 v107, 0xffff0000, v233
v_lshlrev_b32 v108, 16, v234
v_and_b32 v109, 0xffff0000, v234
v_lshlrev_b32 v110, 16, v235
v_and_b32 v111, 0xffff0000, v235
)ASM"
R"ASM(
s_waitcnt vmcnt(6)
v_lshlrev_b32 v39, 16, v236
v_and_b32 v248, 0xffff0000, v236
v_add_f32 v104, v104, v39
v_add_f32 v105, v105, v248
v_lshlrev_b32 v13, 16, v237
v_and_b32 v38, 0xffff0000, v237
v_add_f32 v106, v106, v13
v_add_f32 v107, v107, v38
v_lshlrev_b32 v39, 16, v238
v_and_b32 v248, 0xffff0000, v238
v_add_f32 v108, v108, v39
v_add_f32 v109, v109, v248
v_lshlrev_b32 v13, 16, v239
v_and_b32 v38, 0xffff0000, v239
v_add_f32 v110, v110, v13
v_add_f32 v111, v111, v38
s_waitcnt vmcnt(5)
v_lshlrev_b32 v39, 16, v88
v_and_b32 v248, 0xffff0000, v88
v_add_f32 v104, v104, v39
v_add_f32 v105, v105, v248
v_lshlrev_b32 v13, 16, v89
v_and_b32 v38, 0xffff0000, v89
v_add_f32 v106, v106, v13
v_add_f32 v107, v107, v38
v_lshlrev_b32 v39, 16, v90
v_and_b32 v248, 0xffff0000, v90
v_add_f32 v108, v108, v39
v_add_f32 v109, v109, v248
v_lshlrev_b32 v13, 16, v91
v_and_b32 v38, 0xffff0000, v91
v_add_f32 v110, v110, v13
v_add_f32 v111, v111, v38
s_waitcnt vmcnt(4)
v_lshlrev_b32 v39, 16, v92
v_and_b32 v248, 0xffff0000, v92
v_add_f32 v104, v104, v39
v_add_f32 v105, v105, v248
v_lshlrev_b32 v13, 16, v93
v_and_b32 v38, 0xffff0000, v93
v_add_f32 v106, v106, v13
v_add_f32 v107, v107, v38
v_lshlrev_b32 v39, 16, v94
v_and_b32 v248, 0xffff0000, v94
v_add_f32 v108, v108, v39
v_add_f32 v109, v109, v248
v_lshlrev_b32 v13, 16, v95
v_and_b32 v38, 0xffff0000, v95
v_add_f32 v110, v110, v13
v_add_f32 v111, v111, v38
s_waitcnt vmcnt(3)
v_lshlrev_b32 v39, 16, v96
v_and_b32 v248, 0xffff0000, v96
v_add_f32 v104, v104, v39
v_add_f32 v105, v105, v248
v_lshlrev_b32 v13, 16, v97
v_and_b32 v38, 0xffff0000, v97
v_add_f32 v106, v106, v13
v_add_f32 v107, v107, v38
v_lshlrev_b32 v39, 16, v98
v_and_b32 v248, 0xffff0000, v98
v_add_f32 v108, v108, v39
v_add_f32 v109, v109, v248
v_lshlrev_b32 v13, 16, v99
v_and_b32 v38, 0xffff0000, v99
v_add_f32 v110, v110, v13
v_add_f32 v111, v111, v38
s_waitcnt vmcnt(2)
v_lshlrev_b32 v39, 16, v100
v_and_b32 v248, 0xffff0000, v100
v_add_f32 v104, v104, v39
v_add_f32 v105, v105, v248
v_lshlrev_b32 v13, 16, v101
v_and_b32 v38, 0xffff0000, v101
v_add_f32 v106, v106, v13
v_add_f32 v107, v107, v38
v_lshlrev_b32 v39, 16, v102
v_and_b32 v248, 0xffff0000, v102
v_add_f32 v108, v108, v39
v_add_f32 v109, v109, v248
v_lshlrev_b32 v13, 16, v103
v_and_b32 v38, 0xffff0000, v103
v_add_f32 v110, v110, v13
v_add_f32 v111, v111, v38
s_waitcnt vmcnt(1)
v_lshlrev_b32 v39, 16, v40
v_and_b32 v248, 0xffff0000, v40
v_add_f32 v104, v104, v39
v_add_f32 v105, v105, v248
v_lshlrev_b32 v13, 16, v41
v_and_b32 v38, 0xffff0000, v41
v_add_f32 v106, v106, v13
v_add_f32 v107, v107, v38
v_lshlrev_b32 v39, 16, v42
v_and_b32 v248, 0xffff0000, v42
v_add_f32 v108, v108, v39
v_add_f32 v109, v109, v248
v_lshlrev_b32 v13, 16, v43
v_and_b32 v38, 0xffff0000, v43
v_add_f32 v110, v110, v13
v_add_f32 v111, v111, v38
s_waitcnt vmcnt(0)
v_lshlrev_b32 v39, 16, v44
v_and_b32 v248, 0xffff0000, v44
v_add_f32 v104, v104, v39
v_add_f32 v105, v105, v248
v_lshlrev_b32 v13, 16, v45
v_and_b32 v38, 0xffff0000, v45
v_add_f32 v106, v106, v13
v_add_f32 v107, v107, v38
v_lshlrev_b32 v39, 16, v46
v_and_b32 v248, 0xffff0000, v46
v_add_f32 v108, v108, v39
v_add_f32 v109, v109, v248
v_lshlrev_b32 v13, 16, v47
v_and_b32 v38, 0xffff0000, v47
v_add_f32 v110, v110, v13
v_add_f32 v111, v111, v38
v_lshlrev_b32 v88, 16, v200
v_and_b32 v89, 0xffff0000, v200
v_lshlrev_b32 v90, 16, v201
v_and_b32 v91, 0xffff0000, v201
v_lshlrev_b32 v92, 16, v202
v_and_b32 v93, 0xffff0000, v202
v_lshlrev_b32 v94, 16, v203
v_and_b32 v95, 0xffff0000, v203
v_lshlrev_b32 v39, 16, v204
v_and_b32 v248, 0xffff0000, v204
v_add_f32 v88, v88, v39
v_add_f32 v89, v89, v248
v_lshlrev_b32 v13, 16, v205
v_and_b32 v38, 0xffff0000, v205
v_add_f32 v90, v90, v13
v_add_f32 v91, v91, v38
v_lshlrev_b32 v39, 16, v206
v_and_b32 v248, 0xffff0000, v206
v_add_f32 v92, v92, v39
v_add_f32 v93, v93, v248
v_lshlrev_b32 v13, 16, v207
v_and_b32 v38, 0xffff0000, v207
v_add_f32 v94, v94, v13
v_add_f32 v95, v95, v38
v_lshlrev_b32 v39, 16, v208
v_and_b32 v248, 0xffff0000, v208
v_add_f32 v88, v88, v39
v_add_f32 v89, v89, v248
v_lshlrev_b32 v13, 16, v209
v_and_b32 v38, 0xffff0000, v209
v_add_f32 v90, v90, v13
v_add_f32 v91, v91, v38
v_lshlrev_b32 v39, 16, v210
v_and_b32 v248, 0xffff0000, v210
v_add_f32 v92, v92, v39
v_add_f32 v93, v93, v248
v_lshlrev_b32 v13, 16, v211
v_and_b32 v38, 0xffff0000, v211
v_add_f32 v94, v94, v13
v_add_f32 v95, v95, v38
v_lshlrev_b32 v39, 16, v212
v_and_b32 v248, 0xffff0000, v212
v_add_f32 v88, v88, v39
v_add_f32 v89, v89, v248
v_lshlrev_b32 v13, 16, v213
v_and_b32 v38, 0xffff0000, v213
v_add_f32 v90, v90, v13
v_add_f32 v91, v91, v38
v_lshlrev_b32 v39, 16, v214
v_and_b32 v248, 0xffff0000, v214
v_add_f32 v92, v92, v39
v_add_f32 v93, v93, v248
v_lshlrev_b32 v13, 16, v215
v_and_b32 v38, 0xffff0000, v215
v_add_f32 v94, v94, v13
v_add_f32 v95, v95, v38
v_lshlrev_b32 v39, 16, v216
v_and_b32 v248, 0xffff0000, v216
v_add_f32 v88, v88, v39
v_add_f32 v89, v89, v248
v_lshlrev_b32 v13, 16, v217
v_and_b32 v38, 0xffff0000, v217
v_add_f32 v90, v90, v13
v_add_f32 v91, v91, v38
v_lshlrev_b32 v39, 16, v218
v_and_b32 v248, 0xffff0000, v218
v_add_f32 v92, v92, v39
v_add_f32 v93, v93, v248
v_lshlrev_b32 v13, 16, v219
v_and_b32 v38, 0xffff0000, v219
v_add_f32 v94, v94, v13
v_add_f32 v95, v95, v38
v_lshlrev_b32 v39, 16, v220
v_and_b32 v248, 0xffff0000, v220
v_add_f32 v88, v88, v39
v_add_f32 v89, v89, v248
v_lshlrev_b32 v13, 16, v221
v_and_b32 v38, 0xffff0000, v221
v_add_f32 v90, v90, v13
v_add_f32 v91, v91, v38
v_lshlrev_b32 v39, 16, v222
)ASM"
R"ASM(
v_and_b32 v248, 0xffff0000, v222
v_add_f32 v92, v92, v39
v_add_f32 v93, v93, v248
v_lshlrev_b32 v13, 16, v223
v_and_b32 v38, 0xffff0000, v223
v_add_f32 v94, v94, v13
v_add_f32 v95, v95, v38
v_lshlrev_b32 v39, 16, v224
v_and_b32 v248, 0xffff0000, v224
v_add_f32 v88, v88, v39
v_add_f32 v89, v89, v248
v_lshlrev_b32 v13, 16, v225
v_and_b32 v38, 0xffff0000, v225
v_add_f32 v90, v90, v13
v_add_f32 v91, v91, v38
v_lshlrev_b32 v39, 16, v226
v_and_b32 v248, 0xffff0000, v226
v_add_f32 v92, v92, v39
v_add_f32 v93, v93, v248
v_lshlrev_b32 v13, 16, v227
v_and_b32 v38, 0xffff0000, v227
v_add_f32 v94, v94, v13
v_add_f32 v95, v95, v38
v_lshlrev_b32 v39, 16, v228
v_and_b32 v248, 0xffff0000, v228
v_add_f32 v88, v88, v39
v_add_f32 v89, v89, v248
v_lshlrev_b32 v13, 16, v229
v_and_b32 v38, 0xffff0000, v229
v_add_f32 v90, v90, v13
v_add_f32 v91, v91, v38
v_lshlrev_b32 v39, 16, v230
v_and_b32 v248, 0xffff0000, v230
v_add_f32 v92, v92, v39
v_add_f32 v93, v93, v248
v_lshlrev_b32 v13, 16, v231
v_and_b32 v38, 0xffff0000, v231
v_add_f32 v94, v94, v13
v_add_f32 v95, v95, v38
s_mov_b32 s54, 0x20000
.Ls1_b_p:
buffer_load_dword v13, v255, s[12:15], 0 offen offset:256 sc1
s_waitcnt vmcnt(0)
v_readfirstlane_b32 s58, v13
s_nop 3
s_sub_u32 s58, s58, s5
s_cmp_ge_u32 s58, 32
s_cbranch_scc1 .Ls1_b_d
s_sleep 1
s_add_u32 s54, s54, -1
s_cbranch_scc1 .Ls1_b_p
.Ls1_b_d:
s_mov_b32 s28, 0
buffer_load_dwordx4 v[200:203], v49, s[20:23], s28 offen sc1
s_add_u32 s28, s28, 0x240000
buffer_load_dwordx4 v[204:207], v49, s[20:23], s28 offen sc1
s_add_u32 s28, s28, 0x240000
buffer_load_dwordx4 v[208:211], v49, s[20:23], s28 offen sc1
s_add_u32 s28, s28, 0x240000
buffer_load_dwordx4 v[212:215], v49, s[20:23], s28 offen sc1
s_add_u32 s28, s28, 0x240000
buffer_load_dwordx4 v[216:219], v49, s[20:23], s28 offen sc1
s_add_u32 s28, s28, 0x240000
buffer_load_dwordx4 v[220:223], v49, s[20:23], s28 offen sc1
s_add_u32 s28, s28, 0x240000
buffer_load_dwordx4 v[224:227], v49, s[20:23], s28 offen sc1
s_add_u32 s28, s28, 0x240000
buffer_load_dwordx4 v[228:231], v49, s[20:23], s28 offen sc1
s_mov_b32 s28, 0x2400
buffer_load_dwordx4 v[112:115], v54, s[16:19], s28 offen sc1
buffer_load_dwordx4 v[116:119], v54, s[16:19], s28 offen offset:64 sc1
buffer_load_dwordx4 v[120:123], v55, s[16:19], s28 offen sc1
buffer_load_dwordx4 v[124:127], v55, s[16:19], s28 offen offset:64 sc1
buffer_load_dwordx4 v[128:131], v19, s[16:19], s28 offen sc1
buffer_load_dwordx4 v[132:135], v19, s[16:19], s28 offen offset:64 sc1
s_mov_b32 s28, 0x1b00
buffer_load_dwordx4 v[136:139], v54, s[16:19], s28 offen sc1
buffer_load_dwordx4 v[140:143], v54, s[16:19], s28 offen offset:64 sc1
buffer_load_dwordx4 v[144:147], v55, s[16:19], s28 offen sc1
buffer_load_dwordx4 v[148:151], v55, s[16:19], s28 offen offset:64 sc1
buffer_load_dwordx4 v[152:155], v19, s[16:19], s28 offen sc1
buffer_load_dwordx4 v[156:159], v19, s[16:19], s28 offen offset:64 sc1
v_lshlrev_b32 v39, 16, v76
v_and_b32 v248, 0xffff0000, v76
v_fmac_f32 v39, v176, v88
v_fmac_f32 v248, v177, v89
v_cvt_pk_bf16_f32 v56, v39, v248
v_lshlrev_b32 v88, 16, v56
v_and_b32 v89, 0xffff0000, v56
v_lshlrev_b32 v13, 16, v77
v_and_b32 v38, 0xffff0000, v77
v_fmac_f32 v13, v178, v90
v_fmac_f32 v38, v179, v91
v_cvt_pk_bf16_f32 v57, v13, v38
v_lshlrev_b32 v90, 16, v57
v_and_b32 v91, 0xffff0000, v57
v_lshlrev_b32 v39, 16, v78
v_and_b32 v248, 0xffff0000, v78
v_fmac_f32 v39, v180, v92
v_fmac_f32 v248, v181, v93
v_cvt_pk_bf16_f32 v58, v39, v248
v_lshlrev_b32 v92, 16, v58
v_and_b32 v93, 0xffff0000, v58
v_lshlrev_b32 v13, 16, v79
v_and_b32 v38, 0xffff0000, v79
v_fmac_f32 v13, v182, v94
v_fmac_f32 v38, v183, v95
v_cvt_pk_bf16_f32 v59, v13, v38
v_lshlrev_b32 v94, 16, v59
v_and_b32 v95, 0xffff0000, v59
v_lshlrev_b32 v39, 16, v84
v_and_b32 v248, 0xffff0000, v84
v_fmac_f32 v39, v192, v104
v_fmac_f32 v248, v193, v105
v_cvt_pk_bf16_f32 v34, v39, v248
v_lshlrev_b32 v104, 16, v34
v_and_b32 v105, 0xffff0000, v34
v_lshlrev_b32 v13, 16, v85
v_and_b32 v38, 0xffff0000, v85
v_fmac_f32 v13, v194, v106
v_fmac_f32 v38, v195, v107
v_cvt_pk_bf16_f32 v35, v13, v38
v_lshlrev_b32 v106, 16, v35
v_and_b32 v107, 0xffff0000, v35
v_lshlrev_b32 v39, 16, v86
v_and_b32 v248, 0xffff0000, v86
v_fmac_f32 v39, v196, v108
v_fmac_f32 v248, v197, v109
v_cvt_pk_bf16_f32 v36, v39, v248
v_lshlrev_b32 v108, 16, v36
v_and_b32 v109, 0xffff0000, v36
v_lshlrev_b32 v13, 16, v87
v_and_b32 v38, 0xffff0000, v87
v_fmac_f32 v13, v198, v110
v_fmac_f32 v38, v199, v111
v_cvt_pk_bf16_f32 v37, v13, v38
v_lshlrev_b32 v110, 16, v37
v_and_b32 v111, 0xffff0000, v37
s_waitcnt vmcnt(19)
v_lshlrev_b32 v96, 16, v200
v_and_b32 v97, 0xffff0000, v200
v_lshlrev_b32 v98, 16, v201
v_and_b32 v99, 0xffff0000, v201
v_lshlrev_b32 v100, 16, v202
v_and_b32 v101, 0xffff0000, v202
v_lshlrev_b32 v102, 16, v203
v_and_b32 v103, 0xffff0000, v203
s_waitcnt vmcnt(18)
v_lshlrev_b32 v39, 16, v204
v_and_b32 v248, 0xffff0000, v204
v_add_f32 v96, v96, v39
v_add_f32 v97, v97, v248
v_lshlrev_b32 v13, 16, v205
v_and_b32 v38, 0xffff0000, v205
v_add_f32 v98, v98, v13
v_add_f32 v99, v99, v38
v_lshlrev_b32 v39, 16, v206
v_and_b32 v248, 0xffff0000, v206
v_add_f32 v100, v100, v39
v_add_f32 v101, v101, v248
v_lshlrev_b32 v13, 16, v207
v_and_b32 v38, 0xffff0000, v207
v_add_f32 v102, v102, v13
v_add_f32 v103, v103, v38
s_waitcnt vmcnt(17)
v_lshlrev_b32 v39, 16, v208
v_and_b32 v248, 0xffff0000, v208
v_add_f32 v96, v96, v39
v_add_f32 v97, v97, v248
v_lshlrev_b32 v13, 16, v209
v_and_b32 v38, 0xffff0000, v209
v_add_f32 v98, v98, v13
v_add_f32 v99, v99, v38
v_lshlrev_b32 v39, 16, v210
v_and_b32 v248, 0xffff0000, v210
v_add_f32 v100, v100, v39
v_add_f32 v101, v101, v248
v_lshlrev_b32 v13, 16, v211
v_and_b32 v38, 0xffff0000, v211
v_add_f32 v102, v102, v13
v_add_f32 v103, v103, v38
s_waitcnt vmcnt(16)
v_lshlrev_b32 v39, 16, v212
v_and_b32 v248, 0xffff0000, v212
v_add_f32 v96, v96, v39
v_add_f32 v97, v97, v248
v_lshlrev_b32 v13, 16, v213
v_and_b32 v38, 0xffff0000, v213
v_add_f32 v98, v98, v13
v_add_f32 v99, v99, v38
v_lshlrev_b32 v39, 16, v214
v_and_b32 v248, 0xffff0000, v214
v_add_f32 v100, v100, v39
v_add_f32 v101, v101, v248
v_lshlrev_b32 v13, 16, v215
v_and_b32 v38, 0xffff0000, v215
v_add_f32 v102, v102, v13
v_add_f32 v103, v103, v38
s_waitcnt vmcnt(15)
v_lshlrev_b32 v39, 16, v216
)ASM"
R"ASM(
v_and_b32 v248, 0xffff0000, v216
v_add_f32 v96, v96, v39
v_add_f32 v97, v97, v248
v_lshlrev_b32 v13, 16, v217
v_and_b32 v38, 0xffff0000, v217
v_add_f32 v98, v98, v13
v_add_f32 v99, v99, v38
v_lshlrev_b32 v39, 16, v218
v_and_b32 v248, 0xffff0000, v218
v_add_f32 v100, v100, v39
v_add_f32 v101, v101, v248
v_lshlrev_b32 v13, 16, v219
v_and_b32 v38, 0xffff0000, v219
v_add_f32 v102, v102, v13
v_add_f32 v103, v103, v38
s_waitcnt vmcnt(14)
v_lshlrev_b32 v39, 16, v220
v_and_b32 v248, 0xffff0000, v220
v_add_f32 v96, v96, v39
v_add_f32 v97, v97, v248
v_lshlrev_b32 v13, 16, v221
v_and_b32 v38, 0xffff0000, v221
v_add_f32 v98, v98, v13
v_add_f32 v99, v99, v38
v_lshlrev_b32 v39, 16, v222
v_and_b32 v248, 0xffff0000, v222
v_add_f32 v100, v100, v39
v_add_f32 v101, v101, v248
v_lshlrev_b32 v13, 16, v223
v_and_b32 v38, 0xffff0000, v223
v_add_f32 v102, v102, v13
v_add_f32 v103, v103, v38
s_waitcnt vmcnt(13)
v_lshlrev_b32 v39, 16, v224
v_and_b32 v248, 0xffff0000, v224
v_add_f32 v96, v96, v39
v_add_f32 v97, v97, v248
v_lshlrev_b32 v13, 16, v225
v_and_b32 v38, 0xffff0000, v225
v_add_f32 v98, v98, v13
v_add_f32 v99, v99, v38
v_lshlrev_b32 v39, 16, v226
v_and_b32 v248, 0xffff0000, v226
v_add_f32 v100, v100, v39
v_add_f32 v101, v101, v248
v_lshlrev_b32 v13, 16, v227
v_and_b32 v38, 0xffff0000, v227
v_add_f32 v102, v102, v13
v_add_f32 v103, v103, v38
s_waitcnt vmcnt(12)
v_lshlrev_b32 v39, 16, v228
v_and_b32 v248, 0xffff0000, v228
v_add_f32 v96, v96, v39
v_add_f32 v97, v97, v248
v_lshlrev_b32 v13, 16, v229
v_and_b32 v38, 0xffff0000, v229
v_add_f32 v98, v98, v13
v_add_f32 v99, v99, v38
v_lshlrev_b32 v39, 16, v230
v_and_b32 v248, 0xffff0000, v230
v_add_f32 v100, v100, v39
v_add_f32 v101, v101, v248
v_lshlrev_b32 v13, 16, v231
v_and_b32 v38, 0xffff0000, v231
v_add_f32 v102, v102, v13
v_add_f32 v103, v103, v38
v_lshlrev_b32 v39, 16, v80
v_and_b32 v248, 0xffff0000, v80
v_fmac_f32 v39, v184, v96
v_fmac_f32 v248, v185, v97
v_cvt_pk_bf16_f32 v60, v39, v248
v_lshlrev_b32 v96, 16, v60
v_and_b32 v97, 0xffff0000, v60
v_lshlrev_b32 v13, 16, v81
v_and_b32 v38, 0xffff0000, v81
v_fmac_f32 v13, v186, v98
v_fmac_f32 v38, v187, v99
v_cvt_pk_bf16_f32 v61, v13, v38
v_lshlrev_b32 v98, 16, v61
v_and_b32 v99, 0xffff0000, v61
v_lshlrev_b32 v39, 16, v82
v_and_b32 v248, 0xffff0000, v82
v_fmac_f32 v39, v188, v100
v_fmac_f32 v248, v189, v101
v_cvt_pk_bf16_f32 v62, v39, v248
v_lshlrev_b32 v100, 16, v62
v_and_b32 v101, 0xffff0000, v62
v_lshlrev_b32 v13, 16, v83
v_and_b32 v38, 0xffff0000, v83
v_fmac_f32 v13, v190, v102
v_fmac_f32 v38, v191, v103
v_cvt_pk_bf16_f32 v63, v13, v38
v_lshlrev_b32 v102, 16, v63
v_and_b32 v103, 0xffff0000, v63
v_add_f32 v13, v88, v89
v_mul_f32 v39, v88, v88
v_mul_f32 v250, v89, v89
v_add_f32 v249, v90, v91
v_fmac_f32 v39, v90, v90
v_fmac_f32 v250, v91, v91
v_fmac_f32 v39, v92, v92
v_fmac_f32 v250, v93, v93
v_add_f32 v13, v13, v92
v_add_f32 v249, v249, v93
v_fmac_f32 v39, v94, v94
v_fmac_f32 v250, v95, v95
v_add_f32 v13, v13, v94
v_add_f32 v249, v249, v95
v_fmac_f32 v39, v96, v96
v_fmac_f32 v250, v97, v97
v_add_f32 v13, v13, v96
v_add_f32 v249, v249, v97
v_fmac_f32 v39, v98, v98
v_fmac_f32 v250, v99, v99
v_add_f32 v13, v13, v98
v_add_f32 v249, v249, v99
v_fmac_f32 v39, v100, v100
v_fmac_f32 v250, v101, v101
v_add_f32 v13, v13, v100
v_add_f32 v249, v249, v101
v_fmac_f32 v39, v102, v102
v_fmac_f32 v250, v103, v103
v_add_f32 v13, v13, v102
v_add_f32 v249, v249, v103
v_fmac_f32 v39, v104, v104
v_fmac_f32 v250, v105, v105
v_add_f32 v13, v13, v104
v_add_f32 v249, v249, v105
v_fmac_f32 v39, v106, v106
v_fmac_f32 v250, v107, v107
v_add_f32 v13, v13, v106
v_add_f32 v249, v249, v107
v_fmac_f32 v39, v108, v108
v_fmac_f32 v250, v109, v109
v_add_f32 v13, v13, v108
v_add_f32 v249, v249, v109
v_fmac_f32 v39, v110, v110
v_fmac_f32 v250, v111, v111
v_add_f32 v13, v13, v110
v_add_f32 v249, v249, v111
v_add_f32 v13, v13, v249
v_add_f32 v38, v39, v250
s_nop 1
v_permlane32_swap_b32 v13, v38
v_add_f32 v13, v13, v38
s_nop 1
v_add_f32_dpp v13, v13, v13 row_ror:8 row_mask:0xf bank_mask:0xf
v_mov_b32 v38, v13
s_nop 1
v_permlane16_swap_b32 v13, v38
v_add_f32 v13, v13, v38
ds_write_b32 v253, v13 offset:0
s_waitcnt lgkmcnt(0)
s_barrier
ds_read_b128 v[240:243], v254 offset:0
ds_read_b128 v[244:247], v254 offset:128
s_waitcnt lgkmcnt(1)
v_add_f32 v13, v240, v241
v_add_f32 v38, v242, v243
v_add_f32 v13, v13, v38
v_mul_f32 v249, 0x3ae38e39, v13
s_waitcnt lgkmcnt(0)
v_add_f32 v13, v244, v245
v_add_f32 v38, v246, v247
v_add_f32 v13, v13, v38
v_mul_f32 v38, v249, v249
v_fma_f32 v13, v251, v13, -v38
v_max_f32 v13, 0, v13
v_add_f32 v13, 0x3727c5ac, v13
v_rsq_f32 v250, v13
v_sub_f32 v88, v88, v249
s_waitcnt vmcnt(11)
v_mul_f32 v112, v112, v250
s_waitcnt vmcnt(5)
v_fma_f32 v88, v88, v112, v136
v_sub_f32 v89, v89, v249
v_mul_f32 v113, v113, v250
v_fma_f32 v89, v89, v113, v137
v_sub_f32 v90, v90, v249
v_mul_f32 v114, v114, v250
v_fma_f32 v90, v90, v114, v138
v_sub_f32 v91, v91, v249
v_mul_f32 v115, v115, v250
v_fma_f32 v91, v91, v115, v139
v_sub_f32 v92, v92, v249
v_mul_f32 v116, v116, v250
s_waitcnt vmcnt(4)
v_fma_f32 v92, v92, v116, v140
v_sub_f32 v93, v93, v249
v_mul_f32 v117, v117, v250
v_fma_f32 v93, v93, v117, v141
v_sub_f32 v94, v94, v249
v_mul_f32 v118, v118, v250
v_fma_f32 v94, v94, v118, v142
v_sub_f32 v95, v95, v249
v_mul_f32 v119, v119, v250
v_fma_f32 v95, v95, v119, v143
v_cvt_pk_bf16_f32 v76, v88, v89
v_cvt_pk_bf16_f32 v77, v90, v91
v_cvt_pk_bf16_f32 v78, v92, v93
)ASM"
R"ASM(
v_cvt_pk_bf16_f32 v79, v94, v95
buffer_store_dwordx4 v[76:79], v48, s[24:27], 0 offen sc1
v_sub_f32 v96, v96, v249
v_mul_f32 v120, v120, v250
s_waitcnt vmcnt(4)
v_fma_f32 v96, v96, v120, v144
v_sub_f32 v97, v97, v249
v_mul_f32 v121, v121, v250
v_fma_f32 v97, v97, v121, v145
v_sub_f32 v98, v98, v249
v_mul_f32 v122, v122, v250
v_fma_f32 v98, v98, v122, v146
v_sub_f32 v99, v99, v249
v_mul_f32 v123, v123, v250
v_fma_f32 v99, v99, v123, v147
v_sub_f32 v100, v100, v249
v_mul_f32 v124, v124, v250
s_waitcnt vmcnt(3)
v_fma_f32 v100, v100, v124, v148
v_sub_f32 v101, v101, v249
v_mul_f32 v125, v125, v250
v_fma_f32 v101, v101, v125, v149
v_sub_f32 v102, v102, v249
v_mul_f32 v126, v126, v250
v_fma_f32 v102, v102, v126, v150
v_sub_f32 v103, v103, v249
v_mul_f32 v127, v127, v250
v_fma_f32 v103, v103, v127, v151
v_cvt_pk_bf16_f32 v80, v96, v97
v_cvt_pk_bf16_f32 v81, v98, v99
v_cvt_pk_bf16_f32 v82, v100, v101
v_cvt_pk_bf16_f32 v83, v102, v103
buffer_store_dwordx4 v[80:83], v49, s[24:27], 0 offen sc1
v_sub_f32 v104, v104, v249
v_mul_f32 v128, v128, v250
s_waitcnt vmcnt(3)
v_fma_f32 v104, v104, v128, v152
v_sub_f32 v105, v105, v249
v_mul_f32 v129, v129, v250
v_fma_f32 v105, v105, v129, v153
v_sub_f32 v106, v106, v249
v_mul_f32 v130, v130, v250
v_fma_f32 v106, v106, v130, v154
v_sub_f32 v107, v107, v249
v_mul_f32 v131, v131, v250
v_fma_f32 v107, v107, v131, v155
v_sub_f32 v108, v108, v249
v_mul_f32 v132, v132, v250
s_waitcnt vmcnt(2)
v_fma_f32 v108, v108, v132, v156
v_sub_f32 v109, v109, v249
v_mul_f32 v133, v133, v250
v_fma_f32 v109, v109, v133, v157
v_sub_f32 v110, v110, v249
v_mul_f32 v134, v134, v250
v_fma_f32 v110, v110, v134, v158
v_sub_f32 v111, v111, v249
v_mul_f32 v135, v135, v250
v_fma_f32 v111, v111, v135, v159
v_cvt_pk_bf16_f32 v84, v104, v105
v_cvt_pk_bf16_f32 v85, v106, v107
v_cvt_pk_bf16_f32 v86, v108, v109
v_cvt_pk_bf16_f32 v87, v110, v111
buffer_store_dwordx4 v[84:87], v50, s[24:27], 0 offen sc1
s_waitcnt vmcnt(0)
buffer_atomic_add v72, v73, s[12:15], 0 offen offset:512 sc1
s_branch .Lk2_main
.Lk2_pre:
s_load_dwordx2 s[44:45], %0, 0x60
s_load_dwordx2 s[84:85], %0, 0x40
s_load_dwordx2 s[36:37], %0, 0x48
s_load_dwordx2 s[40:41], %0, 0x50
s_load_dwordx2 s[48:49], %0, 0x68
s_mul_i32 s58, s57, 0x1c72
s_lshr_b32 s58, s58, 16
s_mul_i32 s58, s58, 9
s_sub_u32 s80, s57, s58
s_lshl_b32 s82, s52, 11
s_add_u32 s83, s82, 0x12000
s_lshl_b32 s55, s80, 13
s_add_u32 s81, s55, s82
s_mov_b32 s79, s55
s_mov_b32 s98, s81
s_lshl_b32 s53, s52, 14
s_add_u32 s53, s53, 0x16000
s_add_u32 s68, s60, 0
s_add_u32 s72, s68, 0x510000
s_mul_i32 s58, s80, 0x90000
s_add_u32 s64, s68, s58
s_add_u32 s69, s60, 0x80
s_add_u32 s73, s69, 0x510000
s_mul_i32 s58, s80, 0x90000
s_add_u32 s65, s69, s58
s_add_u32 s70, s60, 0
s_add_u32 s74, s70, 0xa2000
s_mul_i32 s58, s80, 0x12000
s_add_u32 s66, s70, s58
s_add_u32 s71, s60, 0x80
s_add_u32 s75, s71, 0xa2000
s_mul_i32 s58, s80, 0x12000
s_add_u32 s67, s71, s58
s_mov_b32 s34, s64
s_mov_b32 s35, s66
s_sub_u32 s59, 0x200, s60
v_bfe_u32 v11, v1, 4, 2
v_lshlrev_b32 v11, 1, v11
v_and_b32 v12, 7, v1
v_xor_b32 v11, v11, v12
v_lshlrev_b32 v11, 4, v11
s_mov_b32 s58, 0x2400
v_lshrrev_b32 v12, 3, v1
v_mad_u32_u24 v32, v12, s58, v11
s_mul_i32 s58, s52, 0x12000
s_add_u32 s58, s58, s59
v_add_u32 v33, s58, v32
v_add_u32 v20, 0x1000, v32
v_add_u32 v21, 0x12c00, v32
v_add_u32 v22, 0x24800, v32
v_add_u32 v23, 0x36400, v32
v_add_u32 v24, 0x49000, v32
v_add_u32 v25, 0x5ac00, v32
v_add_u32 v26, 0x6c800, v32
v_add_u32 v27, 0x7e400, v32
v_add_u32 v28, 0x1000, v33
v_add_u32 v29, 0x49000, v33
v_bfe_u32 v11, v1, 4, 2
v_lshlrev_b32 v11, 1, v11
v_and_b32 v12, 7, v1
v_xor_b32 v11, v11, v12
v_lshlrev_b32 v11, 4, v11
s_mov_b32 s58, 0x480
v_lshrrev_b32 v12, 3, v1
v_mad_u32_u24 v32, v12, s58, v11
s_mul_i32 s58, s52, 0x2400
s_add_u32 s58, s58, s59
v_add_u32 v33, s58, v32
v_add_u32 v64, 0x1000, v32
v_add_u32 v65, 0x3000, v32
v_add_u32 v66, 0x5000, v32
v_add_u32 v67, 0x7000, v32
v_add_u32 v68, 0xa000, v32
v_add_u32 v69, 0xc000, v32
v_add_u32 v70, 0xe000, v32
v_add_u32 v71, 0x10000, v32
v_add_u32 v30, 0x1000, v33
v_add_u32 v31, 0xa000, v33
v_and_b32 v13, 3, v1
v_lshlrev_b32 v13, 3, v13
v_lshrrev_b32 v11, 2, v1
v_lshl_add_u32 v13, v11, 7, v13
v_bfe_u32 v11, v1, 3, 2
v_lshl_add_u32 v13, v11, 5, v13
s_and_b32 s58, s52, 2
s_lshl_b32 s58, s58, 5
v_xor_b32 v7, s58, v13
v_add_u32 v7, 0x12000, v7
v_xor_b32 v8, 32, v7
v_add_u32 v3, s53, v13
v_xor_b32 v4, 32, v3
v_xor_b32 v5, 64, v3
v_xor_b32 v6, 0x60, v3
s_and_b32 s58, s52, 1
s_lshl_b32 s58, s58, 11
v_add_u32 v14, s58, v2
v_xor_b32 v15, 0x800, v14
v_add_u32 v9, s55, v14
v_add_u32 v10, s55, v15
s_lshl_b32 s76, s60, 6
s_add_u32 s77, s76, 0x1000
s_add_u32 s78, s76, 0x2000
s_add_u32 s96, s76, 0x3000
s_lshr_b32 s58, s52, 1
s_add_u32 s58, s58, 8
s_lshl_b32 s97, s58, 12
v_add_u32 v16, s76, v14
v_add_u32 v17, s76, v15
v_add_u32 v18, s97, v14
s_waitcnt lgkmcnt(0)
s_mul_i32 s58, s57, 0x12000
s_add_u32 s44, s44, s58
s_addc_u32 s45, s45, 0
s_and_b32 s45, s45, 0xffff
s_sub_u32 s46, 0x240000, s58
s_mov_b32 s47, 0x20000
s_mul_i32 s58, s56, 0x480
s_add_u32 s84, s84, s58
s_addc_u32 s85, s85, 0
s_sub_u32 s84, s84, 0x1000
s_subb_u32 s85, s85, 0
s_and_b32 s85, s85, 0xffff
s_sub_u32 s86, 0x511000, s58
s_mov_b32 s87, 0x20000
s_add_u32 s36, s36, s58
s_addc_u32 s37, s37, 0
s_sub_u32 s36, s36, 0x1000
s_subb_u32 s37, s37, 0
s_and_b32 s37, s37, 0xffff
s_sub_u32 s38, 0x511000, s58
s_mov_b32 s39, 0x20000
s_mul_i32 s58, s56, 0xa2000
)ASM"
R"ASM(
s_add_u32 s40, s40, s58
s_addc_u32 s41, s41, 0
s_sub_u32 s40, s40, 0x1000
s_subb_u32 s41, s41, 0
s_and_b32 s41, s41, 0xffff
s_sub_u32 s42, 0x511000, s58
s_mov_b32 s43, 0x20000
s_lshl_b32 s58, s56, 5
s_add_u32 s58, s58, s57
s_mul_i32 s58, s58, 0x12000
s_add_u32 s48, s48, s58
s_addc_u32 s49, s49, 0
s_and_b32 s49, s49, 0xffff
s_sub_u32 s50, 0x1200000, s58
s_mov_b32 s51, 0x20000
s_branch .Ls1_part2
.Lk2_main:
s_cmp_eq_u32 s52, 3
s_cbranch_scc1 .Lk2_f3
s_add_u32 m0, s53, 0
s_nop 0
buffer_load_dwordx4 v20, s[84:87], s64 offen lds
buffer_load_dwordx4 v21, s[84:87], s64 offen offset:1024 lds
buffer_load_dwordx4 v22, s[84:87], s64 offen offset:2048 lds
buffer_load_dwordx4 v23, s[84:87], s64 offen offset:3072 lds
s_add_u32 m0, s53, 0x1000
s_nop 0
buffer_load_dwordx4 v20, s[36:39], s64 offen lds
buffer_load_dwordx4 v21, s[36:39], s64 offen offset:1024 lds
buffer_load_dwordx4 v22, s[36:39], s64 offen offset:2048 lds
buffer_load_dwordx4 v23, s[36:39], s64 offen offset:3072 lds
s_add_u32 m0, s53, 0x2000
s_nop 0
buffer_load_dwordx4 v24, s[84:87], s64 offen lds
buffer_load_dwordx4 v25, s[84:87], s64 offen offset:1024 lds
buffer_load_dwordx4 v26, s[84:87], s64 offen offset:2048 lds
buffer_load_dwordx4 v27, s[84:87], s64 offen offset:3072 lds
s_add_u32 m0, s53, 0x3000
s_nop 0
buffer_load_dwordx4 v24, s[36:39], s64 offen lds
buffer_load_dwordx4 v25, s[36:39], s64 offen offset:1024 lds
buffer_load_dwordx4 v26, s[36:39], s64 offen offset:2048 lds
buffer_load_dwordx4 v27, s[36:39], s64 offen offset:3072 lds
s_cmp_lg_u32 s52, 2
s_cbranch_scc1 .Lk2_f3
s_add_u32 s53, s53, 0x4000
s_add_u32 s64, s64, 0x100
s_add_u32 m0, s53, 0
s_nop 0
buffer_load_dwordx4 v20, s[84:87], s64 offen lds
buffer_load_dwordx4 v21, s[84:87], s64 offen offset:1024 lds
buffer_load_dwordx4 v22, s[84:87], s64 offen offset:2048 lds
buffer_load_dwordx4 v23, s[84:87], s64 offen offset:3072 lds
s_add_u32 m0, s53, 0x1000
s_nop 0
buffer_load_dwordx4 v20, s[36:39], s64 offen lds
buffer_load_dwordx4 v21, s[36:39], s64 offen offset:1024 lds
buffer_load_dwordx4 v22, s[36:39], s64 offen offset:2048 lds
buffer_load_dwordx4 v23, s[36:39], s64 offen offset:3072 lds
s_add_u32 m0, s53, 0x2000
s_nop 0
buffer_load_dwordx4 v24, s[84:87], s64 offen lds
buffer_load_dwordx4 v25, s[84:87], s64 offen offset:1024 lds
buffer_load_dwordx4 v26, s[84:87], s64 offen offset:2048 lds
buffer_load_dwordx4 v27, s[84:87], s64 offen offset:3072 lds
s_add_u32 m0, s53, 0x3000
s_nop 0
buffer_load_dwordx4 v24, s[36:39], s64 offen lds
buffer_load_dwordx4 v25, s[36:39], s64 offen offset:1024 lds
buffer_load_dwordx4 v26, s[36:39], s64 offen offset:2048 lds
buffer_load_dwordx4 v27, s[36:39], s64 offen offset:3072 lds
s_sub_u32 s53, s53, 0x4000
s_sub_u32 s64, s64, 0x100
.Lk2_f3:
s_add_u32 m0, s61, 0x12000
s_nop 0
buffer_load_dwordx4 v28, s[84:87], s34 offen lds
s_add_u32 m0, s61, 0x13000
s_nop 0
buffer_load_dwordx4 v28, s[36:39], s34 offen lds
s_cmp_lg_u32 s52, 3
s_cbranch_scc1 .Lk2_pollc_skip
s_mov_b32 s54, 0x20000
.Lk2_pollc_p:
buffer_load_dword v11, v255, s[12:15], 0 offen offset:512 sc1
s_waitcnt vmcnt(0)
v_readfirstlane_b32 s58, v11
s_nop 3
s_sub_u32 s58, s58, s6
s_cmp_ge_u32 s58, 32
s_cbranch_scc1 .Lk2_pollc_d
s_sleep 1
s_add_u32 s54, s54, -1
s_cbranch_scc1 .Lk2_pollc_p
.Lk2_pollc_d:
.Lk2_pollc_skip:
s_barrier
s_mov_b32 m0, s81
s_add_u32 s58, s81, 0x2000
buffer_load_dwordx4 v2, s[44:47], s81 offen sc1 lds
buffer_load_dwordx4 v2, s[44:47], s81 offen offset:1024 sc1 lds
s_cmp_eq_u32 s58, s83
s_cselect_b32 s81, s82, s58
s_mov_b32 m0, s81
s_add_u32 s58, s81, 0x2000
buffer_load_dwordx4 v2, s[44:47], s81 offen sc1 lds
buffer_load_dwordx4 v2, s[44:47], s81 offen offset:1024 sc1 lds
s_cmp_eq_u32 s58, s83
s_cselect_b32 s81, s82, s58
s_mov_b32 m0, s81
s_add_u32 s58, s81, 0x2000
buffer_load_dwordx4 v2, s[44:47], s81 offen sc1 lds
buffer_load_dwordx4 v2, s[44:47], s81 offen offset:1024 sc1 lds
s_cmp_eq_u32 s58, s83
s_cselect_b32 s81, s82, s58
s_waitcnt vmcnt(2)
s_barrier
ds_read_b64_tr_b16 a[160:161], v3 offset:0
ds_read_b64_tr_b16 a[162:163], v3 offset:2048
ds_read_b64_tr_b16 a[164:165], v4 offset:0
ds_read_b64_tr_b16 a[166:167], v4 offset:2048
ds_read_b64_tr_b16 a[168:169], v5 offset:0
ds_read_b64_tr_b16 a[170:171], v5 offset:2048
ds_read_b64_tr_b16 a[172:173], v6 offset:0
ds_read_b64_tr_b16 a[174:175], v6 offset:2048
ds_read_b128 a[192:195], v9 offset:0
ds_read_b128 a[196:199], v9 offset:1024
ds_read_b128 a[200:203], v10 offset:0
ds_read_b128 a[204:207], v10 offset:1024
s_waitcnt lgkmcnt(0)
v_mfma_f32_16x16x32_bf16 a[0:3], a[160:163], a[192:195], 0
s_add_u32 m0, s61, 0x14000
ds_read_b64_tr_b16 a[176:177], v3 offset:4096
v_mfma_f32_16x16x32_bf16 a[4:7], a[160:163], a[196:199], 0
buffer_load_dwordx4 v29, s[84:87], s34 offen lds
v_mfma_f32_16x16x32_bf16 a[8:11], a[160:163], a[200:203], 0
s_add_u32 m0, s61, 0x15000
ds_read_b64_tr_b16 a[178:179], v3 offset:6144
v_mfma_f32_16x16x32_bf16 a[12:15], a[160:163], a[204:207], 0
ds_read_b128 a[208:211], v9 offset:4096
ds_read_b128 a[212:215], v9 offset:5120
v_mfma_f32_16x16x32_bf16 a[28:31], a[164:167], a[204:207], 0
buffer_load_dwordx4 v29, s[36:39], s34 offen lds
s_cmp_eq_u32 s81, s98
s_cbranch_scc1 .Lk2_h2skip0
s_mov_b32 m0, s81
s_add_u32 s58, s81, 0x2000
buffer_load_dwordx4 v2, s[44:47], s81 offen sc1 lds
buffer_load_dwordx4 v2, s[44:47], s81 offen offset:1024 sc1 lds
s_cmp_eq_u32 s58, s83
s_cselect_b32 s81, s82, s58
.Lk2_h2skip0:
v_mfma_f32_16x16x32_bf16 a[24:27], a[164:167], a[200:203], 0
ds_read_b64_tr_b16 a[180:181], v4 offset:4096
ds_read_b128 a[216:219], v10 offset:4096
v_mfma_f32_16x16x32_bf16 a[20:23], a[164:167], a[196:199], 0
ds_read_b64_tr_b16 a[182:183], v4 offset:6144
ds_read_b128 a[220:223], v10 offset:5120
v_mfma_f32_16x16x32_bf16 a[16:19], a[164:167], a[192:195], 0
v_mfma_f32_16x16x32_bf16 a[32:35], a[168:171], a[192:195], 0
s_add_u32 s64, s64, 0x90000
ds_read_b64_tr_b16 a[184:185], v5 offset:4096
v_mfma_f32_16x16x32_bf16 a[36:39], a[168:171], a[196:199], 0
s_cmp_eq_u32 s64, s72
s_cselect_b32 s64, s68, s64
v_mfma_f32_16x16x32_bf16 a[40:43], a[168:171], a[200:203], 0
s_add_u32 m0, s53, 0
ds_read_b64_tr_b16 a[186:187], v5 offset:6144
v_mfma_f32_16x16x32_bf16 a[44:47], a[168:171], a[204:207], 0
buffer_load_dwordx4 v20, s[84:87], s64 offen lds
v_mfma_f32_16x16x32_bf16 a[60:63], a[172:175], a[204:207], 0
ds_read_b64_tr_b16 a[188:189], v6 offset:4096
ds_read_b64_tr_b16 a[190:191], v6 offset:6144
v_mfma_f32_16x16x32_bf16 a[56:59], a[172:175], a[200:203], 0
v_mfma_f32_16x16x32_bf16 a[52:55], a[172:175], a[196:199], 0
buffer_load_dwordx4 v21, s[84:87], s64 offen offset:1024 lds
v_mfma_f32_16x16x32_bf16 a[48:51], a[172:175], a[192:195], 0
s_waitcnt lgkmcnt(5)
v_mfma_f32_16x16x32_bf16 a[64:67], a[176:179], a[192:195], 0
ds_read_b64_tr_b16 a[160:161], v7 offset:0
ds_read_b64_tr_b16 a[162:163], v7 offset:2048
v_mfma_f32_16x16x32_bf16 a[68:71], a[176:179], a[196:199], 0
buffer_load_dwordx4 v22, s[84:87], s64 offen offset:2048 lds
v_mfma_f32_16x16x32_bf16 a[72:75], a[176:179], a[200:203], 0
v_mfma_f32_16x16x32_bf16 a[76:79], a[176:179], a[204:207], 0
v_mfma_f32_16x16x32_bf16 a[92:95], a[180:183], a[204:207], 0
buffer_load_dwordx4 v23, s[84:87], s64 offen offset:3072 lds
v_mfma_f32_16x16x32_bf16 a[88:91], a[180:183], a[200:203], 0
ds_read_b64_tr_b16 a[164:165], v8 offset:0
ds_read_b64_tr_b16 a[166:167], v8 offset:2048
v_mfma_f32_16x16x32_bf16 a[84:87], a[180:183], a[196:199], 0
v_mfma_f32_16x16x32_bf16 a[80:83], a[180:183], a[192:195], 0
s_waitcnt lgkmcnt(4)
v_mfma_f32_16x16x32_bf16 a[96:99], a[184:187], a[192:195], 0
s_add_u32 m0, s53, 0x1000
ds_read_b64_tr_b16 a[168:169], v7 offset:4096
v_mfma_f32_16x16x32_bf16 a[100:103], a[184:187], a[196:199], 0
buffer_load_dwordx4 v20, s[36:39], s64 offen lds
v_mfma_f32_16x16x32_bf16 a[104:107], a[184:187], a[200:203], 0
ds_read_b64_tr_b16 a[170:171], v7 offset:6144
)ASM"
R"ASM(
v_mfma_f32_16x16x32_bf16 a[108:111], a[184:187], a[204:207], 0
v_mfma_f32_16x16x32_bf16 a[124:127], a[188:191], a[204:207], 0
buffer_load_dwordx4 v21, s[36:39], s64 offen offset:1024 lds
v_mfma_f32_16x16x32_bf16 a[120:123], a[188:191], a[200:203], 0
ds_read_b64_tr_b16 a[172:173], v8 offset:4096
ds_read_b64_tr_b16 a[174:175], v8 offset:6144
v_mfma_f32_16x16x32_bf16 a[116:119], a[188:191], a[196:199], 0
v_mfma_f32_16x16x32_bf16 a[112:115], a[188:191], a[192:195], 0
s_waitcnt lgkmcnt(4)
v_mfma_f32_16x16x32_bf16 a[128:131], a[160:163], a[192:195], 0
buffer_load_dwordx4 v22, s[36:39], s64 offen offset:2048 lds
v_mfma_f32_16x16x32_bf16 a[132:135], a[160:163], a[196:199], 0
ds_read_b64_tr_b16 a[176:177], v3 offset:8192
v_mfma_f32_16x16x32_bf16 a[140:143], a[164:167], a[196:199], 0
ds_read_b64_tr_b16 a[178:179], v3 offset:10240
ds_read_b64_tr_b16 a[180:181], v4 offset:8192
v_mfma_f32_16x16x32_bf16 a[136:139], a[164:167], a[192:195], 0
ds_read_b64_tr_b16 a[182:183], v4 offset:10240
s_waitcnt lgkmcnt(4)
v_mfma_f32_16x16x32_bf16 a[144:147], a[168:171], a[192:195], 0
buffer_load_dwordx4 v23, s[36:39], s64 offen offset:3072 lds
v_mfma_f32_16x16x32_bf16 a[148:151], a[168:171], a[196:199], 0
ds_read_b64_tr_b16 a[184:185], v5 offset:8192
ds_read_b64_tr_b16 a[186:187], v5 offset:10240
v_mfma_f32_16x16x32_bf16 a[156:159], a[172:175], a[196:199], 0
ds_read_b64_tr_b16 a[188:189], v6 offset:8192
ds_read_b64_tr_b16 a[190:191], v6 offset:10240
v_mfma_f32_16x16x32_bf16 a[152:155], a[172:175], a[192:195], 0
s_waitcnt lgkmcnt(4)
s_waitcnt vmcnt(10)
s_barrier
v_mfma_f32_16x16x32_bf16 a[0:3], a[176:179], a[208:211], a[0:3]
s_add_u32 s34, s34, 0x90000
ds_read_b64_tr_b16 a[160:161], v3 offset:12288
v_mfma_f32_16x16x32_bf16 a[4:7], a[176:179], a[212:215], a[4:7]
s_cmp_eq_u32 s34, s72
s_cselect_b32 s34, s68, s34
v_mfma_f32_16x16x32_bf16 a[8:11], a[176:179], a[216:219], a[8:11]
s_add_u32 m0, s61, 0x12000
ds_read_b64_tr_b16 a[162:163], v3 offset:14336
v_mfma_f32_16x16x32_bf16 a[12:15], a[176:179], a[220:223], a[12:15]
buffer_load_dwordx4 v28, s[84:87], s34 offen lds
v_mfma_f32_16x16x32_bf16 a[28:31], a[180:183], a[220:223], a[28:31]
s_add_u32 m0, s61, 0x13000
ds_read_b64_tr_b16 a[164:165], v4 offset:12288
v_mfma_f32_16x16x32_bf16 a[24:27], a[180:183], a[216:219], a[24:27]
ds_read_b64_tr_b16 a[166:167], v4 offset:14336
s_add_u32 s55, s55, 0x2000
v_mfma_f32_16x16x32_bf16 a[20:23], a[180:183], a[212:215], a[20:23]
buffer_load_dwordx4 v28, s[36:39], s34 offen lds
v_mfma_f32_16x16x32_bf16 a[16:19], a[180:183], a[208:211], a[16:19]
s_waitcnt lgkmcnt(4)
v_mfma_f32_16x16x32_bf16 a[32:35], a[184:187], a[208:211], a[32:35]
s_add_u32 m0, s53, 0x2000
ds_read_b64_tr_b16 a[168:169], v5 offset:12288
v_mfma_f32_16x16x32_bf16 a[36:39], a[184:187], a[212:215], a[36:39]
buffer_load_dwordx4 v24, s[84:87], s64 offen lds
v_mfma_f32_16x16x32_bf16 a[40:43], a[184:187], a[216:219], a[40:43]
ds_read_b64_tr_b16 a[170:171], v5 offset:14336
v_mfma_f32_16x16x32_bf16 a[44:47], a[184:187], a[220:223], a[44:47]
s_cmp_eq_u32 s55, 0x12000
s_cselect_b32 s55, 0, s55
v_mfma_f32_16x16x32_bf16 a[60:63], a[188:191], a[220:223], a[60:63]
buffer_load_dwordx4 v25, s[84:87], s64 offen offset:1024 lds
v_mfma_f32_16x16x32_bf16 a[56:59], a[188:191], a[216:219], a[56:59]
ds_read_b64_tr_b16 a[172:173], v6 offset:12288
v_add_u32 v9, s55, v14
v_mfma_f32_16x16x32_bf16 a[52:55], a[188:191], a[212:215], a[52:55]
ds_read_b64_tr_b16 a[174:175], v6 offset:14336
v_add_u32 v10, s55, v15
v_mfma_f32_16x16x32_bf16 a[48:51], a[188:191], a[208:211], a[48:51]
ds_read_b128 a[192:195], v9 offset:0
s_waitcnt lgkmcnt(5)
v_mfma_f32_16x16x32_bf16 a[64:67], a[160:163], a[208:211], a[64:67]
buffer_load_dwordx4 v26, s[84:87], s64 offen offset:2048 lds
v_mfma_f32_16x16x32_bf16 a[68:71], a[160:163], a[212:215], a[68:71]
ds_read_b64_tr_b16 a[176:177], v7 offset:8192
ds_read_b128 a[196:199], v9 offset:1024
v_mfma_f32_16x16x32_bf16 a[72:75], a[160:163], a[216:219], a[72:75]
ds_read_b64_tr_b16 a[178:179], v7 offset:10240
ds_read_b128 a[200:203], v10 offset:0
v_mfma_f32_16x16x32_bf16 a[76:79], a[160:163], a[220:223], a[76:79]
buffer_load_dwordx4 v27, s[84:87], s64 offen offset:3072 lds
v_mfma_f32_16x16x32_bf16 a[92:95], a[164:167], a[220:223], a[92:95]
ds_read_b64_tr_b16 a[180:181], v8 offset:8192
ds_read_b128 a[204:207], v10 offset:1024
v_mfma_f32_16x16x32_bf16 a[88:91], a[164:167], a[216:219], a[88:91]
ds_read_b64_tr_b16 a[182:183], v8 offset:10240
v_mfma_f32_16x16x32_bf16 a[84:87], a[164:167], a[212:215], a[84:87]
v_mfma_f32_16x16x32_bf16 a[80:83], a[164:167], a[208:211], a[80:83]
s_waitcnt lgkmcnt(8)
v_mfma_f32_16x16x32_bf16 a[96:99], a[168:171], a[208:211], a[96:99]
s_add_u32 m0, s53, 0x3000
ds_read_b64_tr_b16 a[184:185], v7 offset:12288
v_mfma_f32_16x16x32_bf16 a[100:103], a[168:171], a[212:215], a[100:103]
buffer_load_dwordx4 v24, s[36:39], s64 offen lds
v_mfma_f32_16x16x32_bf16 a[104:107], a[168:171], a[216:219], a[104:107]
ds_read_b64_tr_b16 a[186:187], v7 offset:14336
v_mfma_f32_16x16x32_bf16 a[108:111], a[168:171], a[220:223], a[108:111]
v_mfma_f32_16x16x32_bf16 a[124:127], a[172:175], a[220:223], a[124:127]
buffer_load_dwordx4 v25, s[36:39], s64 offen offset:1024 lds
v_mfma_f32_16x16x32_bf16 a[120:123], a[172:175], a[216:219], a[120:123]
ds_read_b64_tr_b16 a[188:189], v8 offset:12288
ds_read_b64_tr_b16 a[190:191], v8 offset:14336
v_mfma_f32_16x16x32_bf16 a[116:119], a[172:175], a[212:215], a[116:119]
v_mfma_f32_16x16x32_bf16 a[112:115], a[172:175], a[208:211], a[112:115]
s_waitcnt lgkmcnt(4)
v_mfma_f32_16x16x32_bf16 a[128:131], a[176:179], a[208:211], a[128:131]
buffer_load_dwordx4 v26, s[36:39], s64 offen offset:2048 lds
v_mfma_f32_16x16x32_bf16 a[132:135], a[176:179], a[212:215], a[132:135]
s_waitcnt vmcnt(13)
ds_read_b64_tr_b16 a[160:161], v3 offset:0
v_mfma_f32_16x16x32_bf16 a[140:143], a[180:183], a[212:215], a[140:143]
ds_read_b64_tr_b16 a[162:163], v3 offset:2048
ds_read_b64_tr_b16 a[164:165], v4 offset:0
v_mfma_f32_16x16x32_bf16 a[136:139], a[180:183], a[208:211], a[136:139]
ds_read_b64_tr_b16 a[166:167], v4 offset:2048
s_waitcnt lgkmcnt(4)
v_mfma_f32_16x16x32_bf16 a[144:147], a[184:187], a[208:211], a[144:147]
buffer_load_dwordx4 v27, s[36:39], s64 offen offset:3072 lds
v_mfma_f32_16x16x32_bf16 a[148:151], a[184:187], a[212:215], a[148:151]
ds_read_b64_tr_b16 a[168:169], v5 offset:0
ds_read_b64_tr_b16 a[170:171], v5 offset:2048
v_mfma_f32_16x16x32_bf16 a[156:159], a[188:191], a[212:215], a[156:159]
ds_read_b64_tr_b16 a[172:173], v6 offset:0
ds_read_b64_tr_b16 a[174:175], v6 offset:2048
v_mfma_f32_16x16x32_bf16 a[152:155], a[188:191], a[208:211], a[152:155]
s_waitcnt lgkmcnt(4)
s_mov_b32 s54, 6
.Lk2_G1:
s_waitcnt vmcnt(8)
s_barrier
v_mfma_f32_16x16x32_bf16 a[0:3], a[160:163], a[192:195], a[0:3]
s_add_u32 m0, s61, 0x14000
ds_read_b64_tr_b16 a[176:177], v3 offset:4096
v_mfma_f32_16x16x32_bf16 a[4:7], a[160:163], a[196:199], a[4:7]
buffer_load_dwordx4 v29, s[84:87], s34 offen lds
v_mfma_f32_16x16x32_bf16 a[8:11], a[160:163], a[200:203], a[8:11]
s_add_u32 m0, s61, 0x15000
ds_read_b64_tr_b16 a[178:179], v3 offset:6144
v_mfma_f32_16x16x32_bf16 a[12:15], a[160:163], a[204:207], a[12:15]
ds_read_b128 a[208:211], v9 offset:4096
ds_read_b128 a[212:215], v9 offset:5120
v_mfma_f32_16x16x32_bf16 a[28:31], a[164:167], a[204:207], a[28:31]
buffer_load_dwordx4 v29, s[36:39], s34 offen lds
s_cmp_eq_u32 s81, s98
s_cbranch_scc1 .Lk2_h2skip
s_mov_b32 m0, s81
s_add_u32 s58, s81, 0x2000
buffer_load_dwordx4 v2, s[44:47], s81 offen sc1 lds
buffer_load_dwordx4 v2, s[44:47], s81 offen offset:1024 sc1 lds
s_cmp_eq_u32 s58, s83
s_cselect_b32 s81, s82, s58
.Lk2_h2skip:
v_mfma_f32_16x16x32_bf16 a[24:27], a[164:167], a[200:203], a[24:27]
ds_read_b64_tr_b16 a[180:181], v4 offset:4096
ds_read_b128 a[216:219], v10 offset:4096
v_mfma_f32_16x16x32_bf16 a[20:23], a[164:167], a[196:199], a[20:23]
ds_read_b64_tr_b16 a[182:183], v4 offset:6144
ds_read_b128 a[220:223], v10 offset:5120
v_mfma_f32_16x16x32_bf16 a[16:19], a[164:167], a[192:195], a[16:19]
s_waitcnt lgkmcnt(8)
v_mfma_f32_16x16x32_bf16 a[32:35], a[168:171], a[192:195], a[32:35]
s_add_u32 s64, s64, 0x90000
ds_read_b64_tr_b16 a[184:185], v5 offset:4096
v_mfma_f32_16x16x32_bf16 a[36:39], a[168:171], a[196:199], a[36:39]
s_cmp_eq_u32 s64, s72
s_cselect_b32 s64, s68, s64
v_mfma_f32_16x16x32_bf16 a[40:43], a[168:171], a[200:203], a[40:43]
s_add_u32 m0, s53, 0
ds_read_b64_tr_b16 a[186:187], v5 offset:6144
v_mfma_f32_16x16x32_bf16 a[44:47], a[168:171], a[204:207], a[44:47]
buffer_load_dwordx4 v20, s[84:87], s64 offen lds
v_mfma_f32_16x16x32_bf16 a[60:63], a[172:175], a[204:207], a[60:63]
ds_read_b64_tr_b16 a[188:189], v6 offset:4096
ds_read_b64_tr_b16 a[190:191], v6 offset:6144
v_mfma_f32_16x16x32_bf16 a[56:59], a[172:175], a[200:203], a[56:59]
v_mfma_f32_16x16x32_bf16 a[52:55], a[172:175], a[196:199], a[52:55]
buffer_load_dwordx4 v21, s[84:87], s64 offen offset:1024 lds
v_mfma_f32_16x16x32_bf16 a[48:51], a[172:175], a[192:195], a[48:51]
s_waitcnt lgkmcnt(5)
v_mfma_f32_16x16x32_bf16 a[64:67], a[176:179], a[192:195], a[64:67]
ds_read_b64_tr_b16 a[160:161], v7 offset:0
ds_read_b64_tr_b16 a[162:163], v7 offset:2048
v_mfma_f32_16x16x32_bf16 a[68:71], a[176:179], a[196:199], a[68:71]
buffer_load_dwordx4 v22, s[84:87], s64 offen offset:2048 lds
v_mfma_f32_16x16x32_bf16 a[72:75], a[176:179], a[200:203], a[72:75]
v_mfma_f32_16x16x32_bf16 a[76:79], a[176:179], a[204:207], a[76:79]
v_mfma_f32_16x16x32_bf16 a[92:95], a[180:183], a[204:207], a[92:95]
buffer_load_dwordx4 v23, s[84:87], s64 offen offset:3072 lds
v_mfma_f32_16x16x32_bf16 a[88:91], a[180:183], a[200:203], a[88:91]
ds_read_b64_tr_b16 a[164:165], v8 offset:0
ds_read_b64_tr_b16 a[166:167], v8 offset:2048
v_mfma_f32_16x16x32_bf16 a[84:87], a[180:183], a[196:199], a[84:87]
v_mfma_f32_16x16x32_bf16 a[80:83], a[180:183], a[192:195], a[80:83]
s_waitcnt lgkmcnt(4)
v_mfma_f32_16x16x32_bf16 a[96:99], a[184:187], a[192:195], a[96:99]
s_add_u32 m0, s53, 0x1000
ds_read_b64_tr_b16 a[168:169], v7 offset:4096
v_mfma_f32_16x16x32_bf16 a[100:103], a[184:187], a[196:199], a[100:103]
)ASM"
R"ASM(
buffer_load_dwordx4 v20, s[36:39], s64 offen lds
v_mfma_f32_16x16x32_bf16 a[104:107], a[184:187], a[200:203], a[104:107]
ds_read_b64_tr_b16 a[170:171], v7 offset:6144
v_mfma_f32_16x16x32_bf16 a[108:111], a[184:187], a[204:207], a[108:111]
v_mfma_f32_16x16x32_bf16 a[124:127], a[188:191], a[204:207], a[124:127]
buffer_load_dwordx4 v21, s[36:39], s64 offen offset:1024 lds
v_mfma_f32_16x16x32_bf16 a[120:123], a[188:191], a[200:203], a[120:123]
ds_read_b64_tr_b16 a[172:173], v8 offset:4096
ds_read_b64_tr_b16 a[174:175], v8 offset:6144
v_mfma_f32_16x16x32_bf16 a[116:119], a[188:191], a[196:199], a[116:119]
v_mfma_f32_16x16x32_bf16 a[112:115], a[188:191], a[192:195], a[112:115]
s_waitcnt lgkmcnt(4)
v_mfma_f32_16x16x32_bf16 a[128:131], a[160:163], a[192:195], a[128:131]
buffer_load_dwordx4 v22, s[36:39], s64 offen offset:2048 lds
v_mfma_f32_16x16x32_bf16 a[132:135], a[160:163], a[196:199], a[132:135]
s_waitcnt vmcnt(13)
ds_read_b64_tr_b16 a[176:177], v3 offset:8192
v_mfma_f32_16x16x32_bf16 a[140:143], a[164:167], a[196:199], a[140:143]
ds_read_b64_tr_b16 a[178:179], v3 offset:10240
ds_read_b64_tr_b16 a[180:181], v4 offset:8192
v_mfma_f32_16x16x32_bf16 a[136:139], a[164:167], a[192:195], a[136:139]
ds_read_b64_tr_b16 a[182:183], v4 offset:10240
s_waitcnt lgkmcnt(4)
v_mfma_f32_16x16x32_bf16 a[144:147], a[168:171], a[192:195], a[144:147]
buffer_load_dwordx4 v23, s[36:39], s64 offen offset:3072 lds
v_mfma_f32_16x16x32_bf16 a[148:151], a[168:171], a[196:199], a[148:151]
ds_read_b64_tr_b16 a[184:185], v5 offset:8192
ds_read_b64_tr_b16 a[186:187], v5 offset:10240
v_mfma_f32_16x16x32_bf16 a[156:159], a[172:175], a[196:199], a[156:159]
ds_read_b64_tr_b16 a[188:189], v6 offset:8192
ds_read_b64_tr_b16 a[190:191], v6 offset:10240
v_mfma_f32_16x16x32_bf16 a[152:155], a[172:175], a[192:195], a[152:155]
s_waitcnt lgkmcnt(4)
s_waitcnt vmcnt(8)
s_barrier
v_mfma_f32_16x16x32_bf16 a[0:3], a[176:179], a[208:211], a[0:3]
s_add_u32 s34, s34, 0x90000
ds_read_b64_tr_b16 a[160:161], v3 offset:12288
v_mfma_f32_16x16x32_bf16 a[4:7], a[176:179], a[212:215], a[4:7]
s_cmp_eq_u32 s34, s72
s_cselect_b32 s34, s68, s34
v_mfma_f32_16x16x32_bf16 a[8:11], a[176:179], a[216:219], a[8:11]
s_add_u32 m0, s61, 0x12000
ds_read_b64_tr_b16 a[162:163], v3 offset:14336
v_mfma_f32_16x16x32_bf16 a[12:15], a[176:179], a[220:223], a[12:15]
buffer_load_dwordx4 v28, s[84:87], s34 offen lds
v_mfma_f32_16x16x32_bf16 a[28:31], a[180:183], a[220:223], a[28:31]
s_add_u32 m0, s61, 0x13000
ds_read_b64_tr_b16 a[164:165], v4 offset:12288
v_mfma_f32_16x16x32_bf16 a[24:27], a[180:183], a[216:219], a[24:27]
ds_read_b64_tr_b16 a[166:167], v4 offset:14336
s_add_u32 s55, s55, 0x2000
v_mfma_f32_16x16x32_bf16 a[20:23], a[180:183], a[212:215], a[20:23]
buffer_load_dwordx4 v28, s[36:39], s34 offen lds
v_mfma_f32_16x16x32_bf16 a[16:19], a[180:183], a[208:211], a[16:19]
s_waitcnt lgkmcnt(4)
v_mfma_f32_16x16x32_bf16 a[32:35], a[184:187], a[208:211], a[32:35]
s_add_u32 m0, s53, 0x2000
ds_read_b64_tr_b16 a[168:169], v5 offset:12288
v_mfma_f32_16x16x32_bf16 a[36:39], a[184:187], a[212:215], a[36:39]
buffer_load_dwordx4 v24, s[84:87], s64 offen lds
v_mfma_f32_16x16x32_bf16 a[40:43], a[184:187], a[216:219], a[40:43]
ds_read_b64_tr_b16 a[170:171], v5 offset:14336
v_mfma_f32_16x16x32_bf16 a[44:47], a[184:187], a[220:223], a[44:47]
s_cmp_eq_u32 s55, 0x12000
s_cselect_b32 s55, 0, s55
v_mfma_f32_16x16x32_bf16 a[60:63], a[188:191], a[220:223], a[60:63]
buffer_load_dwordx4 v25, s[84:87], s64 offen offset:1024 lds
v_mfma_f32_16x16x32_bf16 a[56:59], a[188:191], a[216:219], a[56:59]
ds_read_b64_tr_b16 a[172:173], v6 offset:12288
v_add_u32 v9, s55, v14
v_mfma_f32_16x16x32_bf16 a[52:55], a[188:191], a[212:215], a[52:55]
ds_read_b64_tr_b16 a[174:175], v6 offset:14336
v_add_u32 v10, s55, v15
v_mfma_f32_16x16x32_bf16 a[48:51], a[188:191], a[208:211], a[48:51]
ds_read_b128 a[192:195], v9 offset:0
s_waitcnt lgkmcnt(5)
v_mfma_f32_16x16x32_bf16 a[64:67], a[160:163], a[208:211], a[64:67]
buffer_load_dwordx4 v26, s[84:87], s64 offen offset:2048 lds
v_mfma_f32_16x16x32_bf16 a[68:71], a[160:163], a[212:215], a[68:71]
ds_read_b64_tr_b16 a[176:177], v7 offset:8192
ds_read_b128 a[196:199], v9 offset:1024
v_mfma_f32_16x16x32_bf16 a[72:75], a[160:163], a[216:219], a[72:75]
ds_read_b64_tr_b16 a[178:179], v7 offset:10240
ds_read_b128 a[200:203], v10 offset:0
v_mfma_f32_16x16x32_bf16 a[76:79], a[160:163], a[220:223], a[76:79]
buffer_load_dwordx4 v27, s[84:87], s64 offen offset:3072 lds
v_mfma_f32_16x16x32_bf16 a[92:95], a[164:167], a[220:223], a[92:95]
ds_read_b64_tr_b16 a[180:181], v8 offset:8192
ds_read_b128 a[204:207], v10 offset:1024
v_mfma_f32_16x16x32_bf16 a[88:91], a[164:167], a[216:219], a[88:91]
ds_read_b64_tr_b16 a[182:183], v8 offset:10240
v_mfma_f32_16x16x32_bf16 a[84:87], a[164:167], a[212:215], a[84:87]
v_mfma_f32_16x16x32_bf16 a[80:83], a[164:167], a[208:211], a[80:83]
s_waitcnt lgkmcnt(8)
v_mfma_f32_16x16x32_bf16 a[96:99], a[168:171], a[208:211], a[96:99]
s_add_u32 m0, s53, 0x3000
ds_read_b64_tr_b16 a[184:185], v7 offset:12288
v_mfma_f32_16x16x32_bf16 a[100:103], a[168:171], a[212:215], a[100:103]
buffer_load_dwordx4 v24, s[36:39], s64 offen lds
v_mfma_f32_16x16x32_bf16 a[104:107], a[168:171], a[216:219], a[104:107]
ds_read_b64_tr_b16 a[186:187], v7 offset:14336
v_mfma_f32_16x16x32_bf16 a[108:111], a[168:171], a[220:223], a[108:111]
v_mfma_f32_16x16x32_bf16 a[124:127], a[172:175], a[220:223], a[124:127]
buffer_load_dwordx4 v25, s[36:39], s64 offen offset:1024 lds
v_mfma_f32_16x16x32_bf16 a[120:123], a[172:175], a[216:219], a[120:123]
ds_read_b64_tr_b16 a[188:189], v8 offset:12288
ds_read_b64_tr_b16 a[190:191], v8 offset:14336
v_mfma_f32_16x16x32_bf16 a[116:119], a[172:175], a[212:215], a[116:119]
v_mfma_f32_16x16x32_bf16 a[112:115], a[172:175], a[208:211], a[112:115]
s_waitcnt lgkmcnt(4)
v_mfma_f32_16x16x32_bf16 a[128:131], a[176:179], a[208:211], a[128:131]
buffer_load_dwordx4 v26, s[36:39], s64 offen offset:2048 lds
v_mfma_f32_16x16x32_bf16 a[132:135], a[176:179], a[212:215], a[132:135]
s_waitcnt vmcnt(13)
ds_read_b64_tr_b16 a[160:161], v3 offset:0
v_mfma_f32_16x16x32_bf16 a[140:143], a[180:183], a[212:215], a[140:143]
ds_read_b64_tr_b16 a[162:163], v3 offset:2048
ds_read_b64_tr_b16 a[164:165], v4 offset:0
v_mfma_f32_16x16x32_bf16 a[136:139], a[180:183], a[208:211], a[136:139]
ds_read_b64_tr_b16 a[166:167], v4 offset:2048
s_waitcnt lgkmcnt(4)
v_mfma_f32_16x16x32_bf16 a[144:147], a[184:187], a[208:211], a[144:147]
buffer_load_dwordx4 v27, s[36:39], s64 offen offset:3072 lds
v_mfma_f32_16x16x32_bf16 a[148:151], a[184:187], a[212:215], a[148:151]
ds_read_b64_tr_b16 a[168:169], v5 offset:0
ds_read_b64_tr_b16 a[170:171], v5 offset:2048
v_mfma_f32_16x16x32_bf16 a[156:159], a[188:191], a[212:215], a[156:159]
ds_read_b64_tr_b16 a[172:173], v6 offset:0
ds_read_b64_tr_b16 a[174:175], v6 offset:2048
v_mfma_f32_16x16x32_bf16 a[152:155], a[188:191], a[208:211], a[152:155]
s_waitcnt lgkmcnt(4)
s_add_u32 s54, s54, -1
s_cbranch_scc1 .Lk2_G1
s_waitcnt vmcnt(8)
s_barrier
v_mfma_f32_16x16x32_bf16 a[0:3], a[160:163], a[192:195], a[0:3]
s_add_u32 m0, s61, 0x14000
ds_read_b64_tr_b16 a[176:177], v3 offset:4096
v_mfma_f32_16x16x32_bf16 a[4:7], a[160:163], a[196:199], a[4:7]
buffer_load_dwordx4 v29, s[84:87], s34 offen lds
v_mfma_f32_16x16x32_bf16 a[8:11], a[160:163], a[200:203], a[8:11]
s_add_u32 m0, s61, 0x15000
ds_read_b64_tr_b16 a[178:179], v3 offset:6144
v_mfma_f32_16x16x32_bf16 a[12:15], a[160:163], a[204:207], a[12:15]
ds_read_b128 a[208:211], v9 offset:4096
ds_read_b128 a[212:215], v9 offset:5120
v_mfma_f32_16x16x32_bf16 a[28:31], a[164:167], a[204:207], a[28:31]
buffer_load_dwordx4 v29, s[36:39], s34 offen lds
v_mfma_f32_16x16x32_bf16 a[24:27], a[164:167], a[200:203], a[24:27]
ds_read_b64_tr_b16 a[180:181], v4 offset:4096
ds_read_b128 a[216:219], v10 offset:4096
v_mfma_f32_16x16x32_bf16 a[20:23], a[164:167], a[196:199], a[20:23]
ds_read_b64_tr_b16 a[182:183], v4 offset:6144
ds_read_b128 a[220:223], v10 offset:5120
v_mfma_f32_16x16x32_bf16 a[16:19], a[164:167], a[192:195], a[16:19]
s_waitcnt lgkmcnt(8)
v_mfma_f32_16x16x32_bf16 a[32:35], a[168:171], a[192:195], a[32:35]
s_add_u32 m0, s53, 0
ds_read_b64_tr_b16 a[184:185], v5 offset:4096
v_mfma_f32_16x16x32_bf16 a[36:39], a[168:171], a[196:199], a[36:39]
buffer_load_dwordx4 v20, s[84:87], s65 offen lds
v_mfma_f32_16x16x32_bf16 a[40:43], a[168:171], a[200:203], a[40:43]
ds_read_b64_tr_b16 a[186:187], v5 offset:6144
v_mfma_f32_16x16x32_bf16 a[44:47], a[168:171], a[204:207], a[44:47]
v_mfma_f32_16x16x32_bf16 a[60:63], a[172:175], a[204:207], a[60:63]
buffer_load_dwordx4 v21, s[84:87], s65 offen offset:1024 lds
v_mfma_f32_16x16x32_bf16 a[56:59], a[172:175], a[200:203], a[56:59]
ds_read_b64_tr_b16 a[188:189], v6 offset:4096
ds_read_b64_tr_b16 a[190:191], v6 offset:6144
v_mfma_f32_16x16x32_bf16 a[52:55], a[172:175], a[196:199], a[52:55]
v_mfma_f32_16x16x32_bf16 a[48:51], a[172:175], a[192:195], a[48:51]
s_waitcnt lgkmcnt(5)
v_mfma_f32_16x16x32_bf16 a[64:67], a[176:179], a[192:195], a[64:67]
buffer_load_dwordx4 v22, s[84:87], s65 offen offset:2048 lds
v_mfma_f32_16x16x32_bf16 a[68:71], a[176:179], a[196:199], a[68:71]
ds_read_b64_tr_b16 a[160:161], v7 offset:0
ds_read_b64_tr_b16 a[162:163], v7 offset:2048
v_mfma_f32_16x16x32_bf16 a[72:75], a[176:179], a[200:203], a[72:75]
v_mfma_f32_16x16x32_bf16 a[76:79], a[176:179], a[204:207], a[76:79]
buffer_load_dwordx4 v23, s[84:87], s65 offen offset:3072 lds
v_mfma_f32_16x16x32_bf16 a[92:95], a[180:183], a[204:207], a[92:95]
ds_read_b64_tr_b16 a[164:165], v8 offset:0
ds_read_b64_tr_b16 a[166:167], v8 offset:2048
v_mfma_f32_16x16x32_bf16 a[88:91], a[180:183], a[200:203], a[88:91]
v_mfma_f32_16x16x32_bf16 a[84:87], a[180:183], a[196:199], a[84:87]
v_mfma_f32_16x16x32_bf16 a[80:83], a[180:183], a[192:195], a[80:83]
s_waitcnt lgkmcnt(4)
v_mfma_f32_16x16x32_bf16 a[96:99], a[184:187], a[192:195], a[96:99]
s_add_u32 m0, s53, 0x1000
ds_read_b64_tr_b16 a[168:169], v7 offset:4096
v_mfma_f32_16x16x32_bf16 a[100:103], a[184:187], a[196:199], a[100:103]
buffer_load_dwordx4 v20, s[36:39], s65 offen lds
v_mfma_f32_16x16x32_bf16 a[104:107], a[184:187], a[200:203], a[104:107]
ds_read_b64_tr_b16 a[170:171], v7 offset:6144
v_mfma_f32_16x16x32_bf16 a[108:111], a[184:187], a[204:207], a[108:111]
v_mfma_f32_16x16x32_bf16 a[124:127], a[188:191], a[204:207], a[124:127]
buffer_load_dwordx4 v21, s[36:39], s65 offen offset:1024 lds
v_mfma_f32_16x16x32_bf16 a[120:123], a[188:191], a[200:203], a[120:123]
ds_read_b64_tr_b16 a[172:173], v8 offset:4096
)ASM"
R"ASM(
ds_read_b64_tr_b16 a[174:175], v8 offset:6144
v_mfma_f32_16x16x32_bf16 a[116:119], a[188:191], a[196:199], a[116:119]
v_mfma_f32_16x16x32_bf16 a[112:115], a[188:191], a[192:195], a[112:115]
s_waitcnt lgkmcnt(4)
v_mfma_f32_16x16x32_bf16 a[128:131], a[160:163], a[192:195], a[128:131]
buffer_load_dwordx4 v22, s[36:39], s65 offen offset:2048 lds
v_mfma_f32_16x16x32_bf16 a[132:135], a[160:163], a[196:199], a[132:135]
s_waitcnt vmcnt(13)
ds_read_b64_tr_b16 a[176:177], v3 offset:8192
v_mfma_f32_16x16x32_bf16 a[140:143], a[164:167], a[196:199], a[140:143]
ds_read_b64_tr_b16 a[178:179], v3 offset:10240
ds_read_b64_tr_b16 a[180:181], v4 offset:8192
v_mfma_f32_16x16x32_bf16 a[136:139], a[164:167], a[192:195], a[136:139]
ds_read_b64_tr_b16 a[182:183], v4 offset:10240
s_waitcnt lgkmcnt(4)
v_mfma_f32_16x16x32_bf16 a[144:147], a[168:171], a[192:195], a[144:147]
buffer_load_dwordx4 v23, s[36:39], s65 offen offset:3072 lds
v_mfma_f32_16x16x32_bf16 a[148:151], a[168:171], a[196:199], a[148:151]
ds_read_b64_tr_b16 a[184:185], v5 offset:8192
ds_read_b64_tr_b16 a[186:187], v5 offset:10240
v_mfma_f32_16x16x32_bf16 a[156:159], a[172:175], a[196:199], a[156:159]
ds_read_b64_tr_b16 a[188:189], v6 offset:8192
ds_read_b64_tr_b16 a[190:191], v6 offset:10240
v_mfma_f32_16x16x32_bf16 a[152:155], a[172:175], a[192:195], a[152:155]
s_waitcnt lgkmcnt(4)
s_waitcnt vmcnt(8)
s_barrier
v_mfma_f32_16x16x32_bf16 a[0:3], a[176:179], a[208:211], a[0:3]
ds_read_b64_tr_b16 a[160:161], v3 offset:12288
v_mfma_f32_16x16x32_bf16 a[4:7], a[176:179], a[212:215], a[4:7]
ds_read_b64_tr_b16 a[162:163], v3 offset:14336
s_add_u32 s55, s55, 0x2000
v_mfma_f32_16x16x32_bf16 a[8:11], a[176:179], a[216:219], a[8:11]
s_cmp_eq_u32 s55, 0x12000
s_cselect_b32 s55, 0, s55
v_mfma_f32_16x16x32_bf16 a[12:15], a[176:179], a[220:223], a[12:15]
v_add_u32 v9, s55, v14
v_add_u32 v10, s55, v15
v_mfma_f32_16x16x32_bf16 a[28:31], a[180:183], a[220:223], a[28:31]
ds_read_b64_tr_b16 a[164:165], v4 offset:12288
ds_read_b128 a[192:195], v9 offset:0
v_mfma_f32_16x16x32_bf16 a[24:27], a[180:183], a[216:219], a[24:27]
ds_read_b64_tr_b16 a[166:167], v4 offset:14336
ds_read_b128 a[196:199], v9 offset:1024
v_mfma_f32_16x16x32_bf16 a[20:23], a[180:183], a[212:215], a[20:23]
ds_read_b128 a[200:203], v10 offset:0
ds_read_b128 a[204:207], v10 offset:1024
v_mfma_f32_16x16x32_bf16 a[16:19], a[180:183], a[208:211], a[16:19]
s_waitcnt lgkmcnt(8)
v_mfma_f32_16x16x32_bf16 a[32:35], a[184:187], a[208:211], a[32:35]
s_add_u32 m0, s53, 0x2000
ds_read_b64_tr_b16 a[168:169], v5 offset:12288
v_mfma_f32_16x16x32_bf16 a[36:39], a[184:187], a[212:215], a[36:39]
buffer_load_dwordx4 v24, s[84:87], s65 offen lds
v_mfma_f32_16x16x32_bf16 a[40:43], a[184:187], a[216:219], a[40:43]
ds_read_b64_tr_b16 a[170:171], v5 offset:14336
v_mfma_f32_16x16x32_bf16 a[44:47], a[184:187], a[220:223], a[44:47]
v_mfma_f32_16x16x32_bf16 a[60:63], a[188:191], a[220:223], a[60:63]
buffer_load_dwordx4 v25, s[84:87], s65 offen offset:1024 lds
v_mfma_f32_16x16x32_bf16 a[56:59], a[188:191], a[216:219], a[56:59]
ds_read_b64_tr_b16 a[172:173], v6 offset:12288
ds_read_b64_tr_b16 a[174:175], v6 offset:14336
v_mfma_f32_16x16x32_bf16 a[52:55], a[188:191], a[212:215], a[52:55]
v_mfma_f32_16x16x32_bf16 a[48:51], a[188:191], a[208:211], a[48:51]
s_waitcnt lgkmcnt(7)
v_mfma_f32_16x16x32_bf16 a[64:67], a[160:163], a[208:211], a[64:67]
buffer_load_dwordx4 v26, s[84:87], s65 offen offset:2048 lds
v_mfma_f32_16x16x32_bf16 a[68:71], a[160:163], a[212:215], a[68:71]
ds_read_b64_tr_b16 a[176:177], v7 offset:8192
ds_read_b64_tr_b16 a[178:179], v7 offset:10240
v_mfma_f32_16x16x32_bf16 a[72:75], a[160:163], a[216:219], a[72:75]
v_mfma_f32_16x16x32_bf16 a[76:79], a[160:163], a[220:223], a[76:79]
buffer_load_dwordx4 v27, s[84:87], s65 offen offset:3072 lds
v_mfma_f32_16x16x32_bf16 a[92:95], a[164:167], a[220:223], a[92:95]
ds_read_b64_tr_b16 a[180:181], v8 offset:8192
ds_read_b64_tr_b16 a[182:183], v8 offset:10240
v_mfma_f32_16x16x32_bf16 a[88:91], a[164:167], a[216:219], a[88:91]
v_mfma_f32_16x16x32_bf16 a[84:87], a[164:167], a[212:215], a[84:87]
v_mfma_f32_16x16x32_bf16 a[80:83], a[164:167], a[208:211], a[80:83]
s_waitcnt lgkmcnt(4)
v_mfma_f32_16x16x32_bf16 a[96:99], a[168:171], a[208:211], a[96:99]
s_add_u32 m0, s53, 0x3000
ds_read_b64_tr_b16 a[184:185], v7 offset:12288
v_mfma_f32_16x16x32_bf16 a[100:103], a[168:171], a[212:215], a[100:103]
buffer_load_dwordx4 v24, s[36:39], s65 offen lds
v_mfma_f32_16x16x32_bf16 a[104:107], a[168:171], a[216:219], a[104:107]
ds_read_b64_tr_b16 a[186:187], v7 offset:14336
v_mfma_f32_16x16x32_bf16 a[108:111], a[168:171], a[220:223], a[108:111]
v_mfma_f32_16x16x32_bf16 a[124:127], a[172:175], a[220:223], a[124:127]
buffer_load_dwordx4 v25, s[36:39], s65 offen offset:1024 lds
v_mfma_f32_16x16x32_bf16 a[120:123], a[172:175], a[216:219], a[120:123]
ds_read_b64_tr_b16 a[188:189], v8 offset:12288
ds_read_b64_tr_b16 a[190:191], v8 offset:14336
v_mfma_f32_16x16x32_bf16 a[116:119], a[172:175], a[212:215], a[116:119]
v_mfma_f32_16x16x32_bf16 a[112:115], a[172:175], a[208:211], a[112:115]
s_waitcnt lgkmcnt(4)
v_mfma_f32_16x16x32_bf16 a[128:131], a[176:179], a[208:211], a[128:131]
buffer_load_dwordx4 v26, s[36:39], s65 offen offset:2048 lds
v_mfma_f32_16x16x32_bf16 a[132:135], a[176:179], a[212:215], a[132:135]
s_waitcnt vmcnt(11)
ds_read_b64_tr_b16 a[160:161], v3 offset:0
v_mfma_f32_16x16x32_bf16 a[140:143], a[180:183], a[212:215], a[140:143]
ds_read_b64_tr_b16 a[162:163], v3 offset:2048
ds_read_b64_tr_b16 a[164:165], v4 offset:0
v_mfma_f32_16x16x32_bf16 a[136:139], a[180:183], a[208:211], a[136:139]
ds_read_b64_tr_b16 a[166:167], v4 offset:2048
s_waitcnt lgkmcnt(4)
v_mfma_f32_16x16x32_bf16 a[144:147], a[184:187], a[208:211], a[144:147]
buffer_load_dwordx4 v27, s[36:39], s65 offen offset:3072 lds
v_mfma_f32_16x16x32_bf16 a[148:151], a[184:187], a[212:215], a[148:151]
ds_read_b64_tr_b16 a[168:169], v5 offset:0
ds_read_b64_tr_b16 a[170:171], v5 offset:2048
v_mfma_f32_16x16x32_bf16 a[156:159], a[188:191], a[212:215], a[156:159]
ds_read_b64_tr_b16 a[172:173], v6 offset:0
ds_read_b64_tr_b16 a[174:175], v6 offset:2048
v_mfma_f32_16x16x32_bf16 a[152:155], a[188:191], a[208:211], a[152:155]
s_waitcnt lgkmcnt(6)
v_mfma_f32_16x16x32_bf16 v[128:131], a[160:163], a[192:195], 0
s_waitcnt vmcnt(8)
ds_read_b64_tr_b16 a[176:177], v3 offset:4096
v_mfma_f32_16x16x32_bf16 v[132:135], a[160:163], a[196:199], 0
ds_read_b64_tr_b16 a[178:179], v3 offset:6144
ds_read_b128 a[208:211], v9 offset:4096
ds_read_b128 a[212:215], v9 offset:5120
v_mfma_f32_16x16x32_bf16 v[136:139], a[160:163], a[200:203], 0
ds_read_b128 a[216:219], v10 offset:4096
ds_read_b128 a[220:223], v10 offset:5120
v_mfma_f32_16x16x32_bf16 v[140:143], a[160:163], a[204:207], 0
s_waitcnt lgkmcnt(10)
v_mfma_f32_16x16x32_bf16 v[156:159], a[164:167], a[204:207], 0
ds_read_b64_tr_b16 a[180:181], v4 offset:4096
ds_read_b64_tr_b16 a[182:183], v4 offset:6144
v_mfma_f32_16x16x32_bf16 v[152:155], a[164:167], a[200:203], 0
v_mfma_f32_16x16x32_bf16 v[148:151], a[164:167], a[196:199], 0
v_mfma_f32_16x16x32_bf16 v[144:147], a[164:167], a[192:195], 0
s_waitcnt lgkmcnt(8)
v_mfma_f32_16x16x32_bf16 v[160:163], a[168:171], a[192:195], 0
s_add_u32 s65, s65, 0x90000
ds_read_b64_tr_b16 a[184:185], v5 offset:4096
v_mfma_f32_16x16x32_bf16 v[164:167], a[168:171], a[196:199], 0
s_cmp_eq_u32 s65, s73
s_cselect_b32 s65, s69, s65
ds_read_b64_tr_b16 a[186:187], v5 offset:6144
v_mfma_f32_16x16x32_bf16 v[168:171], a[168:171], a[200:203], 0
s_add_u32 m0, s53, 0
v_accvgpr_read_b32 v48, a0
v_mfma_f32_16x16x32_bf16 v[172:175], a[168:171], a[204:207], 0
buffer_load_dwordx4 v20, s[84:87], s65 offen lds
v_accvgpr_read_b32 v49, a1
v_mfma_f32_16x16x32_bf16 v[188:191], a[172:175], a[204:207], 0
ds_read_b64_tr_b16 a[188:189], v6 offset:4096
ds_read_b64_tr_b16 a[190:191], v6 offset:6144
v_mfma_f32_16x16x32_bf16 v[184:187], a[172:175], a[200:203], 0
v_accvgpr_read_b32 v50, a64
v_accvgpr_read_b32 v51, a65
v_max_f32 v48, 0, v48
v_mfma_f32_16x16x32_bf16 v[180:183], a[172:175], a[196:199], 0
buffer_load_dwordx4 v21, s[84:87], s65 offen offset:1024 lds
v_mfma_f32_16x16x32_bf16 v[176:179], a[172:175], a[192:195], 0
v_max_f32 v49, 0, v49
v_mul_f32 v48, v48, v50
s_waitcnt lgkmcnt(4)
v_mfma_f32_16x16x32_bf16 v[192:195], a[176:179], a[192:195], 0
s_waitcnt vmcnt(6)
ds_read_b64_tr_b16 a[160:161], v3 offset:8192
v_mfma_f32_16x16x32_bf16 v[196:199], a[176:179], a[196:199], 0
buffer_load_dwordx4 v22, s[84:87], s65 offen offset:2048 lds
ds_read_b64_tr_b16 a[162:163], v3 offset:10240
v_mfma_f32_16x16x32_bf16 v[200:203], a[176:179], a[200:203], 0
v_mul_f32 v49, v49, v51
v_cvt_pk_bf16_f32 v80, v48, v49
v_mfma_f32_16x16x32_bf16 v[204:207], a[176:179], a[204:207], 0
v_accvgpr_read_b32 v52, a2
v_accvgpr_read_b32 v53, a3
v_accvgpr_read_b32 v54, a66
v_mfma_f32_16x16x32_bf16 v[220:223], a[180:183], a[204:207], 0
buffer_load_dwordx4 v23, s[84:87], s65 offen offset:3072 lds
v_mfma_f32_16x16x32_bf16 v[216:219], a[180:183], a[200:203], 0
ds_read_b64_tr_b16 a[164:165], v4 offset:8192
ds_read_b64_tr_b16 a[166:167], v4 offset:10240
v_accvgpr_read_b32 v55, a67
v_mfma_f32_16x16x32_bf16 v[212:215], a[180:183], a[196:199], 0
v_max_f32 v52, 0, v52
v_max_f32 v53, 0, v53
v_mfma_f32_16x16x32_bf16 v[208:211], a[180:183], a[192:195], 0
v_mul_f32 v52, v52, v54
v_mul_f32 v53, v53, v55
s_waitcnt lgkmcnt(4)
v_mfma_f32_16x16x32_bf16 v[224:227], a[184:187], a[192:195], 0
s_add_u32 m0, s53, 0x1000
ds_read_b64_tr_b16 a[168:169], v5 offset:8192
v_mfma_f32_16x16x32_bf16 v[228:231], a[184:187], a[196:199], 0
buffer_load_dwordx4 v20, s[36:39], s65 offen lds
ds_read_b64_tr_b16 a[170:171], v5 offset:10240
v_mfma_f32_16x16x32_bf16 v[232:235], a[184:187], a[200:203], 0
v_cvt_pk_bf16_f32 v81, v52, v53
v_accvgpr_read_b32 v48, a16
v_mfma_f32_16x16x32_bf16 v[236:239], a[184:187], a[204:207], 0
v_accvgpr_read_b32 v49, a17
v_accvgpr_read_b32 v50, a80
)ASM"
R"ASM(
v_accvgpr_read_b32 v51, a81
v_mfma_f32_16x16x32_bf16 v[252:255], a[188:191], a[204:207], 0
buffer_load_dwordx4 v21, s[36:39], s65 offen offset:1024 lds
v_mfma_f32_16x16x32_bf16 v[248:251], a[188:191], a[200:203], 0
ds_read_b64_tr_b16 a[172:173], v6 offset:8192
ds_read_b64_tr_b16 a[174:175], v6 offset:10240
v_max_f32 v48, 0, v48
v_mfma_f32_16x16x32_bf16 v[244:247], a[188:191], a[196:199], 0
v_max_f32 v49, 0, v49
v_mul_f32 v48, v48, v50
v_mfma_f32_16x16x32_bf16 v[240:243], a[188:191], a[192:195], 0
v_mul_f32 v49, v49, v51
v_cvt_pk_bf16_f32 v82, v48, v49
s_waitcnt lgkmcnt(4)
v_mfma_f32_16x16x32_bf16 v[128:131], a[160:163], a[208:211], v[128:131]
buffer_load_dwordx4 v22, s[36:39], s65 offen offset:2048 lds
v_mfma_f32_16x16x32_bf16 v[132:135], a[160:163], a[212:215], v[132:135]
s_waitcnt vmcnt(7)
ds_read_b64_tr_b16 a[176:177], v3 offset:12288
s_add_u32 s55, s55, 0x2000
v_mfma_f32_16x16x32_bf16 v[136:139], a[160:163], a[216:219], v[136:139]
ds_read_b64_tr_b16 a[178:179], v3 offset:14336
v_mfma_f32_16x16x32_bf16 v[140:143], a[160:163], a[220:223], v[140:143]
buffer_load_dwordx4 v23, s[36:39], s65 offen offset:3072 lds
v_mfma_f32_16x16x32_bf16 v[156:159], a[164:167], a[220:223], v[156:159]
ds_read_b64_tr_b16 a[180:181], v4 offset:12288
ds_read_b64_tr_b16 a[182:183], v4 offset:14336
v_mfma_f32_16x16x32_bf16 v[152:155], a[164:167], a[216:219], v[152:155]
s_cmp_eq_u32 s55, 0x12000
s_cselect_b32 s55, 0, s55
v_add_u32 v9, s55, v14
v_mfma_f32_16x16x32_bf16 v[148:151], a[164:167], a[212:215], v[148:151]
v_add_u32 v10, s55, v15
ds_read_b128 a[192:195], v9 offset:0
v_mfma_f32_16x16x32_bf16 v[144:147], a[164:167], a[208:211], v[144:147]
ds_read_b128 a[196:199], v9 offset:1024
ds_read_b128 a[200:203], v10 offset:0
s_waitcnt lgkmcnt(7)
v_mfma_f32_16x16x32_bf16 v[160:163], a[168:171], a[208:211], v[160:163]
s_add_u32 m0, s53, 0x2000
ds_read_b64_tr_b16 a[184:185], v5 offset:12288
v_mfma_f32_16x16x32_bf16 v[164:167], a[168:171], a[212:215], v[164:167]
buffer_load_dwordx4 v24, s[84:87], s65 offen lds
ds_read_b128 a[204:207], v10 offset:1024
v_mfma_f32_16x16x32_bf16 v[168:171], a[168:171], a[216:219], v[168:171]
ds_read_b64_tr_b16 a[186:187], v5 offset:14336
v_accvgpr_read_b32 v52, a18
v_mfma_f32_16x16x32_bf16 v[172:175], a[168:171], a[220:223], v[172:175]
v_accvgpr_read_b32 v53, a19
v_accvgpr_read_b32 v54, a82
v_accvgpr_read_b32 v55, a83
v_mfma_f32_16x16x32_bf16 v[188:191], a[172:175], a[220:223], v[188:191]
buffer_load_dwordx4 v25, s[84:87], s65 offen offset:1024 lds
v_mfma_f32_16x16x32_bf16 v[184:187], a[172:175], a[216:219], v[184:187]
ds_read_b64_tr_b16 a[188:189], v6 offset:12288
ds_read_b64_tr_b16 a[190:191], v6 offset:14336
v_max_f32 v52, 0, v52
v_mfma_f32_16x16x32_bf16 v[180:183], a[172:175], a[212:215], v[180:183]
v_max_f32 v53, 0, v53
v_mul_f32 v52, v52, v54
v_mfma_f32_16x16x32_bf16 v[176:179], a[172:175], a[208:211], v[176:179]
v_mul_f32 v53, v53, v55
v_cvt_pk_bf16_f32 v83, v52, v53
s_waitcnt lgkmcnt(8)
v_mfma_f32_16x16x32_bf16 v[192:195], a[176:179], a[208:211], v[192:195]
buffer_load_dwordx4 v26, s[84:87], s65 offen offset:2048 lds
v_mfma_f32_16x16x32_bf16 v[196:199], a[176:179], a[212:215], v[196:199]
s_waitcnt vmcnt(7)
ds_read_b64_tr_b16 a[160:161], v3 offset:0
ds_read_b64_tr_b16 a[162:163], v3 offset:2048
v_mfma_f32_16x16x32_bf16 v[200:203], a[176:179], a[216:219], v[200:203]
v_accvgpr_read_b32 v48, a4
v_accvgpr_read_b32 v49, a5
v_mfma_f32_16x16x32_bf16 v[204:207], a[176:179], a[220:223], v[204:207]
buffer_load_dwordx4 v27, s[84:87], s65 offen offset:3072 lds
v_accvgpr_read_b32 v50, a68
v_mfma_f32_16x16x32_bf16 v[220:223], a[180:183], a[220:223], v[220:223]
ds_read_b64_tr_b16 a[164:165], v4 offset:0
ds_read_b64_tr_b16 a[166:167], v4 offset:2048
v_mfma_f32_16x16x32_bf16 v[216:219], a[180:183], a[216:219], v[216:219]
v_accvgpr_read_b32 v51, a69
v_max_f32 v48, 0, v48
v_max_f32 v49, 0, v49
v_mfma_f32_16x16x32_bf16 v[212:215], a[180:183], a[212:215], v[212:215]
v_mul_f32 v48, v48, v50
v_mul_f32 v49, v49, v51
v_mfma_f32_16x16x32_bf16 v[208:211], a[180:183], a[208:211], v[208:211]
v_cvt_pk_bf16_f32 v84, v48, v49
v_accvgpr_read_b32 v52, a6
s_waitcnt lgkmcnt(4)
v_mfma_f32_16x16x32_bf16 v[224:227], a[184:187], a[208:211], v[224:227]
s_add_u32 m0, s53, 0x3000
ds_read_b64_tr_b16 a[168:169], v5 offset:0
v_mfma_f32_16x16x32_bf16 v[228:231], a[184:187], a[212:215], v[228:231]
buffer_load_dwordx4 v24, s[36:39], s65 offen lds
ds_read_b64_tr_b16 a[170:171], v5 offset:2048
v_mfma_f32_16x16x32_bf16 v[232:235], a[184:187], a[216:219], v[232:235]
v_accvgpr_read_b32 v53, a7
v_accvgpr_read_b32 v54, a70
v_mfma_f32_16x16x32_bf16 v[236:239], a[184:187], a[220:223], v[236:239]
v_accvgpr_read_b32 v55, a71
v_max_f32 v52, 0, v52
v_max_f32 v53, 0, v53
v_mfma_f32_16x16x32_bf16 v[252:255], a[188:191], a[220:223], v[252:255]
buffer_load_dwordx4 v25, s[36:39], s65 offen offset:1024 lds
v_mfma_f32_16x16x32_bf16 v[248:251], a[188:191], a[216:219], v[248:251]
ds_read_b64_tr_b16 a[172:173], v6 offset:0
ds_read_b64_tr_b16 a[174:175], v6 offset:2048
v_mul_f32 v52, v52, v54
v_mfma_f32_16x16x32_bf16 v[244:247], a[188:191], a[212:215], v[244:247]
v_mul_f32 v53, v53, v55
v_cvt_pk_bf16_f32 v85, v52, v53
v_mfma_f32_16x16x32_bf16 v[240:243], a[188:191], a[208:211], v[240:243]
v_accvgpr_read_b32 v48, a20
v_accvgpr_read_b32 v49, a21
s_waitcnt lgkmcnt(4)
s_barrier
v_mfma_f32_16x16x32_bf16 v[128:131], a[160:163], a[192:195], v[128:131]
buffer_load_dwordx4 v26, s[36:39], s65 offen offset:2048 lds
v_mfma_f32_16x16x32_bf16 v[132:135], a[160:163], a[196:199], v[132:135]
s_waitcnt vmcnt(7)
ds_read_b64_tr_b16 a[176:177], v3 offset:4096
ds_read_b128 a[208:211], v9 offset:4096
v_mfma_f32_16x16x32_bf16 v[136:139], a[160:163], a[200:203], v[136:139]
ds_read_b64_tr_b16 a[178:179], v3 offset:6144
ds_read_b128 a[212:215], v9 offset:5120
v_mfma_f32_16x16x32_bf16 v[140:143], a[160:163], a[204:207], v[140:143]
buffer_load_dwordx4 v27, s[36:39], s65 offen offset:3072 lds
ds_read_b128 a[216:219], v10 offset:4096
v_mfma_f32_16x16x32_bf16 v[156:159], a[164:167], a[204:207], v[156:159]
ds_read_b64_tr_b16 a[180:181], v4 offset:4096
ds_read_b128 a[220:223], v10 offset:5120
v_mfma_f32_16x16x32_bf16 v[152:155], a[164:167], a[200:203], v[152:155]
ds_read_b64_tr_b16 a[182:183], v4 offset:6144
v_accvgpr_read_b32 v50, a84
v_accvgpr_read_b32 v51, a85
v_mfma_f32_16x16x32_bf16 v[148:151], a[164:167], a[196:199], v[148:151]
v_max_f32 v48, 0, v48
v_max_f32 v49, 0, v49
v_mfma_f32_16x16x32_bf16 v[144:147], a[164:167], a[192:195], v[144:147]
v_mul_f32 v48, v48, v50
v_mul_f32 v49, v49, v51
s_waitcnt lgkmcnt(8)
v_mfma_f32_16x16x32_bf16 v[160:163], a[168:171], a[192:195], v[160:163]
s_add_u32 s65, s65, 0x90000
ds_read_b64_tr_b16 a[184:185], v5 offset:4096
v_mfma_f32_16x16x32_bf16 v[164:167], a[168:171], a[196:199], v[164:167]
s_cmp_eq_u32 s65, s73
s_cselect_b32 s65, s69, s65
ds_read_b64_tr_b16 a[186:187], v5 offset:6144
v_mfma_f32_16x16x32_bf16 v[168:171], a[168:171], a[200:203], v[168:171]
s_add_u32 m0, s53, 0
v_cvt_pk_bf16_f32 v86, v48, v49
v_mfma_f32_16x16x32_bf16 v[172:175], a[168:171], a[204:207], v[172:175]
buffer_load_dwordx4 v20, s[84:87], s65 offen lds
v_accvgpr_read_b32 v52, a22
v_mfma_f32_16x16x32_bf16 v[188:191], a[172:175], a[204:207], v[188:191]
ds_read_b64_tr_b16 a[188:189], v6 offset:4096
ds_read_b64_tr_b16 a[190:191], v6 offset:6144
v_mfma_f32_16x16x32_bf16 v[184:187], a[172:175], a[200:203], v[184:187]
v_accvgpr_read_b32 v53, a23
v_accvgpr_read_b32 v54, a86
v_accvgpr_read_b32 v55, a87
v_mfma_f32_16x16x32_bf16 v[180:183], a[172:175], a[196:199], v[180:183]
buffer_load_dwordx4 v21, s[84:87], s65 offen offset:1024 lds
v_mfma_f32_16x16x32_bf16 v[176:179], a[172:175], a[192:195], v[176:179]
v_max_f32 v52, 0, v52
v_max_f32 v53, 0, v53
s_waitcnt lgkmcnt(4)
v_mfma_f32_16x16x32_bf16 v[192:195], a[176:179], a[192:195], v[192:195]
s_waitcnt vmcnt(6)
ds_read_b64_tr_b16 a[160:161], v3 offset:8192
v_mfma_f32_16x16x32_bf16 v[196:199], a[176:179], a[196:199], v[196:199]
buffer_load_dwordx4 v22, s[84:87], s65 offen offset:2048 lds
ds_read_b64_tr_b16 a[162:163], v3 offset:10240
v_mfma_f32_16x16x32_bf16 v[200:203], a[176:179], a[200:203], v[200:203]
v_mul_f32 v52, v52, v54
v_mul_f32 v53, v53, v55
v_mfma_f32_16x16x32_bf16 v[204:207], a[176:179], a[204:207], v[204:207]
v_cvt_pk_bf16_f32 v87, v52, v53
v_accvgpr_read_b32 v48, a8
v_accvgpr_read_b32 v49, a9
v_mfma_f32_16x16x32_bf16 v[220:223], a[180:183], a[204:207], v[220:223]
buffer_load_dwordx4 v23, s[84:87], s65 offen offset:3072 lds
v_mfma_f32_16x16x32_bf16 v[216:219], a[180:183], a[200:203], v[216:219]
ds_read_b64_tr_b16 a[164:165], v4 offset:8192
ds_read_b64_tr_b16 a[166:167], v4 offset:10240
v_accvgpr_read_b32 v50, a72
v_mfma_f32_16x16x32_bf16 v[212:215], a[180:183], a[196:199], v[212:215]
v_accvgpr_read_b32 v51, a73
v_max_f32 v48, 0, v48
v_mfma_f32_16x16x32_bf16 v[208:211], a[180:183], a[192:195], v[208:211]
v_max_f32 v49, 0, v49
v_mul_f32 v48, v48, v50
s_waitcnt lgkmcnt(4)
v_mfma_f32_16x16x32_bf16 v[224:227], a[184:187], a[192:195], v[224:227]
s_add_u32 m0, s53, 0x1000
ds_read_b64_tr_b16 a[168:169], v5 offset:8192
v_mfma_f32_16x16x32_bf16 v[228:231], a[184:187], a[196:199], v[228:231]
buffer_load_dwordx4 v20, s[36:39], s65 offen lds
)ASM"
R"ASM(
ds_read_b64_tr_b16 a[170:171], v5 offset:10240
v_mfma_f32_16x16x32_bf16 v[232:235], a[184:187], a[200:203], v[232:235]
v_mul_f32 v49, v49, v51
v_cvt_pk_bf16_f32 v88, v48, v49
v_mfma_f32_16x16x32_bf16 v[236:239], a[184:187], a[204:207], v[236:239]
v_accvgpr_read_b32 v52, a10
v_accvgpr_read_b32 v53, a11
v_accvgpr_read_b32 v54, a74
v_mfma_f32_16x16x32_bf16 v[252:255], a[188:191], a[204:207], v[252:255]
buffer_load_dwordx4 v21, s[36:39], s65 offen offset:1024 lds
v_mfma_f32_16x16x32_bf16 v[248:251], a[188:191], a[200:203], v[248:251]
ds_read_b64_tr_b16 a[172:173], v6 offset:8192
ds_read_b64_tr_b16 a[174:175], v6 offset:10240
v_accvgpr_read_b32 v55, a75
v_mfma_f32_16x16x32_bf16 v[244:247], a[188:191], a[196:199], v[244:247]
v_max_f32 v52, 0, v52
v_max_f32 v53, 0, v53
v_mfma_f32_16x16x32_bf16 v[240:243], a[188:191], a[192:195], v[240:243]
v_mul_f32 v52, v52, v54
v_mul_f32 v53, v53, v55
s_waitcnt lgkmcnt(4)
v_mfma_f32_16x16x32_bf16 v[128:131], a[160:163], a[208:211], v[128:131]
buffer_load_dwordx4 v22, s[36:39], s65 offen offset:2048 lds
v_mfma_f32_16x16x32_bf16 v[132:135], a[160:163], a[212:215], v[132:135]
s_waitcnt vmcnt(7)
ds_read_b64_tr_b16 a[176:177], v3 offset:12288
s_add_u32 s55, s55, 0x2000
v_mfma_f32_16x16x32_bf16 v[136:139], a[160:163], a[216:219], v[136:139]
ds_read_b64_tr_b16 a[178:179], v3 offset:14336
v_mfma_f32_16x16x32_bf16 v[140:143], a[160:163], a[220:223], v[140:143]
buffer_load_dwordx4 v23, s[36:39], s65 offen offset:3072 lds
v_mfma_f32_16x16x32_bf16 v[156:159], a[164:167], a[220:223], v[156:159]
ds_read_b64_tr_b16 a[180:181], v4 offset:12288
ds_read_b64_tr_b16 a[182:183], v4 offset:14336
v_mfma_f32_16x16x32_bf16 v[152:155], a[164:167], a[216:219], v[152:155]
s_cmp_eq_u32 s55, 0x12000
s_cselect_b32 s55, 0, s55
v_add_u32 v9, s55, v14
v_mfma_f32_16x16x32_bf16 v[148:151], a[164:167], a[212:215], v[148:151]
v_add_u32 v10, s55, v15
ds_read_b128 a[192:195], v9 offset:0
v_mfma_f32_16x16x32_bf16 v[144:147], a[164:167], a[208:211], v[144:147]
ds_read_b128 a[196:199], v9 offset:1024
ds_read_b128 a[200:203], v10 offset:0
s_waitcnt lgkmcnt(7)
v_mfma_f32_16x16x32_bf16 v[160:163], a[168:171], a[208:211], v[160:163]
s_add_u32 m0, s53, 0x2000
ds_read_b64_tr_b16 a[184:185], v5 offset:12288
v_mfma_f32_16x16x32_bf16 v[164:167], a[168:171], a[212:215], v[164:167]
buffer_load_dwordx4 v24, s[84:87], s65 offen lds
ds_read_b128 a[204:207], v10 offset:1024
v_mfma_f32_16x16x32_bf16 v[168:171], a[168:171], a[216:219], v[168:171]
ds_read_b64_tr_b16 a[186:187], v5 offset:14336
v_cvt_pk_bf16_f32 v89, v52, v53
v_mfma_f32_16x16x32_bf16 v[172:175], a[168:171], a[220:223], v[172:175]
v_accvgpr_read_b32 v48, a24
v_accvgpr_read_b32 v49, a25
v_accvgpr_read_b32 v50, a88
v_mfma_f32_16x16x32_bf16 v[188:191], a[172:175], a[220:223], v[188:191]
buffer_load_dwordx4 v25, s[84:87], s65 offen offset:1024 lds
v_mfma_f32_16x16x32_bf16 v[184:187], a[172:175], a[216:219], v[184:187]
ds_read_b64_tr_b16 a[188:189], v6 offset:12288
ds_read_b64_tr_b16 a[190:191], v6 offset:14336
v_accvgpr_read_b32 v51, a89
v_mfma_f32_16x16x32_bf16 v[180:183], a[172:175], a[212:215], v[180:183]
v_max_f32 v48, 0, v48
v_max_f32 v49, 0, v49
v_mfma_f32_16x16x32_bf16 v[176:179], a[172:175], a[208:211], v[176:179]
v_mul_f32 v48, v48, v50
v_mul_f32 v49, v49, v51
s_waitcnt lgkmcnt(8)
v_mfma_f32_16x16x32_bf16 v[192:195], a[176:179], a[208:211], v[192:195]
buffer_load_dwordx4 v26, s[84:87], s65 offen offset:2048 lds
v_mfma_f32_16x16x32_bf16 v[196:199], a[176:179], a[212:215], v[196:199]
s_waitcnt vmcnt(7)
ds_read_b64_tr_b16 a[160:161], v3 offset:0
ds_read_b64_tr_b16 a[162:163], v3 offset:2048
v_mfma_f32_16x16x32_bf16 v[200:203], a[176:179], a[216:219], v[200:203]
v_cvt_pk_bf16_f32 v90, v48, v49
v_accvgpr_read_b32 v52, a26
v_mfma_f32_16x16x32_bf16 v[204:207], a[176:179], a[220:223], v[204:207]
buffer_load_dwordx4 v27, s[84:87], s65 offen offset:3072 lds
v_accvgpr_read_b32 v53, a27
v_mfma_f32_16x16x32_bf16 v[220:223], a[180:183], a[220:223], v[220:223]
ds_read_b64_tr_b16 a[164:165], v4 offset:0
ds_read_b64_tr_b16 a[166:167], v4 offset:2048
v_mfma_f32_16x16x32_bf16 v[216:219], a[180:183], a[216:219], v[216:219]
v_accvgpr_read_b32 v54, a90
v_accvgpr_read_b32 v55, a91
v_max_f32 v52, 0, v52
v_mfma_f32_16x16x32_bf16 v[212:215], a[180:183], a[212:215], v[212:215]
v_max_f32 v53, 0, v53
v_mul_f32 v52, v52, v54
v_mfma_f32_16x16x32_bf16 v[208:211], a[180:183], a[208:211], v[208:211]
v_mul_f32 v53, v53, v55
v_cvt_pk_bf16_f32 v91, v52, v53
s_waitcnt lgkmcnt(4)
v_mfma_f32_16x16x32_bf16 v[224:227], a[184:187], a[208:211], v[224:227]
s_add_u32 m0, s53, 0x3000
ds_read_b64_tr_b16 a[168:169], v5 offset:0
v_mfma_f32_16x16x32_bf16 v[228:231], a[184:187], a[212:215], v[228:231]
buffer_load_dwordx4 v24, s[36:39], s65 offen lds
ds_read_b64_tr_b16 a[170:171], v5 offset:2048
v_mfma_f32_16x16x32_bf16 v[232:235], a[184:187], a[216:219], v[232:235]
v_accvgpr_read_b32 v48, a12
v_accvgpr_read_b32 v49, a13
v_mfma_f32_16x16x32_bf16 v[236:239], a[184:187], a[220:223], v[236:239]
v_accvgpr_read_b32 v50, a76
v_accvgpr_read_b32 v51, a77
v_max_f32 v48, 0, v48
v_mfma_f32_16x16x32_bf16 v[252:255], a[188:191], a[220:223], v[252:255]
buffer_load_dwordx4 v25, s[36:39], s65 offen offset:1024 lds
v_mfma_f32_16x16x32_bf16 v[248:251], a[188:191], a[216:219], v[248:251]
ds_read_b64_tr_b16 a[172:173], v6 offset:0
ds_read_b64_tr_b16 a[174:175], v6 offset:2048
v_max_f32 v49, 0, v49
v_mfma_f32_16x16x32_bf16 v[244:247], a[188:191], a[212:215], v[244:247]
v_mul_f32 v48, v48, v50
v_mul_f32 v49, v49, v51
v_mfma_f32_16x16x32_bf16 v[240:243], a[188:191], a[208:211], v[240:243]
v_cvt_pk_bf16_f32 v92, v48, v49
v_accvgpr_read_b32 v52, a14
s_waitcnt lgkmcnt(4)
v_mfma_f32_16x16x32_bf16 v[128:131], a[160:163], a[192:195], v[128:131]
buffer_load_dwordx4 v26, s[36:39], s65 offen offset:2048 lds
v_mfma_f32_16x16x32_bf16 v[132:135], a[160:163], a[196:199], v[132:135]
s_waitcnt vmcnt(7)
ds_read_b64_tr_b16 a[176:177], v3 offset:4096
ds_read_b128 a[208:211], v9 offset:4096
v_mfma_f32_16x16x32_bf16 v[136:139], a[160:163], a[200:203], v[136:139]
ds_read_b64_tr_b16 a[178:179], v3 offset:6144
ds_read_b128 a[212:215], v9 offset:5120
v_mfma_f32_16x16x32_bf16 v[140:143], a[160:163], a[204:207], v[140:143]
buffer_load_dwordx4 v27, s[36:39], s65 offen offset:3072 lds
ds_read_b128 a[216:219], v10 offset:4096
v_mfma_f32_16x16x32_bf16 v[156:159], a[164:167], a[204:207], v[156:159]
ds_read_b64_tr_b16 a[180:181], v4 offset:4096
ds_read_b128 a[220:223], v10 offset:5120
v_mfma_f32_16x16x32_bf16 v[152:155], a[164:167], a[200:203], v[152:155]
ds_read_b64_tr_b16 a[182:183], v4 offset:6144
v_accvgpr_read_b32 v53, a15
v_accvgpr_read_b32 v54, a78
v_mfma_f32_16x16x32_bf16 v[148:151], a[164:167], a[196:199], v[148:151]
v_accvgpr_read_b32 v55, a79
v_max_f32 v52, 0, v52
v_mfma_f32_16x16x32_bf16 v[144:147], a[164:167], a[192:195], v[144:147]
v_max_f32 v53, 0, v53
v_mul_f32 v52, v52, v54
s_waitcnt lgkmcnt(8)
v_mfma_f32_16x16x32_bf16 v[160:163], a[168:171], a[192:195], v[160:163]
s_add_u32 s65, s65, 0x90000
ds_read_b64_tr_b16 a[184:185], v5 offset:4096
v_mfma_f32_16x16x32_bf16 v[164:167], a[168:171], a[196:199], v[164:167]
s_cmp_eq_u32 s65, s73
s_cselect_b32 s65, s69, s65
ds_read_b64_tr_b16 a[186:187], v5 offset:6144
v_mfma_f32_16x16x32_bf16 v[168:171], a[168:171], a[200:203], v[168:171]
s_add_u32 m0, s53, 0
v_mul_f32 v53, v53, v55
v_mfma_f32_16x16x32_bf16 v[172:175], a[168:171], a[204:207], v[172:175]
buffer_load_dwordx4 v20, s[84:87], s65 offen lds
v_cvt_pk_bf16_f32 v93, v52, v53
v_mfma_f32_16x16x32_bf16 v[188:191], a[172:175], a[204:207], v[188:191]
ds_read_b64_tr_b16 a[188:189], v6 offset:4096
ds_read_b64_tr_b16 a[190:191], v6 offset:6144
v_mfma_f32_16x16x32_bf16 v[184:187], a[172:175], a[200:203], v[184:187]
v_accvgpr_read_b32 v48, a28
v_accvgpr_read_b32 v49, a29
v_accvgpr_read_b32 v50, a92
v_mfma_f32_16x16x32_bf16 v[180:183], a[172:175], a[196:199], v[180:183]
buffer_load_dwordx4 v21, s[84:87], s65 offen offset:1024 lds
v_mfma_f32_16x16x32_bf16 v[176:179], a[172:175], a[192:195], v[176:179]
v_accvgpr_read_b32 v51, a93
v_max_f32 v48, 0, v48
s_waitcnt lgkmcnt(4)
v_mfma_f32_16x16x32_bf16 v[192:195], a[176:179], a[192:195], v[192:195]
s_waitcnt vmcnt(6)
ds_read_b64_tr_b16 a[160:161], v3 offset:8192
v_mfma_f32_16x16x32_bf16 v[196:199], a[176:179], a[196:199], v[196:199]
buffer_load_dwordx4 v22, s[84:87], s65 offen offset:2048 lds
ds_read_b64_tr_b16 a[162:163], v3 offset:10240
v_mfma_f32_16x16x32_bf16 v[200:203], a[176:179], a[200:203], v[200:203]
v_max_f32 v49, 0, v49
v_mul_f32 v48, v48, v50
v_mfma_f32_16x16x32_bf16 v[204:207], a[176:179], a[204:207], v[204:207]
v_mul_f32 v49, v49, v51
v_cvt_pk_bf16_f32 v94, v48, v49
v_accvgpr_read_b32 v52, a30
v_mfma_f32_16x16x32_bf16 v[220:223], a[180:183], a[204:207], v[220:223]
buffer_load_dwordx4 v23, s[84:87], s65 offen offset:3072 lds
v_mfma_f32_16x16x32_bf16 v[216:219], a[180:183], a[200:203], v[216:219]
ds_read_b64_tr_b16 a[164:165], v4 offset:8192
ds_read_b64_tr_b16 a[166:167], v4 offset:10240
v_accvgpr_read_b32 v53, a31
v_mfma_f32_16x16x32_bf16 v[212:215], a[180:183], a[196:199], v[212:215]
v_accvgpr_read_b32 v54, a94
v_accvgpr_read_b32 v55, a95
v_mfma_f32_16x16x32_bf16 v[208:211], a[180:183], a[192:195], v[208:211]
v_max_f32 v52, 0, v52
v_max_f32 v53, 0, v53
)ASM"
R"ASM(
s_waitcnt lgkmcnt(4)
v_mfma_f32_16x16x32_bf16 v[224:227], a[184:187], a[192:195], v[224:227]
s_add_u32 m0, s53, 0x1000
ds_read_b64_tr_b16 a[168:169], v5 offset:8192
v_mfma_f32_16x16x32_bf16 v[228:231], a[184:187], a[196:199], v[228:231]
buffer_load_dwordx4 v20, s[36:39], s65 offen lds
ds_read_b64_tr_b16 a[170:171], v5 offset:10240
v_mfma_f32_16x16x32_bf16 v[232:235], a[184:187], a[200:203], v[232:235]
v_mul_f32 v52, v52, v54
v_mul_f32 v53, v53, v55
v_mfma_f32_16x16x32_bf16 v[236:239], a[184:187], a[204:207], v[236:239]
v_cvt_pk_bf16_f32 v95, v52, v53
v_accvgpr_read_b32 v48, a32
v_accvgpr_read_b32 v49, a33
v_mfma_f32_16x16x32_bf16 v[252:255], a[188:191], a[204:207], v[252:255]
buffer_load_dwordx4 v21, s[36:39], s65 offen offset:1024 lds
v_mfma_f32_16x16x32_bf16 v[248:251], a[188:191], a[200:203], v[248:251]
ds_read_b64_tr_b16 a[172:173], v6 offset:8192
ds_read_b64_tr_b16 a[174:175], v6 offset:10240
v_accvgpr_read_b32 v50, a96
v_mfma_f32_16x16x32_bf16 v[244:247], a[188:191], a[196:199], v[244:247]
v_accvgpr_read_b32 v51, a97
v_max_f32 v48, 0, v48
v_mfma_f32_16x16x32_bf16 v[240:243], a[188:191], a[192:195], v[240:243]
v_max_f32 v49, 0, v49
v_mul_f32 v48, v48, v50
s_waitcnt lgkmcnt(4)
v_mfma_f32_16x16x32_bf16 v[128:131], a[160:163], a[208:211], v[128:131]
buffer_load_dwordx4 v22, s[36:39], s65 offen offset:2048 lds
v_mfma_f32_16x16x32_bf16 v[132:135], a[160:163], a[212:215], v[132:135]
s_waitcnt vmcnt(7)
ds_read_b64_tr_b16 a[176:177], v3 offset:12288
s_add_u32 s55, s55, 0x2000
v_mfma_f32_16x16x32_bf16 v[136:139], a[160:163], a[216:219], v[136:139]
ds_read_b64_tr_b16 a[178:179], v3 offset:14336
v_mfma_f32_16x16x32_bf16 v[140:143], a[160:163], a[220:223], v[140:143]
buffer_load_dwordx4 v23, s[36:39], s65 offen offset:3072 lds
v_mfma_f32_16x16x32_bf16 v[156:159], a[164:167], a[220:223], v[156:159]
ds_read_b64_tr_b16 a[180:181], v4 offset:12288
ds_read_b64_tr_b16 a[182:183], v4 offset:14336
v_mfma_f32_16x16x32_bf16 v[152:155], a[164:167], a[216:219], v[152:155]
s_cmp_eq_u32 s55, 0x12000
s_cselect_b32 s55, 0, s55
v_add_u32 v9, s55, v14
v_mfma_f32_16x16x32_bf16 v[148:151], a[164:167], a[212:215], v[148:151]
v_add_u32 v10, s55, v15
ds_read_b128 a[192:195], v9 offset:0
v_mfma_f32_16x16x32_bf16 v[144:147], a[164:167], a[208:211], v[144:147]
ds_read_b128 a[196:199], v9 offset:1024
ds_read_b128 a[200:203], v10 offset:0
s_waitcnt lgkmcnt(7)
v_mfma_f32_16x16x32_bf16 v[160:163], a[168:171], a[208:211], v[160:163]
s_add_u32 m0, s53, 0x2000
ds_read_b64_tr_b16 a[184:185], v5 offset:12288
v_mfma_f32_16x16x32_bf16 v[164:167], a[168:171], a[212:215], v[164:167]
buffer_load_dwordx4 v24, s[84:87], s65 offen lds
ds_read_b128 a[204:207], v10 offset:1024
v_mfma_f32_16x16x32_bf16 v[168:171], a[168:171], a[216:219], v[168:171]
ds_read_b64_tr_b16 a[186:187], v5 offset:14336
v_mul_f32 v49, v49, v51
v_mfma_f32_16x16x32_bf16 v[172:175], a[168:171], a[220:223], v[172:175]
v_cvt_pk_bf16_f32 v96, v48, v49
v_accvgpr_read_b32 v52, a34
v_accvgpr_read_b32 v53, a35
v_mfma_f32_16x16x32_bf16 v[188:191], a[172:175], a[220:223], v[188:191]
buffer_load_dwordx4 v25, s[84:87], s65 offen offset:1024 lds
v_mfma_f32_16x16x32_bf16 v[184:187], a[172:175], a[216:219], v[184:187]
ds_read_b64_tr_b16 a[188:189], v6 offset:12288
ds_read_b64_tr_b16 a[190:191], v6 offset:14336
v_accvgpr_read_b32 v54, a98
v_mfma_f32_16x16x32_bf16 v[180:183], a[172:175], a[212:215], v[180:183]
v_accvgpr_read_b32 v55, a99
v_max_f32 v52, 0, v52
v_mfma_f32_16x16x32_bf16 v[176:179], a[172:175], a[208:211], v[176:179]
v_max_f32 v53, 0, v53
v_mul_f32 v52, v52, v54
s_waitcnt lgkmcnt(8)
v_mfma_f32_16x16x32_bf16 v[192:195], a[176:179], a[208:211], v[192:195]
buffer_load_dwordx4 v26, s[84:87], s65 offen offset:2048 lds
v_mfma_f32_16x16x32_bf16 v[196:199], a[176:179], a[212:215], v[196:199]
s_waitcnt vmcnt(7)
ds_read_b64_tr_b16 a[160:161], v3 offset:0
ds_read_b64_tr_b16 a[162:163], v3 offset:2048
v_mfma_f32_16x16x32_bf16 v[200:203], a[176:179], a[216:219], v[200:203]
v_mul_f32 v53, v53, v55
v_cvt_pk_bf16_f32 v97, v52, v53
v_mfma_f32_16x16x32_bf16 v[204:207], a[176:179], a[220:223], v[204:207]
buffer_load_dwordx4 v27, s[84:87], s65 offen offset:3072 lds
v_accvgpr_read_b32 v48, a48
v_mfma_f32_16x16x32_bf16 v[220:223], a[180:183], a[220:223], v[220:223]
ds_read_b64_tr_b16 a[164:165], v4 offset:0
ds_read_b64_tr_b16 a[166:167], v4 offset:2048
v_mfma_f32_16x16x32_bf16 v[216:219], a[180:183], a[216:219], v[216:219]
v_accvgpr_read_b32 v49, a49
v_accvgpr_read_b32 v50, a112
v_accvgpr_read_b32 v51, a113
v_mfma_f32_16x16x32_bf16 v[212:215], a[180:183], a[212:215], v[212:215]
v_max_f32 v48, 0, v48
v_max_f32 v49, 0, v49
v_mfma_f32_16x16x32_bf16 v[208:211], a[180:183], a[208:211], v[208:211]
v_mul_f32 v48, v48, v50
v_mul_f32 v49, v49, v51
s_waitcnt lgkmcnt(4)
v_mfma_f32_16x16x32_bf16 v[224:227], a[184:187], a[208:211], v[224:227]
s_add_u32 m0, s53, 0x3000
ds_read_b64_tr_b16 a[168:169], v5 offset:0
v_mfma_f32_16x16x32_bf16 v[228:231], a[184:187], a[212:215], v[228:231]
buffer_load_dwordx4 v24, s[36:39], s65 offen lds
ds_read_b64_tr_b16 a[170:171], v5 offset:2048
v_mfma_f32_16x16x32_bf16 v[232:235], a[184:187], a[216:219], v[232:235]
v_cvt_pk_bf16_f32 v98, v48, v49
v_accvgpr_read_b32 v52, a50
v_mfma_f32_16x16x32_bf16 v[236:239], a[184:187], a[220:223], v[236:239]
v_accvgpr_read_b32 v53, a51
v_accvgpr_read_b32 v54, a114
v_accvgpr_read_b32 v55, a115
v_mfma_f32_16x16x32_bf16 v[252:255], a[188:191], a[220:223], v[252:255]
buffer_load_dwordx4 v25, s[36:39], s65 offen offset:1024 lds
v_mfma_f32_16x16x32_bf16 v[248:251], a[188:191], a[216:219], v[248:251]
ds_read_b64_tr_b16 a[172:173], v6 offset:0
ds_read_b64_tr_b16 a[174:175], v6 offset:2048
v_max_f32 v52, 0, v52
v_mfma_f32_16x16x32_bf16 v[244:247], a[188:191], a[212:215], v[244:247]
v_max_f32 v53, 0, v53
v_mul_f32 v52, v52, v54
v_mfma_f32_16x16x32_bf16 v[240:243], a[188:191], a[208:211], v[240:243]
v_mul_f32 v53, v53, v55
v_cvt_pk_bf16_f32 v99, v52, v53
s_waitcnt lgkmcnt(4)
v_mfma_f32_16x16x32_bf16 v[128:131], a[160:163], a[192:195], v[128:131]
buffer_load_dwordx4 v26, s[36:39], s65 offen offset:2048 lds
v_mfma_f32_16x16x32_bf16 v[132:135], a[160:163], a[196:199], v[132:135]
s_waitcnt vmcnt(7)
ds_read_b64_tr_b16 a[176:177], v3 offset:4096
ds_read_b128 a[208:211], v9 offset:4096
v_mfma_f32_16x16x32_bf16 v[136:139], a[160:163], a[200:203], v[136:139]
ds_read_b64_tr_b16 a[178:179], v3 offset:6144
ds_read_b128 a[212:215], v9 offset:5120
v_mfma_f32_16x16x32_bf16 v[140:143], a[160:163], a[204:207], v[140:143]
buffer_load_dwordx4 v27, s[36:39], s65 offen offset:3072 lds
ds_read_b128 a[216:219], v10 offset:4096
v_mfma_f32_16x16x32_bf16 v[156:159], a[164:167], a[204:207], v[156:159]
ds_read_b64_tr_b16 a[180:181], v4 offset:4096
ds_read_b128 a[220:223], v10 offset:5120
v_mfma_f32_16x16x32_bf16 v[152:155], a[164:167], a[200:203], v[152:155]
ds_read_b64_tr_b16 a[182:183], v4 offset:6144
v_accvgpr_read_b32 v48, a36
v_accvgpr_read_b32 v49, a37
v_mfma_f32_16x16x32_bf16 v[148:151], a[164:167], a[196:199], v[148:151]
v_accvgpr_read_b32 v50, a100
v_accvgpr_read_b32 v51, a101
v_mfma_f32_16x16x32_bf16 v[144:147], a[164:167], a[192:195], v[144:147]
v_max_f32 v48, 0, v48
v_max_f32 v49, 0, v49
s_waitcnt lgkmcnt(8)
v_mfma_f32_16x16x32_bf16 v[160:163], a[168:171], a[192:195], v[160:163]
s_add_u32 s65, s65, 0x90000
ds_read_b64_tr_b16 a[184:185], v5 offset:4096
v_mfma_f32_16x16x32_bf16 v[164:167], a[168:171], a[196:199], v[164:167]
s_cmp_eq_u32 s65, s73
s_cselect_b32 s65, s69, s65
ds_read_b64_tr_b16 a[186:187], v5 offset:6144
v_mfma_f32_16x16x32_bf16 v[168:171], a[168:171], a[200:203], v[168:171]
s_add_u32 m0, s53, 0
v_mul_f32 v48, v48, v50
v_mfma_f32_16x16x32_bf16 v[172:175], a[168:171], a[204:207], v[172:175]
buffer_load_dwordx4 v20, s[84:87], s65 offen lds
v_mul_f32 v49, v49, v51
v_mfma_f32_16x16x32_bf16 v[188:191], a[172:175], a[204:207], v[188:191]
ds_read_b64_tr_b16 a[188:189], v6 offset:4096
ds_read_b64_tr_b16 a[190:191], v6 offset:6144
v_mfma_f32_16x16x32_bf16 v[184:187], a[172:175], a[200:203], v[184:187]
v_cvt_pk_bf16_f32 v100, v48, v49
v_accvgpr_read_b32 v52, a38
v_accvgpr_read_b32 v53, a39
v_mfma_f32_16x16x32_bf16 v[180:183], a[172:175], a[196:199], v[180:183]
buffer_load_dwordx4 v21, s[84:87], s65 offen offset:1024 lds
v_mfma_f32_16x16x32_bf16 v[176:179], a[172:175], a[192:195], v[176:179]
v_accvgpr_read_b32 v54, a102
v_accvgpr_read_b32 v55, a103
s_waitcnt lgkmcnt(4)
v_mfma_f32_16x16x32_bf16 v[192:195], a[176:179], a[192:195], v[192:195]
s_waitcnt vmcnt(6)
ds_read_b64_tr_b16 a[160:161], v3 offset:8192
v_mfma_f32_16x16x32_bf16 v[196:199], a[176:179], a[196:199], v[196:199]
buffer_load_dwordx4 v22, s[84:87], s65 offen offset:2048 lds
ds_read_b64_tr_b16 a[162:163], v3 offset:10240
v_mfma_f32_16x16x32_bf16 v[200:203], a[176:179], a[200:203], v[200:203]
v_max_f32 v52, 0, v52
v_max_f32 v53, 0, v53
v_mfma_f32_16x16x32_bf16 v[204:207], a[176:179], a[204:207], v[204:207]
v_mul_f32 v52, v52, v54
v_mul_f32 v53, v53, v55
v_cvt_pk_bf16_f32 v101, v52, v53
v_mfma_f32_16x16x32_bf16 v[220:223], a[180:183], a[204:207], v[220:223]
buffer_load_dwordx4 v23, s[84:87], s65 offen offset:3072 lds
v_mfma_f32_16x16x32_bf16 v[216:219], a[180:183], a[200:203], v[216:219]
ds_read_b64_tr_b16 a[164:165], v4 offset:8192
ds_read_b64_tr_b16 a[166:167], v4 offset:10240
v_accvgpr_read_b32 v48, a52
)ASM"
R"ASM(
v_mfma_f32_16x16x32_bf16 v[212:215], a[180:183], a[196:199], v[212:215]
v_accvgpr_read_b32 v49, a53
v_accvgpr_read_b32 v50, a116
v_mfma_f32_16x16x32_bf16 v[208:211], a[180:183], a[192:195], v[208:211]
v_accvgpr_read_b32 v51, a117
v_max_f32 v48, 0, v48
s_waitcnt lgkmcnt(4)
v_mfma_f32_16x16x32_bf16 v[224:227], a[184:187], a[192:195], v[224:227]
s_add_u32 m0, s53, 0x1000
ds_read_b64_tr_b16 a[168:169], v5 offset:8192
v_mfma_f32_16x16x32_bf16 v[228:231], a[184:187], a[196:199], v[228:231]
buffer_load_dwordx4 v20, s[36:39], s65 offen lds
ds_read_b64_tr_b16 a[170:171], v5 offset:10240
v_mfma_f32_16x16x32_bf16 v[232:235], a[184:187], a[200:203], v[232:235]
v_max_f32 v49, 0, v49
v_mul_f32 v48, v48, v50
v_mfma_f32_16x16x32_bf16 v[236:239], a[184:187], a[204:207], v[236:239]
v_mul_f32 v49, v49, v51
v_cvt_pk_bf16_f32 v102, v48, v49
v_accvgpr_read_b32 v52, a54
v_mfma_f32_16x16x32_bf16 v[252:255], a[188:191], a[204:207], v[252:255]
buffer_load_dwordx4 v21, s[36:39], s65 offen offset:1024 lds
v_mfma_f32_16x16x32_bf16 v[248:251], a[188:191], a[200:203], v[248:251]
ds_read_b64_tr_b16 a[172:173], v6 offset:8192
ds_read_b64_tr_b16 a[174:175], v6 offset:10240
v_accvgpr_read_b32 v53, a55
v_mfma_f32_16x16x32_bf16 v[244:247], a[188:191], a[196:199], v[244:247]
v_accvgpr_read_b32 v54, a118
v_accvgpr_read_b32 v55, a119
v_mfma_f32_16x16x32_bf16 v[240:243], a[188:191], a[192:195], v[240:243]
v_max_f32 v52, 0, v52
v_max_f32 v53, 0, v53
s_waitcnt lgkmcnt(4)
v_mfma_f32_16x16x32_bf16 v[128:131], a[160:163], a[208:211], v[128:131]
buffer_load_dwordx4 v22, s[36:39], s65 offen offset:2048 lds
v_mfma_f32_16x16x32_bf16 v[132:135], a[160:163], a[212:215], v[132:135]
s_waitcnt vmcnt(7)
ds_read_b64_tr_b16 a[176:177], v3 offset:12288
s_add_u32 s55, s55, 0x2000
v_mfma_f32_16x16x32_bf16 v[136:139], a[160:163], a[216:219], v[136:139]
ds_read_b64_tr_b16 a[178:179], v3 offset:14336
v_mfma_f32_16x16x32_bf16 v[140:143], a[160:163], a[220:223], v[140:143]
buffer_load_dwordx4 v23, s[36:39], s65 offen offset:3072 lds
v_mfma_f32_16x16x32_bf16 v[156:159], a[164:167], a[220:223], v[156:159]
ds_read_b64_tr_b16 a[180:181], v4 offset:12288
ds_read_b64_tr_b16 a[182:183], v4 offset:14336
v_mfma_f32_16x16x32_bf16 v[152:155], a[164:167], a[216:219], v[152:155]
s_cmp_eq_u32 s55, 0x12000
s_cselect_b32 s55, 0, s55
v_add_u32 v9, s55, v14
v_mfma_f32_16x16x32_bf16 v[148:151], a[164:167], a[212:215], v[148:151]
v_add_u32 v10, s55, v15
ds_read_b128 a[192:195], v9 offset:0
v_mfma_f32_16x16x32_bf16 v[144:147], a[164:167], a[208:211], v[144:147]
ds_read_b128 a[196:199], v9 offset:1024
ds_read_b128 a[200:203], v10 offset:0
s_waitcnt lgkmcnt(7)
v_mfma_f32_16x16x32_bf16 v[160:163], a[168:171], a[208:211], v[160:163]
s_add_u32 m0, s53, 0x2000
ds_read_b64_tr_b16 a[184:185], v5 offset:12288
v_mfma_f32_16x16x32_bf16 v[164:167], a[168:171], a[212:215], v[164:167]
buffer_load_dwordx4 v24, s[84:87], s65 offen lds
ds_read_b128 a[204:207], v10 offset:1024
v_mfma_f32_16x16x32_bf16 v[168:171], a[168:171], a[216:219], v[168:171]
ds_read_b64_tr_b16 a[186:187], v5 offset:14336
v_mul_f32 v52, v52, v54
v_mfma_f32_16x16x32_bf16 v[172:175], a[168:171], a[220:223], v[172:175]
v_mul_f32 v53, v53, v55
v_cvt_pk_bf16_f32 v103, v52, v53
v_accvgpr_read_b32 v48, a40
v_mfma_f32_16x16x32_bf16 v[188:191], a[172:175], a[220:223], v[188:191]
buffer_load_dwordx4 v25, s[84:87], s65 offen offset:1024 lds
v_mfma_f32_16x16x32_bf16 v[184:187], a[172:175], a[216:219], v[184:187]
ds_read_b64_tr_b16 a[188:189], v6 offset:12288
ds_read_b64_tr_b16 a[190:191], v6 offset:14336
v_accvgpr_read_b32 v49, a41
v_mfma_f32_16x16x32_bf16 v[180:183], a[172:175], a[212:215], v[180:183]
v_accvgpr_read_b32 v50, a104
v_accvgpr_read_b32 v51, a105
v_mfma_f32_16x16x32_bf16 v[176:179], a[172:175], a[208:211], v[176:179]
v_max_f32 v48, 0, v48
v_max_f32 v49, 0, v49
s_waitcnt lgkmcnt(8)
v_mfma_f32_16x16x32_bf16 v[192:195], a[176:179], a[208:211], v[192:195]
buffer_load_dwordx4 v26, s[84:87], s65 offen offset:2048 lds
v_mfma_f32_16x16x32_bf16 v[196:199], a[176:179], a[212:215], v[196:199]
s_waitcnt vmcnt(7)
ds_read_b64_tr_b16 a[160:161], v3 offset:0
ds_read_b64_tr_b16 a[162:163], v3 offset:2048
v_mfma_f32_16x16x32_bf16 v[200:203], a[176:179], a[216:219], v[200:203]
v_mul_f32 v48, v48, v50
v_mul_f32 v49, v49, v51
v_mfma_f32_16x16x32_bf16 v[204:207], a[176:179], a[220:223], v[204:207]
buffer_load_dwordx4 v27, s[84:87], s65 offen offset:3072 lds
v_cvt_pk_bf16_f32 v104, v48, v49
v_mfma_f32_16x16x32_bf16 v[220:223], a[180:183], a[220:223], v[220:223]
ds_read_b64_tr_b16 a[164:165], v4 offset:0
ds_read_b64_tr_b16 a[166:167], v4 offset:2048
v_mfma_f32_16x16x32_bf16 v[216:219], a[180:183], a[216:219], v[216:219]
v_accvgpr_read_b32 v52, a42
v_accvgpr_read_b32 v53, a43
v_accvgpr_read_b32 v54, a106
v_mfma_f32_16x16x32_bf16 v[212:215], a[180:183], a[212:215], v[212:215]
v_accvgpr_read_b32 v55, a107
v_max_f32 v52, 0, v52
v_mfma_f32_16x16x32_bf16 v[208:211], a[180:183], a[208:211], v[208:211]
v_max_f32 v53, 0, v53
v_mul_f32 v52, v52, v54
s_waitcnt lgkmcnt(4)
v_mfma_f32_16x16x32_bf16 v[224:227], a[184:187], a[208:211], v[224:227]
s_add_u32 m0, s53, 0x3000
ds_read_b64_tr_b16 a[168:169], v5 offset:0
v_mfma_f32_16x16x32_bf16 v[228:231], a[184:187], a[212:215], v[228:231]
buffer_load_dwordx4 v24, s[36:39], s65 offen lds
ds_read_b64_tr_b16 a[170:171], v5 offset:2048
v_mfma_f32_16x16x32_bf16 v[232:235], a[184:187], a[216:219], v[232:235]
v_mul_f32 v53, v53, v55
v_cvt_pk_bf16_f32 v105, v52, v53
v_mfma_f32_16x16x32_bf16 v[236:239], a[184:187], a[220:223], v[236:239]
v_accvgpr_read_b32 v48, a56
v_accvgpr_read_b32 v49, a57
v_accvgpr_read_b32 v50, a120
v_mfma_f32_16x16x32_bf16 v[252:255], a[188:191], a[220:223], v[252:255]
buffer_load_dwordx4 v25, s[36:39], s65 offen offset:1024 lds
v_mfma_f32_16x16x32_bf16 v[248:251], a[188:191], a[216:219], v[248:251]
ds_read_b64_tr_b16 a[172:173], v6 offset:0
ds_read_b64_tr_b16 a[174:175], v6 offset:2048
v_accvgpr_read_b32 v51, a121
v_mfma_f32_16x16x32_bf16 v[244:247], a[188:191], a[212:215], v[244:247]
v_max_f32 v48, 0, v48
v_max_f32 v49, 0, v49
v_mfma_f32_16x16x32_bf16 v[240:243], a[188:191], a[208:211], v[240:243]
v_mul_f32 v48, v48, v50
v_mul_f32 v49, v49, v51
s_waitcnt lgkmcnt(4)
v_mfma_f32_16x16x32_bf16 v[128:131], a[160:163], a[192:195], v[128:131]
buffer_load_dwordx4 v26, s[36:39], s65 offen offset:2048 lds
v_mfma_f32_16x16x32_bf16 v[132:135], a[160:163], a[196:199], v[132:135]
s_waitcnt vmcnt(7)
ds_read_b64_tr_b16 a[176:177], v3 offset:4096
ds_read_b128 a[208:211], v9 offset:4096
v_mfma_f32_16x16x32_bf16 v[136:139], a[160:163], a[200:203], v[136:139]
ds_read_b64_tr_b16 a[178:179], v3 offset:6144
ds_read_b128 a[212:215], v9 offset:5120
v_mfma_f32_16x16x32_bf16 v[140:143], a[160:163], a[204:207], v[140:143]
buffer_load_dwordx4 v27, s[36:39], s65 offen offset:3072 lds
ds_read_b128 a[216:219], v10 offset:4096
v_mfma_f32_16x16x32_bf16 v[156:159], a[164:167], a[204:207], v[156:159]
ds_read_b64_tr_b16 a[180:181], v4 offset:4096
ds_read_b128 a[220:223], v10 offset:5120
v_mfma_f32_16x16x32_bf16 v[152:155], a[164:167], a[200:203], v[152:155]
ds_read_b64_tr_b16 a[182:183], v4 offset:6144
v_cvt_pk_bf16_f32 v106, v48, v49
v_accvgpr_read_b32 v52, a58
v_mfma_f32_16x16x32_bf16 v[148:151], a[164:167], a[196:199], v[148:151]
v_accvgpr_read_b32 v53, a59
v_accvgpr_read_b32 v54, a122
v_mfma_f32_16x16x32_bf16 v[144:147], a[164:167], a[192:195], v[144:147]
v_accvgpr_read_b32 v55, a123
v_max_f32 v52, 0, v52
s_waitcnt lgkmcnt(8)
v_mfma_f32_16x16x32_bf16 v[160:163], a[168:171], a[192:195], v[160:163]
s_add_u32 s65, s65, 0x90000
ds_read_b64_tr_b16 a[184:185], v5 offset:4096
v_mfma_f32_16x16x32_bf16 v[164:167], a[168:171], a[196:199], v[164:167]
s_cmp_eq_u32 s65, s73
s_cselect_b32 s65, s69, s65
ds_read_b64_tr_b16 a[186:187], v5 offset:6144
v_mfma_f32_16x16x32_bf16 v[168:171], a[168:171], a[200:203], v[168:171]
s_add_u32 m0, s53, 0
v_max_f32 v53, 0, v53
v_mfma_f32_16x16x32_bf16 v[172:175], a[168:171], a[204:207], v[172:175]
buffer_load_dwordx4 v20, s[84:87], s65 offen lds
v_mul_f32 v52, v52, v54
v_mfma_f32_16x16x32_bf16 v[188:191], a[172:175], a[204:207], v[188:191]
ds_read_b64_tr_b16 a[188:189], v6 offset:4096
ds_read_b64_tr_b16 a[190:191], v6 offset:6144
v_mfma_f32_16x16x32_bf16 v[184:187], a[172:175], a[200:203], v[184:187]
v_mul_f32 v53, v53, v55
v_cvt_pk_bf16_f32 v107, v52, v53
v_accvgpr_read_b32 v48, a44
v_mfma_f32_16x16x32_bf16 v[180:183], a[172:175], a[196:199], v[180:183]
buffer_load_dwordx4 v21, s[84:87], s65 offen offset:1024 lds
v_mfma_f32_16x16x32_bf16 v[176:179], a[172:175], a[192:195], v[176:179]
v_accvgpr_read_b32 v49, a45
v_accvgpr_read_b32 v50, a108
s_waitcnt lgkmcnt(4)
v_mfma_f32_16x16x32_bf16 v[192:195], a[176:179], a[192:195], v[192:195]
s_waitcnt vmcnt(6)
ds_read_b64_tr_b16 a[160:161], v3 offset:8192
v_mfma_f32_16x16x32_bf16 v[196:199], a[176:179], a[196:199], v[196:199]
buffer_load_dwordx4 v22, s[84:87], s65 offen offset:2048 lds
ds_read_b64_tr_b16 a[162:163], v3 offset:10240
v_mfma_f32_16x16x32_bf16 v[200:203], a[176:179], a[200:203], v[200:203]
v_accvgpr_read_b32 v51, a109
v_max_f32 v48, 0, v48
v_mfma_f32_16x16x32_bf16 v[204:207], a[176:179], a[204:207], v[204:207]
v_max_f32 v49, 0, v49
v_mul_f32 v48, v48, v50
v_mul_f32 v49, v49, v51
)ASM"
R"ASM(
v_mfma_f32_16x16x32_bf16 v[220:223], a[180:183], a[204:207], v[220:223]
buffer_load_dwordx4 v23, s[84:87], s65 offen offset:3072 lds
v_mfma_f32_16x16x32_bf16 v[216:219], a[180:183], a[200:203], v[216:219]
ds_read_b64_tr_b16 a[164:165], v4 offset:8192
ds_read_b64_tr_b16 a[166:167], v4 offset:10240
v_cvt_pk_bf16_f32 v108, v48, v49
v_mfma_f32_16x16x32_bf16 v[212:215], a[180:183], a[196:199], v[212:215]
v_accvgpr_read_b32 v52, a46
v_accvgpr_read_b32 v53, a47
v_mfma_f32_16x16x32_bf16 v[208:211], a[180:183], a[192:195], v[208:211]
v_accvgpr_read_b32 v54, a110
v_accvgpr_read_b32 v55, a111
s_waitcnt lgkmcnt(4)
v_mfma_f32_16x16x32_bf16 v[224:227], a[184:187], a[192:195], v[224:227]
s_add_u32 m0, s53, 0x1000
ds_read_b64_tr_b16 a[168:169], v5 offset:8192
v_mfma_f32_16x16x32_bf16 v[228:231], a[184:187], a[196:199], v[228:231]
buffer_load_dwordx4 v20, s[36:39], s65 offen lds
ds_read_b64_tr_b16 a[170:171], v5 offset:10240
v_mfma_f32_16x16x32_bf16 v[232:235], a[184:187], a[200:203], v[232:235]
v_max_f32 v52, 0, v52
v_max_f32 v53, 0, v53
v_mfma_f32_16x16x32_bf16 v[236:239], a[184:187], a[204:207], v[236:239]
v_mul_f32 v52, v52, v54
v_mul_f32 v53, v53, v55
v_cvt_pk_bf16_f32 v109, v52, v53
v_mfma_f32_16x16x32_bf16 v[252:255], a[188:191], a[204:207], v[252:255]
buffer_load_dwordx4 v21, s[36:39], s65 offen offset:1024 lds
v_mfma_f32_16x16x32_bf16 v[248:251], a[188:191], a[200:203], v[248:251]
ds_read_b64_tr_b16 a[172:173], v6 offset:8192
ds_read_b64_tr_b16 a[174:175], v6 offset:10240
v_accvgpr_read_b32 v48, a60
v_mfma_f32_16x16x32_bf16 v[244:247], a[188:191], a[196:199], v[244:247]
v_accvgpr_read_b32 v49, a61
v_accvgpr_read_b32 v50, a124
v_mfma_f32_16x16x32_bf16 v[240:243], a[188:191], a[192:195], v[240:243]
v_accvgpr_read_b32 v51, a125
v_max_f32 v48, 0, v48
s_waitcnt lgkmcnt(4)
v_mfma_f32_16x16x32_bf16 v[128:131], a[160:163], a[208:211], v[128:131]
buffer_load_dwordx4 v22, s[36:39], s65 offen offset:2048 lds
v_mfma_f32_16x16x32_bf16 v[132:135], a[160:163], a[212:215], v[132:135]
s_waitcnt vmcnt(7)
ds_read_b64_tr_b16 a[176:177], v3 offset:12288
s_add_u32 s55, s55, 0x2000
v_mfma_f32_16x16x32_bf16 v[136:139], a[160:163], a[216:219], v[136:139]
ds_read_b64_tr_b16 a[178:179], v3 offset:14336
v_mfma_f32_16x16x32_bf16 v[140:143], a[160:163], a[220:223], v[140:143]
buffer_load_dwordx4 v23, s[36:39], s65 offen offset:3072 lds
v_mfma_f32_16x16x32_bf16 v[156:159], a[164:167], a[220:223], v[156:159]
ds_read_b64_tr_b16 a[180:181], v4 offset:12288
ds_read_b64_tr_b16 a[182:183], v4 offset:14336
v_mfma_f32_16x16x32_bf16 v[152:155], a[164:167], a[216:219], v[152:155]
s_cmp_eq_u32 s55, 0x12000
s_cselect_b32 s55, 0, s55
v_add_u32 v9, s55, v14
v_mfma_f32_16x16x32_bf16 v[148:151], a[164:167], a[212:215], v[148:151]
v_add_u32 v10, s55, v15
ds_read_b128 a[192:195], v9 offset:0
v_mfma_f32_16x16x32_bf16 v[144:147], a[164:167], a[208:211], v[144:147]
ds_read_b128 a[196:199], v9 offset:1024
ds_read_b128 a[200:203], v10 offset:0
s_waitcnt lgkmcnt(7)
v_mfma_f32_16x16x32_bf16 v[160:163], a[168:171], a[208:211], v[160:163]
s_add_u32 m0, s53, 0x2000
ds_read_b64_tr_b16 a[184:185], v5 offset:12288
v_mfma_f32_16x16x32_bf16 v[164:167], a[168:171], a[212:215], v[164:167]
buffer_load_dwordx4 v24, s[84:87], s65 offen lds
ds_read_b128 a[204:207], v10 offset:1024
v_mfma_f32_16x16x32_bf16 v[168:171], a[168:171], a[216:219], v[168:171]
ds_read_b64_tr_b16 a[186:187], v5 offset:14336
v_max_f32 v49, 0, v49
v_mfma_f32_16x16x32_bf16 v[172:175], a[168:171], a[220:223], v[172:175]
v_mul_f32 v48, v48, v50
v_mul_f32 v49, v49, v51
v_cvt_pk_bf16_f32 v110, v48, v49
v_mfma_f32_16x16x32_bf16 v[188:191], a[172:175], a[220:223], v[188:191]
buffer_load_dwordx4 v25, s[84:87], s65 offen offset:1024 lds
v_mfma_f32_16x16x32_bf16 v[184:187], a[172:175], a[216:219], v[184:187]
ds_read_b64_tr_b16 a[188:189], v6 offset:12288
ds_read_b64_tr_b16 a[190:191], v6 offset:14336
v_accvgpr_read_b32 v52, a62
v_mfma_f32_16x16x32_bf16 v[180:183], a[172:175], a[212:215], v[180:183]
v_accvgpr_read_b32 v53, a63
v_accvgpr_read_b32 v54, a126
v_mfma_f32_16x16x32_bf16 v[176:179], a[172:175], a[208:211], v[176:179]
v_accvgpr_read_b32 v55, a127
v_max_f32 v52, 0, v52
s_waitcnt lgkmcnt(8)
v_mfma_f32_16x16x32_bf16 v[192:195], a[176:179], a[208:211], v[192:195]
buffer_load_dwordx4 v26, s[84:87], s65 offen offset:2048 lds
v_mfma_f32_16x16x32_bf16 v[196:199], a[176:179], a[212:215], v[196:199]
s_waitcnt vmcnt(7)
ds_read_b64_tr_b16 a[160:161], v3 offset:0
ds_read_b64_tr_b16 a[162:163], v3 offset:2048
v_mfma_f32_16x16x32_bf16 v[200:203], a[176:179], a[216:219], v[200:203]
v_max_f32 v53, 0, v53
v_mul_f32 v52, v52, v54
v_mfma_f32_16x16x32_bf16 v[204:207], a[176:179], a[220:223], v[204:207]
buffer_load_dwordx4 v27, s[84:87], s65 offen offset:3072 lds
v_mul_f32 v53, v53, v55
v_mfma_f32_16x16x32_bf16 v[220:223], a[180:183], a[220:223], v[220:223]
ds_read_b64_tr_b16 a[164:165], v4 offset:0
ds_read_b64_tr_b16 a[166:167], v4 offset:2048
v_mfma_f32_16x16x32_bf16 v[216:219], a[180:183], a[216:219], v[216:219]
v_cvt_pk_bf16_f32 v111, v52, v53
v_accvgpr_read_b32 v48, a128
v_accvgpr_read_b32 v49, a129
v_mfma_f32_16x16x32_bf16 v[212:215], a[180:183], a[212:215], v[212:215]
v_accvgpr_read_b32 v50, a144
v_accvgpr_read_b32 v51, a145
v_mfma_f32_16x16x32_bf16 v[208:211], a[180:183], a[208:211], v[208:211]
v_max_f32 v48, 0, v48
v_max_f32 v49, 0, v49
s_waitcnt lgkmcnt(4)
v_mfma_f32_16x16x32_bf16 v[224:227], a[184:187], a[208:211], v[224:227]
s_add_u32 m0, s53, 0x3000
ds_read_b64_tr_b16 a[168:169], v5 offset:0
v_mfma_f32_16x16x32_bf16 v[228:231], a[184:187], a[212:215], v[228:231]
buffer_load_dwordx4 v24, s[36:39], s65 offen lds
ds_read_b64_tr_b16 a[170:171], v5 offset:2048
v_mfma_f32_16x16x32_bf16 v[232:235], a[184:187], a[216:219], v[232:235]
v_mul_f32 v48, v48, v50
v_mul_f32 v49, v49, v51
v_mfma_f32_16x16x32_bf16 v[236:239], a[184:187], a[220:223], v[236:239]
v_cvt_pk_bf16_f32 v112, v48, v49
v_accvgpr_read_b32 v52, a130
v_accvgpr_read_b32 v53, a131
v_mfma_f32_16x16x32_bf16 v[252:255], a[188:191], a[220:223], v[252:255]
buffer_load_dwordx4 v25, s[36:39], s65 offen offset:1024 lds
v_mfma_f32_16x16x32_bf16 v[248:251], a[188:191], a[216:219], v[248:251]
ds_read_b64_tr_b16 a[172:173], v6 offset:0
ds_read_b64_tr_b16 a[174:175], v6 offset:2048
v_accvgpr_read_b32 v54, a146
v_mfma_f32_16x16x32_bf16 v[244:247], a[188:191], a[212:215], v[244:247]
v_accvgpr_read_b32 v55, a147
v_max_f32 v52, 0, v52
v_mfma_f32_16x16x32_bf16 v[240:243], a[188:191], a[208:211], v[240:243]
v_max_f32 v53, 0, v53
v_mul_f32 v52, v52, v54
s_waitcnt lgkmcnt(4)
v_mfma_f32_16x16x32_bf16 v[128:131], a[160:163], a[192:195], v[128:131]
buffer_load_dwordx4 v26, s[36:39], s65 offen offset:2048 lds
v_mfma_f32_16x16x32_bf16 v[132:135], a[160:163], a[196:199], v[132:135]
s_waitcnt vmcnt(7)
ds_read_b64_tr_b16 a[176:177], v3 offset:4096
ds_read_b128 a[208:211], v9 offset:4096
v_mfma_f32_16x16x32_bf16 v[136:139], a[160:163], a[200:203], v[136:139]
ds_read_b64_tr_b16 a[178:179], v3 offset:6144
ds_read_b128 a[212:215], v9 offset:5120
v_mfma_f32_16x16x32_bf16 v[140:143], a[160:163], a[204:207], v[140:143]
buffer_load_dwordx4 v27, s[36:39], s65 offen offset:3072 lds
ds_read_b128 a[216:219], v10 offset:4096
v_mfma_f32_16x16x32_bf16 v[156:159], a[164:167], a[204:207], v[156:159]
ds_read_b64_tr_b16 a[180:181], v4 offset:4096
ds_read_b128 a[220:223], v10 offset:5120
v_mfma_f32_16x16x32_bf16 v[152:155], a[164:167], a[200:203], v[152:155]
ds_read_b64_tr_b16 a[182:183], v4 offset:6144
v_mul_f32 v53, v53, v55
v_cvt_pk_bf16_f32 v113, v52, v53
v_mfma_f32_16x16x32_bf16 v[148:151], a[164:167], a[196:199], v[148:151]
v_accvgpr_read_b32 v48, a136
v_accvgpr_read_b32 v49, a137
v_mfma_f32_16x16x32_bf16 v[144:147], a[164:167], a[192:195], v[144:147]
v_accvgpr_read_b32 v50, a152
v_accvgpr_read_b32 v51, a153
s_waitcnt lgkmcnt(8)
v_mfma_f32_16x16x32_bf16 v[160:163], a[168:171], a[192:195], v[160:163]
s_add_u32 s65, s65, 0x90000
ds_read_b64_tr_b16 a[184:185], v5 offset:4096
v_mfma_f32_16x16x32_bf16 v[164:167], a[168:171], a[196:199], v[164:167]
s_cmp_eq_u32 s65, s73
s_cselect_b32 s65, s69, s65
ds_read_b64_tr_b16 a[186:187], v5 offset:6144
v_mfma_f32_16x16x32_bf16 v[168:171], a[168:171], a[200:203], v[168:171]
s_add_u32 m0, s53, 0
v_max_f32 v48, 0, v48
v_mfma_f32_16x16x32_bf16 v[172:175], a[168:171], a[204:207], v[172:175]
buffer_load_dwordx4 v20, s[84:87], s65 offen lds
v_max_f32 v49, 0, v49
v_mfma_f32_16x16x32_bf16 v[188:191], a[172:175], a[204:207], v[188:191]
ds_read_b64_tr_b16 a[188:189], v6 offset:4096
ds_read_b64_tr_b16 a[190:191], v6 offset:6144
v_mfma_f32_16x16x32_bf16 v[184:187], a[172:175], a[200:203], v[184:187]
v_mul_f32 v48, v48, v50
v_mul_f32 v49, v49, v51
v_cvt_pk_bf16_f32 v114, v48, v49
v_mfma_f32_16x16x32_bf16 v[180:183], a[172:175], a[196:199], v[180:183]
buffer_load_dwordx4 v21, s[84:87], s65 offen offset:1024 lds
v_mfma_f32_16x16x32_bf16 v[176:179], a[172:175], a[192:195], v[176:179]
v_accvgpr_read_b32 v52, a138
v_accvgpr_read_b32 v53, a139
s_waitcnt lgkmcnt(4)
v_mfma_f32_16x16x32_bf16 v[192:195], a[176:179], a[192:195], v[192:195]
s_waitcnt vmcnt(6)
ds_read_b64_tr_b16 a[160:161], v3 offset:8192
v_mfma_f32_16x16x32_bf16 v[196:199], a[176:179], a[196:199], v[196:199]
buffer_load_dwordx4 v22, s[84:87], s65 offen offset:2048 lds
ds_read_b64_tr_b16 a[162:163], v3 offset:10240
v_mfma_f32_16x16x32_bf16 v[200:203], a[176:179], a[200:203], v[200:203]
)ASM"
R"ASM(
v_accvgpr_read_b32 v54, a154
v_accvgpr_read_b32 v55, a155
v_mfma_f32_16x16x32_bf16 v[204:207], a[176:179], a[204:207], v[204:207]
v_max_f32 v52, 0, v52
v_max_f32 v53, 0, v53
v_mul_f32 v52, v52, v54
v_mfma_f32_16x16x32_bf16 v[220:223], a[180:183], a[204:207], v[220:223]
buffer_load_dwordx4 v23, s[84:87], s65 offen offset:3072 lds
v_mfma_f32_16x16x32_bf16 v[216:219], a[180:183], a[200:203], v[216:219]
ds_read_b64_tr_b16 a[164:165], v4 offset:8192
ds_read_b64_tr_b16 a[166:167], v4 offset:10240
v_mul_f32 v53, v53, v55
v_mfma_f32_16x16x32_bf16 v[212:215], a[180:183], a[196:199], v[212:215]
v_cvt_pk_bf16_f32 v115, v52, v53
v_accvgpr_read_b32 v48, a132
v_mfma_f32_16x16x32_bf16 v[208:211], a[180:183], a[192:195], v[208:211]
v_accvgpr_read_b32 v49, a133
v_accvgpr_read_b32 v50, a148
s_waitcnt lgkmcnt(4)
v_mfma_f32_16x16x32_bf16 v[224:227], a[184:187], a[192:195], v[224:227]
s_add_u32 m0, s53, 0x1000
ds_read_b64_tr_b16 a[168:169], v5 offset:8192
v_mfma_f32_16x16x32_bf16 v[228:231], a[184:187], a[196:199], v[228:231]
buffer_load_dwordx4 v20, s[36:39], s65 offen lds
ds_read_b64_tr_b16 a[170:171], v5 offset:10240
v_mfma_f32_16x16x32_bf16 v[232:235], a[184:187], a[200:203], v[232:235]
v_accvgpr_read_b32 v51, a149
v_max_f32 v48, 0, v48
v_mfma_f32_16x16x32_bf16 v[236:239], a[184:187], a[204:207], v[236:239]
v_max_f32 v49, 0, v49
v_mul_f32 v48, v48, v50
v_mul_f32 v49, v49, v51
v_mfma_f32_16x16x32_bf16 v[252:255], a[188:191], a[204:207], v[252:255]
buffer_load_dwordx4 v21, s[36:39], s65 offen offset:1024 lds
v_mfma_f32_16x16x32_bf16 v[248:251], a[188:191], a[200:203], v[248:251]
ds_read_b64_tr_b16 a[172:173], v6 offset:8192
ds_read_b64_tr_b16 a[174:175], v6 offset:10240
v_cvt_pk_bf16_f32 v116, v48, v49
v_mfma_f32_16x16x32_bf16 v[244:247], a[188:191], a[196:199], v[244:247]
v_accvgpr_read_b32 v52, a134
v_accvgpr_read_b32 v53, a135
v_mfma_f32_16x16x32_bf16 v[240:243], a[188:191], a[192:195], v[240:243]
v_accvgpr_read_b32 v54, a150
v_accvgpr_read_b32 v55, a151
s_waitcnt lgkmcnt(4)
v_mfma_f32_16x16x32_bf16 v[128:131], a[160:163], a[208:211], v[128:131]
buffer_load_dwordx4 v22, s[36:39], s65 offen offset:2048 lds
v_mfma_f32_16x16x32_bf16 v[132:135], a[160:163], a[212:215], v[132:135]
s_waitcnt vmcnt(7)
ds_read_b64_tr_b16 a[176:177], v3 offset:12288
s_add_u32 s55, s55, 0x2000
v_mfma_f32_16x16x32_bf16 v[136:139], a[160:163], a[216:219], v[136:139]
ds_read_b64_tr_b16 a[178:179], v3 offset:14336
v_mfma_f32_16x16x32_bf16 v[140:143], a[160:163], a[220:223], v[140:143]
buffer_load_dwordx4 v23, s[36:39], s65 offen offset:3072 lds
v_mfma_f32_16x16x32_bf16 v[156:159], a[164:167], a[220:223], v[156:159]
ds_read_b64_tr_b16 a[180:181], v4 offset:12288
ds_read_b64_tr_b16 a[182:183], v4 offset:14336
v_mfma_f32_16x16x32_bf16 v[152:155], a[164:167], a[216:219], v[152:155]
s_cmp_eq_u32 s55, 0x12000
s_cselect_b32 s55, 0, s55
v_add_u32 v9, s55, v14
v_mfma_f32_16x16x32_bf16 v[148:151], a[164:167], a[212:215], v[148:151]
v_add_u32 v10, s55, v15
ds_read_b128 a[192:195], v9 offset:0
v_mfma_f32_16x16x32_bf16 v[144:147], a[164:167], a[208:211], v[144:147]
ds_read_b128 a[196:199], v9 offset:1024
ds_read_b128 a[200:203], v10 offset:0
s_waitcnt lgkmcnt(7)
v_mfma_f32_16x16x32_bf16 v[160:163], a[168:171], a[208:211], v[160:163]
s_add_u32 m0, s53, 0x2000
ds_read_b64_tr_b16 a[184:185], v5 offset:12288
v_mfma_f32_16x16x32_bf16 v[164:167], a[168:171], a[212:215], v[164:167]
buffer_load_dwordx4 v24, s[84:87], s65 offen lds
ds_read_b128 a[204:207], v10 offset:1024
v_mfma_f32_16x16x32_bf16 v[168:171], a[168:171], a[216:219], v[168:171]
ds_read_b64_tr_b16 a[186:187], v5 offset:14336
v_max_f32 v52, 0, v52
v_mfma_f32_16x16x32_bf16 v[172:175], a[168:171], a[220:223], v[172:175]
v_max_f32 v53, 0, v53
v_mul_f32 v52, v52, v54
v_mul_f32 v53, v53, v55
v_mfma_f32_16x16x32_bf16 v[188:191], a[172:175], a[220:223], v[188:191]
buffer_load_dwordx4 v25, s[84:87], s65 offen offset:1024 lds
v_mfma_f32_16x16x32_bf16 v[184:187], a[172:175], a[216:219], v[184:187]
ds_read_b64_tr_b16 a[188:189], v6 offset:12288
ds_read_b64_tr_b16 a[190:191], v6 offset:14336
v_cvt_pk_bf16_f32 v117, v52, v53
v_mfma_f32_16x16x32_bf16 v[180:183], a[172:175], a[212:215], v[180:183]
v_accvgpr_read_b32 v48, a140
v_accvgpr_read_b32 v49, a141
v_mfma_f32_16x16x32_bf16 v[176:179], a[172:175], a[208:211], v[176:179]
v_accvgpr_read_b32 v50, a156
v_accvgpr_read_b32 v51, a157
s_waitcnt lgkmcnt(8)
v_mfma_f32_16x16x32_bf16 v[192:195], a[176:179], a[208:211], v[192:195]
buffer_load_dwordx4 v26, s[84:87], s65 offen offset:2048 lds
v_mfma_f32_16x16x32_bf16 v[196:199], a[176:179], a[212:215], v[196:199]
s_waitcnt vmcnt(7)
ds_read_b64_tr_b16 a[160:161], v3 offset:0
ds_read_b64_tr_b16 a[162:163], v3 offset:2048
v_mfma_f32_16x16x32_bf16 v[200:203], a[176:179], a[216:219], v[200:203]
v_max_f32 v48, 0, v48
v_max_f32 v49, 0, v49
v_mfma_f32_16x16x32_bf16 v[204:207], a[176:179], a[220:223], v[204:207]
buffer_load_dwordx4 v27, s[84:87], s65 offen offset:3072 lds
v_mul_f32 v48, v48, v50
v_mfma_f32_16x16x32_bf16 v[220:223], a[180:183], a[220:223], v[220:223]
ds_read_b64_tr_b16 a[164:165], v4 offset:0
ds_read_b64_tr_b16 a[166:167], v4 offset:2048
v_mfma_f32_16x16x32_bf16 v[216:219], a[180:183], a[216:219], v[216:219]
v_mul_f32 v49, v49, v51
v_cvt_pk_bf16_f32 v118, v48, v49
v_accvgpr_read_b32 v52, a142
v_mfma_f32_16x16x32_bf16 v[212:215], a[180:183], a[212:215], v[212:215]
v_accvgpr_read_b32 v53, a143
v_accvgpr_read_b32 v54, a158
v_mfma_f32_16x16x32_bf16 v[208:211], a[180:183], a[208:211], v[208:211]
v_accvgpr_read_b32 v55, a159
v_max_f32 v52, 0, v52
s_waitcnt lgkmcnt(4)
v_mfma_f32_16x16x32_bf16 v[224:227], a[184:187], a[208:211], v[224:227]
s_add_u32 m0, s53, 0x3000
ds_read_b64_tr_b16 a[168:169], v5 offset:0
v_mfma_f32_16x16x32_bf16 v[228:231], a[184:187], a[212:215], v[228:231]
buffer_load_dwordx4 v24, s[36:39], s65 offen lds
ds_read_b64_tr_b16 a[170:171], v5 offset:2048
v_mfma_f32_16x16x32_bf16 v[232:235], a[184:187], a[216:219], v[232:235]
v_max_f32 v53, 0, v53
v_mul_f32 v52, v52, v54
v_mfma_f32_16x16x32_bf16 v[236:239], a[184:187], a[220:223], v[236:239]
v_mul_f32 v53, v53, v55
v_cvt_pk_bf16_f32 v119, v52, v53
v_mfma_f32_16x16x32_bf16 v[252:255], a[188:191], a[220:223], v[252:255]
buffer_load_dwordx4 v25, s[36:39], s65 offen offset:1024 lds
v_mfma_f32_16x16x32_bf16 v[248:251], a[188:191], a[216:219], v[248:251]
ds_read_b64_tr_b16 a[172:173], v6 offset:0
ds_read_b64_tr_b16 a[174:175], v6 offset:2048
v_mfma_f32_16x16x32_bf16 v[244:247], a[188:191], a[212:215], v[244:247]
v_mfma_f32_16x16x32_bf16 v[240:243], a[188:191], a[208:211], v[240:243]
s_waitcnt lgkmcnt(4)
s_mov_b32 s54, 1
.Lk2_G2:
v_mfma_f32_16x16x32_bf16 v[128:131], a[160:163], a[192:195], v[128:131]
buffer_load_dwordx4 v26, s[36:39], s65 offen offset:2048 lds
v_mfma_f32_16x16x32_bf16 v[132:135], a[160:163], a[196:199], v[132:135]
s_waitcnt vmcnt(7)
ds_read_b64_tr_b16 a[176:177], v3 offset:4096
ds_read_b128 a[208:211], v9 offset:4096
v_mfma_f32_16x16x32_bf16 v[136:139], a[160:163], a[200:203], v[136:139]
ds_read_b64_tr_b16 a[178:179], v3 offset:6144
ds_read_b128 a[212:215], v9 offset:5120
v_mfma_f32_16x16x32_bf16 v[140:143], a[160:163], a[204:207], v[140:143]
buffer_load_dwordx4 v27, s[36:39], s65 offen offset:3072 lds
ds_read_b128 a[216:219], v10 offset:4096
v_mfma_f32_16x16x32_bf16 v[156:159], a[164:167], a[204:207], v[156:159]
ds_read_b64_tr_b16 a[180:181], v4 offset:4096
ds_read_b128 a[220:223], v10 offset:5120
v_mfma_f32_16x16x32_bf16 v[152:155], a[164:167], a[200:203], v[152:155]
ds_read_b64_tr_b16 a[182:183], v4 offset:6144
v_mfma_f32_16x16x32_bf16 v[148:151], a[164:167], a[196:199], v[148:151]
v_mfma_f32_16x16x32_bf16 v[144:147], a[164:167], a[192:195], v[144:147]
s_waitcnt lgkmcnt(8)
v_mfma_f32_16x16x32_bf16 v[160:163], a[168:171], a[192:195], v[160:163]
s_add_u32 s65, s65, 0x90000
ds_read_b64_tr_b16 a[184:185], v5 offset:4096
v_mfma_f32_16x16x32_bf16 v[164:167], a[168:171], a[196:199], v[164:167]
s_cmp_eq_u32 s65, s73
s_cselect_b32 s65, s69, s65
ds_read_b64_tr_b16 a[186:187], v5 offset:6144
v_mfma_f32_16x16x32_bf16 v[168:171], a[168:171], a[200:203], v[168:171]
s_add_u32 m0, s53, 0
v_mfma_f32_16x16x32_bf16 v[172:175], a[168:171], a[204:207], v[172:175]
buffer_load_dwordx4 v20, s[84:87], s65 offen lds
v_mfma_f32_16x16x32_bf16 v[188:191], a[172:175], a[204:207], v[188:191]
ds_read_b64_tr_b16 a[188:189], v6 offset:4096
ds_read_b64_tr_b16 a[190:191], v6 offset:6144
v_mfma_f32_16x16x32_bf16 v[184:187], a[172:175], a[200:203], v[184:187]
v_mfma_f32_16x16x32_bf16 v[180:183], a[172:175], a[196:199], v[180:183]
buffer_load_dwordx4 v21, s[84:87], s65 offen offset:1024 lds
v_mfma_f32_16x16x32_bf16 v[176:179], a[172:175], a[192:195], v[176:179]
s_waitcnt lgkmcnt(4)
v_mfma_f32_16x16x32_bf16 v[192:195], a[176:179], a[192:195], v[192:195]
s_waitcnt vmcnt(6)
ds_read_b64_tr_b16 a[160:161], v3 offset:8192
v_mfma_f32_16x16x32_bf16 v[196:199], a[176:179], a[196:199], v[196:199]
buffer_load_dwordx4 v22, s[84:87], s65 offen offset:2048 lds
ds_read_b64_tr_b16 a[162:163], v3 offset:10240
v_mfma_f32_16x16x32_bf16 v[200:203], a[176:179], a[200:203], v[200:203]
v_mfma_f32_16x16x32_bf16 v[204:207], a[176:179], a[204:207], v[204:207]
v_mfma_f32_16x16x32_bf16 v[220:223], a[180:183], a[204:207], v[220:223]
buffer_load_dwordx4 v23, s[84:87], s65 offen offset:3072 lds
v_mfma_f32_16x16x32_bf16 v[216:219], a[180:183], a[200:203], v[216:219]
ds_read_b64_tr_b16 a[164:165], v4 offset:8192
ds_read_b64_tr_b16 a[166:167], v4 offset:10240
v_mfma_f32_16x16x32_bf16 v[212:215], a[180:183], a[196:199], v[212:215]
v_mfma_f32_16x16x32_bf16 v[208:211], a[180:183], a[192:195], v[208:211]
s_waitcnt lgkmcnt(4)
v_mfma_f32_16x16x32_bf16 v[224:227], a[184:187], a[192:195], v[224:227]
s_add_u32 m0, s53, 0x1000
)ASM"
R"ASM(
ds_read_b64_tr_b16 a[168:169], v5 offset:8192
v_mfma_f32_16x16x32_bf16 v[228:231], a[184:187], a[196:199], v[228:231]
buffer_load_dwordx4 v20, s[36:39], s65 offen lds
ds_read_b64_tr_b16 a[170:171], v5 offset:10240
v_mfma_f32_16x16x32_bf16 v[232:235], a[184:187], a[200:203], v[232:235]
v_mfma_f32_16x16x32_bf16 v[236:239], a[184:187], a[204:207], v[236:239]
v_mfma_f32_16x16x32_bf16 v[252:255], a[188:191], a[204:207], v[252:255]
buffer_load_dwordx4 v21, s[36:39], s65 offen offset:1024 lds
v_mfma_f32_16x16x32_bf16 v[248:251], a[188:191], a[200:203], v[248:251]
ds_read_b64_tr_b16 a[172:173], v6 offset:8192
ds_read_b64_tr_b16 a[174:175], v6 offset:10240
v_mfma_f32_16x16x32_bf16 v[244:247], a[188:191], a[196:199], v[244:247]
v_mfma_f32_16x16x32_bf16 v[240:243], a[188:191], a[192:195], v[240:243]
s_waitcnt lgkmcnt(4)
v_mfma_f32_16x16x32_bf16 v[128:131], a[160:163], a[208:211], v[128:131]
buffer_load_dwordx4 v22, s[36:39], s65 offen offset:2048 lds
v_mfma_f32_16x16x32_bf16 v[132:135], a[160:163], a[212:215], v[132:135]
s_waitcnt vmcnt(7)
ds_read_b64_tr_b16 a[176:177], v3 offset:12288
s_add_u32 s55, s55, 0x2000
v_mfma_f32_16x16x32_bf16 v[136:139], a[160:163], a[216:219], v[136:139]
ds_read_b64_tr_b16 a[178:179], v3 offset:14336
v_mfma_f32_16x16x32_bf16 v[140:143], a[160:163], a[220:223], v[140:143]
buffer_load_dwordx4 v23, s[36:39], s65 offen offset:3072 lds
v_mfma_f32_16x16x32_bf16 v[156:159], a[164:167], a[220:223], v[156:159]
ds_read_b64_tr_b16 a[180:181], v4 offset:12288
ds_read_b64_tr_b16 a[182:183], v4 offset:14336
v_mfma_f32_16x16x32_bf16 v[152:155], a[164:167], a[216:219], v[152:155]
s_cmp_eq_u32 s55, 0x12000
s_cselect_b32 s55, 0, s55
v_add_u32 v9, s55, v14
v_mfma_f32_16x16x32_bf16 v[148:151], a[164:167], a[212:215], v[148:151]
v_add_u32 v10, s55, v15
ds_read_b128 a[192:195], v9 offset:0
v_mfma_f32_16x16x32_bf16 v[144:147], a[164:167], a[208:211], v[144:147]
ds_read_b128 a[196:199], v9 offset:1024
ds_read_b128 a[200:203], v10 offset:0
s_waitcnt lgkmcnt(7)
v_mfma_f32_16x16x32_bf16 v[160:163], a[168:171], a[208:211], v[160:163]
s_add_u32 m0, s53, 0x2000
ds_read_b64_tr_b16 a[184:185], v5 offset:12288
v_mfma_f32_16x16x32_bf16 v[164:167], a[168:171], a[212:215], v[164:167]
buffer_load_dwordx4 v24, s[84:87], s65 offen lds
ds_read_b128 a[204:207], v10 offset:1024
v_mfma_f32_16x16x32_bf16 v[168:171], a[168:171], a[216:219], v[168:171]
ds_read_b64_tr_b16 a[186:187], v5 offset:14336
v_mfma_f32_16x16x32_bf16 v[172:175], a[168:171], a[220:223], v[172:175]
v_mfma_f32_16x16x32_bf16 v[188:191], a[172:175], a[220:223], v[188:191]
buffer_load_dwordx4 v25, s[84:87], s65 offen offset:1024 lds
v_mfma_f32_16x16x32_bf16 v[184:187], a[172:175], a[216:219], v[184:187]
ds_read_b64_tr_b16 a[188:189], v6 offset:12288
ds_read_b64_tr_b16 a[190:191], v6 offset:14336
v_mfma_f32_16x16x32_bf16 v[180:183], a[172:175], a[212:215], v[180:183]
v_mfma_f32_16x16x32_bf16 v[176:179], a[172:175], a[208:211], v[176:179]
s_waitcnt lgkmcnt(8)
v_mfma_f32_16x16x32_bf16 v[192:195], a[176:179], a[208:211], v[192:195]
buffer_load_dwordx4 v26, s[84:87], s65 offen offset:2048 lds
v_mfma_f32_16x16x32_bf16 v[196:199], a[176:179], a[212:215], v[196:199]
s_waitcnt vmcnt(7)
ds_read_b64_tr_b16 a[160:161], v3 offset:0
ds_read_b64_tr_b16 a[162:163], v3 offset:2048
v_mfma_f32_16x16x32_bf16 v[200:203], a[176:179], a[216:219], v[200:203]
v_mfma_f32_16x16x32_bf16 v[204:207], a[176:179], a[220:223], v[204:207]
buffer_load_dwordx4 v27, s[84:87], s65 offen offset:3072 lds
v_mfma_f32_16x16x32_bf16 v[220:223], a[180:183], a[220:223], v[220:223]
ds_read_b64_tr_b16 a[164:165], v4 offset:0
ds_read_b64_tr_b16 a[166:167], v4 offset:2048
v_mfma_f32_16x16x32_bf16 v[216:219], a[180:183], a[216:219], v[216:219]
v_mfma_f32_16x16x32_bf16 v[212:215], a[180:183], a[212:215], v[212:215]
v_mfma_f32_16x16x32_bf16 v[208:211], a[180:183], a[208:211], v[208:211]
s_waitcnt lgkmcnt(4)
v_mfma_f32_16x16x32_bf16 v[224:227], a[184:187], a[208:211], v[224:227]
s_add_u32 m0, s53, 0x3000
ds_read_b64_tr_b16 a[168:169], v5 offset:0
v_mfma_f32_16x16x32_bf16 v[228:231], a[184:187], a[212:215], v[228:231]
buffer_load_dwordx4 v24, s[36:39], s65 offen lds
ds_read_b64_tr_b16 a[170:171], v5 offset:2048
v_mfma_f32_16x16x32_bf16 v[232:235], a[184:187], a[216:219], v[232:235]
v_mfma_f32_16x16x32_bf16 v[236:239], a[184:187], a[220:223], v[236:239]
v_mfma_f32_16x16x32_bf16 v[252:255], a[188:191], a[220:223], v[252:255]
buffer_load_dwordx4 v25, s[36:39], s65 offen offset:1024 lds
v_mfma_f32_16x16x32_bf16 v[248:251], a[188:191], a[216:219], v[248:251]
ds_read_b64_tr_b16 a[172:173], v6 offset:0
ds_read_b64_tr_b16 a[174:175], v6 offset:2048
v_mfma_f32_16x16x32_bf16 v[244:247], a[188:191], a[212:215], v[244:247]
v_mfma_f32_16x16x32_bf16 v[240:243], a[188:191], a[208:211], v[240:243]
s_waitcnt lgkmcnt(4)
s_add_u32 s54, s54, -1
s_cbranch_scc1 .Lk2_G2
v_mfma_f32_16x16x32_bf16 v[128:131], a[160:163], a[192:195], v[128:131]
buffer_load_dwordx4 v26, s[36:39], s65 offen offset:2048 lds
v_mfma_f32_16x16x32_bf16 v[132:135], a[160:163], a[196:199], v[132:135]
s_waitcnt vmcnt(7)
ds_read_b64_tr_b16 a[176:177], v3 offset:4096
ds_read_b128 a[208:211], v9 offset:4096
v_mfma_f32_16x16x32_bf16 v[136:139], a[160:163], a[200:203], v[136:139]
ds_read_b64_tr_b16 a[178:179], v3 offset:6144
ds_read_b128 a[212:215], v9 offset:5120
v_mfma_f32_16x16x32_bf16 v[140:143], a[160:163], a[204:207], v[140:143]
buffer_load_dwordx4 v27, s[36:39], s65 offen offset:3072 lds
ds_read_b128 a[216:219], v10 offset:4096
v_mfma_f32_16x16x32_bf16 v[156:159], a[164:167], a[204:207], v[156:159]
ds_read_b64_tr_b16 a[180:181], v4 offset:4096
ds_read_b128 a[220:223], v10 offset:5120
v_mfma_f32_16x16x32_bf16 v[152:155], a[164:167], a[200:203], v[152:155]
ds_read_b64_tr_b16 a[182:183], v4 offset:6144
v_mfma_f32_16x16x32_bf16 v[148:151], a[164:167], a[196:199], v[148:151]
v_mfma_f32_16x16x32_bf16 v[144:147], a[164:167], a[192:195], v[144:147]
s_waitcnt lgkmcnt(8)
v_mfma_f32_16x16x32_bf16 v[160:163], a[168:171], a[192:195], v[160:163]
s_add_u32 m0, s53, 0
ds_read_b64_tr_b16 a[184:185], v5 offset:4096
v_mfma_f32_16x16x32_bf16 v[164:167], a[168:171], a[196:199], v[164:167]
buffer_load_dwordx4 v64, s[40:43], s66 offen lds
ds_read_b64_tr_b16 a[186:187], v5 offset:6144
v_mfma_f32_16x16x32_bf16 v[168:171], a[168:171], a[200:203], v[168:171]
v_mfma_f32_16x16x32_bf16 v[172:175], a[168:171], a[204:207], v[172:175]
v_mfma_f32_16x16x32_bf16 v[188:191], a[172:175], a[204:207], v[188:191]
buffer_load_dwordx4 v65, s[40:43], s66 offen offset:1024 lds
v_mfma_f32_16x16x32_bf16 v[184:187], a[172:175], a[200:203], v[184:187]
ds_read_b64_tr_b16 a[188:189], v6 offset:4096
ds_read_b64_tr_b16 a[190:191], v6 offset:6144
v_mfma_f32_16x16x32_bf16 v[180:183], a[172:175], a[196:199], v[180:183]
v_mfma_f32_16x16x32_bf16 v[176:179], a[172:175], a[192:195], v[176:179]
s_waitcnt lgkmcnt(4)
v_mfma_f32_16x16x32_bf16 v[192:195], a[176:179], a[192:195], v[192:195]
buffer_load_dwordx4 v66, s[40:43], s66 offen offset:2048 lds
v_mfma_f32_16x16x32_bf16 v[196:199], a[176:179], a[196:199], v[196:199]
s_waitcnt vmcnt(7)
ds_read_b64_tr_b16 a[160:161], v3 offset:8192
ds_read_b64_tr_b16 a[162:163], v3 offset:10240
v_mfma_f32_16x16x32_bf16 v[200:203], a[176:179], a[200:203], v[200:203]
v_mfma_f32_16x16x32_bf16 v[204:207], a[176:179], a[204:207], v[204:207]
buffer_load_dwordx4 v67, s[40:43], s66 offen offset:3072 lds
v_mfma_f32_16x16x32_bf16 v[220:223], a[180:183], a[204:207], v[220:223]
ds_read_b64_tr_b16 a[164:165], v4 offset:8192
ds_read_b64_tr_b16 a[166:167], v4 offset:10240
v_mfma_f32_16x16x32_bf16 v[216:219], a[180:183], a[200:203], v[216:219]
v_mfma_f32_16x16x32_bf16 v[212:215], a[180:183], a[196:199], v[212:215]
v_mfma_f32_16x16x32_bf16 v[208:211], a[180:183], a[192:195], v[208:211]
s_waitcnt lgkmcnt(4)
v_mfma_f32_16x16x32_bf16 v[224:227], a[184:187], a[192:195], v[224:227]
s_add_u32 m0, s53, 0x1000
ds_read_b64_tr_b16 a[168:169], v5 offset:8192
v_mfma_f32_16x16x32_bf16 v[228:231], a[184:187], a[196:199], v[228:231]
buffer_load_dwordx4 v68, s[40:43], s66 offen lds
ds_read_b64_tr_b16 a[170:171], v5 offset:10240
v_mfma_f32_16x16x32_bf16 v[232:235], a[184:187], a[200:203], v[232:235]
v_mfma_f32_16x16x32_bf16 v[236:239], a[184:187], a[204:207], v[236:239]
v_mfma_f32_16x16x32_bf16 v[252:255], a[188:191], a[204:207], v[252:255]
buffer_load_dwordx4 v69, s[40:43], s66 offen offset:1024 lds
v_mfma_f32_16x16x32_bf16 v[248:251], a[188:191], a[200:203], v[248:251]
ds_read_b64_tr_b16 a[172:173], v6 offset:8192
ds_read_b64_tr_b16 a[174:175], v6 offset:10240
v_mfma_f32_16x16x32_bf16 v[244:247], a[188:191], a[196:199], v[244:247]
v_mfma_f32_16x16x32_bf16 v[240:243], a[188:191], a[192:195], v[240:243]
s_waitcnt lgkmcnt(4)
v_mfma_f32_16x16x32_bf16 v[128:131], a[160:163], a[208:211], v[128:131]
s_add_u32 m0, s61, 0x12000
s_waitcnt vmcnt(6)
ds_read_b64_tr_b16 a[176:177], v3 offset:12288
v_mfma_f32_16x16x32_bf16 v[132:135], a[160:163], a[212:215], v[132:135]
buffer_load_dwordx4 v30, s[40:43], s35 offen lds
ds_read_b64_tr_b16 a[178:179], v3 offset:14336
v_mfma_f32_16x16x32_bf16 v[136:139], a[160:163], a[216:219], v[136:139]
s_add_u32 m0, s61, 0x13000
v_mfma_f32_16x16x32_bf16 v[140:143], a[160:163], a[220:223], v[140:143]
buffer_load_dwordx4 v31, s[40:43], s35 offen lds
v_mfma_f32_16x16x32_bf16 v[156:159], a[164:167], a[220:223], v[156:159]
s_add_u32 m0, s53, 0x1000
ds_read_b64_tr_b16 a[180:181], v4 offset:12288
v_mfma_f32_16x16x32_bf16 v[152:155], a[164:167], a[216:219], v[152:155]
ds_read_b64_tr_b16 a[182:183], v4 offset:14336
v_mfma_f32_16x16x32_bf16 v[148:151], a[164:167], a[212:215], v[148:151]
buffer_load_dwordx4 v70, s[40:43], s66 offen offset:2048 lds
v_mfma_f32_16x16x32_bf16 v[144:147], a[164:167], a[208:211], v[144:147]
s_waitcnt lgkmcnt(4)
v_mfma_f32_16x16x32_bf16 v[160:163], a[168:171], a[208:211], v[160:163]
ds_read_b64_tr_b16 a[184:185], v5 offset:12288
ds_read_b64_tr_b16 a[186:187], v5 offset:14336
v_mfma_f32_16x16x32_bf16 v[164:167], a[168:171], a[212:215], v[164:167]
buffer_load_dwordx4 v71, s[40:43], s66 offen offset:3072 lds
v_mfma_f32_16x16x32_bf16 v[168:171], a[168:171], a[216:219], v[168:171]
s_add_u32 s66, s66, 0x12000
v_mfma_f32_16x16x32_bf16 v[172:175], a[168:171], a[220:223], v[172:175]
s_cmp_eq_u32 s66, s74
s_cselect_b32 s66, s70, s66
v_mfma_f32_16x16x32_bf16 v[188:191], a[172:175], a[220:223], v[188:191]
s_add_u32 m0, s53, 0x2000
ds_read_b64_tr_b16 a[188:189], v6 offset:12288
v_mfma_f32_16x16x32_bf16 v[184:187], a[172:175], a[216:219], v[184:187]
buffer_load_dwordx4 v64, s[40:43], s66 offen lds
ds_read_b64_tr_b16 a[190:191], v6 offset:14336
v_mfma_f32_16x16x32_bf16 v[180:183], a[172:175], a[212:215], v[180:183]
v_mfma_f32_16x16x32_bf16 v[176:179], a[172:175], a[208:211], v[176:179]
s_waitcnt lgkmcnt(4)
v_mfma_f32_16x16x32_bf16 v[192:195], a[176:179], a[208:211], v[192:195]
buffer_load_dwordx4 v65, s[40:43], s66 offen offset:1024 lds
v_mfma_f32_16x16x32_bf16 v[196:199], a[176:179], a[212:215], v[196:199]
s_waitcnt vmcnt(8)
)ASM"
R"ASM(
ds_read_b64_tr_b16 a[160:161], v3 offset:0
ds_read_b64_tr_b16 a[162:163], v3 offset:2048
v_mfma_f32_16x16x32_bf16 v[200:203], a[176:179], a[216:219], v[200:203]
v_mfma_f32_16x16x32_bf16 v[204:207], a[176:179], a[220:223], v[204:207]
buffer_load_dwordx4 v66, s[40:43], s66 offen offset:2048 lds
v_mfma_f32_16x16x32_bf16 v[220:223], a[180:183], a[220:223], v[220:223]
ds_read_b64_tr_b16 a[164:165], v4 offset:0
ds_read_b64_tr_b16 a[166:167], v4 offset:2048
v_mfma_f32_16x16x32_bf16 v[216:219], a[180:183], a[216:219], v[216:219]
v_mfma_f32_16x16x32_bf16 v[212:215], a[180:183], a[212:215], v[212:215]
buffer_load_dwordx4 v67, s[40:43], s66 offen offset:3072 lds
v_mfma_f32_16x16x32_bf16 v[208:211], a[180:183], a[208:211], v[208:211]
s_waitcnt lgkmcnt(4)
v_mfma_f32_16x16x32_bf16 v[224:227], a[184:187], a[208:211], v[224:227]
s_add_u32 m0, s53, 0x3000
ds_read_b64_tr_b16 a[168:169], v5 offset:0
v_mfma_f32_16x16x32_bf16 v[228:231], a[184:187], a[212:215], v[228:231]
buffer_load_dwordx4 v68, s[40:43], s66 offen lds
ds_read_b64_tr_b16 a[170:171], v5 offset:2048
v_mfma_f32_16x16x32_bf16 v[232:235], a[184:187], a[216:219], v[232:235]
v_mfma_f32_16x16x32_bf16 v[236:239], a[184:187], a[220:223], v[236:239]
v_mfma_f32_16x16x32_bf16 v[252:255], a[188:191], a[220:223], v[252:255]
buffer_load_dwordx4 v69, s[40:43], s66 offen offset:1024 lds
v_mfma_f32_16x16x32_bf16 v[248:251], a[188:191], a[216:219], v[248:251]
ds_read_b64_tr_b16 a[172:173], v6 offset:0
ds_read_b64_tr_b16 a[174:175], v6 offset:2048
v_mfma_f32_16x16x32_bf16 v[244:247], a[188:191], a[212:215], v[244:247]
v_mfma_f32_16x16x32_bf16 v[240:243], a[188:191], a[208:211], v[240:243]
buffer_load_dwordx4 v70, s[40:43], s66 offen offset:2048 lds
s_load_dwordx2 s[16:17], %0, 0x8
s_load_dwordx2 s[20:21], %0, 0x68
s_load_dwordx2 s[24:25], %0, 0x70
v_max_f32 v128, 0, v128
v_max_f32 v129, 0, v129
v_mul_f32 v128, v128, v192
v_mul_f32 v129, v129, v193
v_cvt_pk_bf16_f32 v128, v128, v129
v_max_f32 v130, 0, v130
v_max_f32 v131, 0, v131
v_mul_f32 v130, v130, v194
v_mul_f32 v131, v131, v195
v_cvt_pk_bf16_f32 v129, v130, v131
v_max_f32 v144, 0, v144
v_max_f32 v145, 0, v145
v_mul_f32 v144, v144, v208
v_mul_f32 v145, v145, v209
v_cvt_pk_bf16_f32 v130, v144, v145
v_max_f32 v146, 0, v146
v_max_f32 v147, 0, v147
v_mul_f32 v146, v146, v210
v_mul_f32 v147, v147, v211
v_cvt_pk_bf16_f32 v131, v146, v147
v_max_f32 v132, 0, v132
v_max_f32 v133, 0, v133
v_mul_f32 v132, v132, v196
v_mul_f32 v133, v133, v197
v_cvt_pk_bf16_f32 v132, v132, v133
v_max_f32 v134, 0, v134
v_max_f32 v135, 0, v135
v_mul_f32 v134, v134, v198
v_mul_f32 v135, v135, v199
v_cvt_pk_bf16_f32 v133, v134, v135
v_max_f32 v148, 0, v148
v_max_f32 v149, 0, v149
v_mul_f32 v148, v148, v212
v_mul_f32 v149, v149, v213
v_cvt_pk_bf16_f32 v134, v148, v149
v_max_f32 v150, 0, v150
v_max_f32 v151, 0, v151
v_mul_f32 v150, v150, v214
v_mul_f32 v151, v151, v215
v_cvt_pk_bf16_f32 v135, v150, v151
v_max_f32 v136, 0, v136
v_max_f32 v137, 0, v137
v_mul_f32 v136, v136, v200
v_mul_f32 v137, v137, v201
v_cvt_pk_bf16_f32 v136, v136, v137
v_max_f32 v138, 0, v138
v_max_f32 v139, 0, v139
v_mul_f32 v138, v138, v202
v_mul_f32 v139, v139, v203
v_cvt_pk_bf16_f32 v137, v138, v139
v_max_f32 v152, 0, v152
v_max_f32 v153, 0, v153
v_mul_f32 v152, v152, v216
v_mul_f32 v153, v153, v217
v_cvt_pk_bf16_f32 v138, v152, v153
v_max_f32 v154, 0, v154
v_max_f32 v155, 0, v155
v_mul_f32 v154, v154, v218
v_mul_f32 v155, v155, v219
v_cvt_pk_bf16_f32 v139, v154, v155
v_max_f32 v140, 0, v140
v_max_f32 v141, 0, v141
v_mul_f32 v140, v140, v204
v_mul_f32 v141, v141, v205
v_cvt_pk_bf16_f32 v140, v140, v141
v_max_f32 v142, 0, v142
v_max_f32 v143, 0, v143
v_mul_f32 v142, v142, v206
v_mul_f32 v143, v143, v207
v_cvt_pk_bf16_f32 v141, v142, v143
v_max_f32 v156, 0, v156
v_max_f32 v157, 0, v157
v_mul_f32 v156, v156, v220
v_mul_f32 v157, v157, v221
v_cvt_pk_bf16_f32 v142, v156, v157
v_max_f32 v158, 0, v158
v_max_f32 v159, 0, v159
v_mul_f32 v158, v158, v222
v_mul_f32 v159, v159, v223
v_cvt_pk_bf16_f32 v143, v158, v159
v_max_f32 v160, 0, v160
v_max_f32 v161, 0, v161
v_mul_f32 v160, v160, v224
v_mul_f32 v161, v161, v225
v_cvt_pk_bf16_f32 v160, v160, v161
v_max_f32 v162, 0, v162
v_max_f32 v163, 0, v163
v_mul_f32 v162, v162, v226
v_mul_f32 v163, v163, v227
v_cvt_pk_bf16_f32 v161, v162, v163
v_max_f32 v176, 0, v176
v_max_f32 v177, 0, v177
v_mul_f32 v176, v176, v240
v_mul_f32 v177, v177, v241
v_cvt_pk_bf16_f32 v162, v176, v177
v_max_f32 v178, 0, v178
v_max_f32 v179, 0, v179
v_mul_f32 v178, v178, v242
v_mul_f32 v179, v179, v243
v_cvt_pk_bf16_f32 v163, v178, v179
v_max_f32 v164, 0, v164
v_max_f32 v165, 0, v165
v_mul_f32 v164, v164, v228
v_mul_f32 v165, v165, v229
v_cvt_pk_bf16_f32 v164, v164, v165
v_max_f32 v166, 0, v166
v_max_f32 v167, 0, v167
v_mul_f32 v166, v166, v230
v_mul_f32 v167, v167, v231
v_cvt_pk_bf16_f32 v165, v166, v167
v_max_f32 v180, 0, v180
v_max_f32 v181, 0, v181
v_mul_f32 v180, v180, v244
v_mul_f32 v181, v181, v245
v_cvt_pk_bf16_f32 v166, v180, v181
v_max_f32 v182, 0, v182
v_max_f32 v183, 0, v183
v_mul_f32 v182, v182, v246
v_mul_f32 v183, v183, v247
v_cvt_pk_bf16_f32 v167, v182, v183
v_max_f32 v168, 0, v168
v_max_f32 v169, 0, v169
v_mul_f32 v168, v168, v232
v_mul_f32 v169, v169, v233
v_cvt_pk_bf16_f32 v168, v168, v169
v_max_f32 v170, 0, v170
v_max_f32 v171, 0, v171
v_mul_f32 v170, v170, v234
v_mul_f32 v171, v171, v235
v_cvt_pk_bf16_f32 v169, v170, v171
v_max_f32 v184, 0, v184
v_max_f32 v185, 0, v185
v_mul_f32 v184, v184, v248
v_mul_f32 v185, v185, v249
v_cvt_pk_bf16_f32 v170, v184, v185
v_max_f32 v186, 0, v186
v_max_f32 v187, 0, v187
v_mul_f32 v186, v186, v250
v_mul_f32 v187, v187, v251
v_cvt_pk_bf16_f32 v171, v186, v187
v_max_f32 v172, 0, v172
v_max_f32 v173, 0, v173
v_mul_f32 v172, v172, v236
v_mul_f32 v173, v173, v237
v_cvt_pk_bf16_f32 v172, v172, v173
v_max_f32 v174, 0, v174
v_max_f32 v175, 0, v175
v_mul_f32 v174, v174, v238
v_mul_f32 v175, v175, v239
v_cvt_pk_bf16_f32 v173, v174, v175
v_max_f32 v188, 0, v188
v_max_f32 v189, 0, v189
v_mul_f32 v188, v188, v252
v_mul_f32 v189, v189, v253
v_cvt_pk_bf16_f32 v174, v188, v189
v_max_f32 v190, 0, v190
v_max_f32 v191, 0, v191
v_mul_f32 v190, v190, v254
v_mul_f32 v191, v191, v255
v_cvt_pk_bf16_f32 v175, v190, v191
s_barrier
ds_write_b128 v16, v[80:83] offset:0
ds_write_b128 v16, v[128:131] offset:8192
ds_write_b128 v16, v[84:87] offset:1024
ds_write_b128 v16, v[132:135] offset:9216
ds_write_b128 v17, v[88:91] offset:0
ds_write_b128 v17, v[136:139] offset:8192
ds_write_b128 v17, v[92:95] offset:1024
)ASM"
R"ASM(
ds_write_b128 v17, v[140:143] offset:9216
ds_write_b128 v16, v[96:99] offset:4096
ds_write_b128 v16, v[160:163] offset:12288
ds_write_b128 v16, v[100:103] offset:5120
ds_write_b128 v16, v[164:167] offset:13312
ds_write_b128 v17, v[104:107] offset:4096
ds_write_b128 v17, v[168:171] offset:12288
ds_write_b128 v17, v[108:111] offset:5120
ds_write_b128 v17, v[172:175] offset:13312
ds_write_b128 v18, v[112:115] offset:0
ds_write_b128 v18, v[116:119] offset:1024
s_mov_b32 s55, s79
v_add_u32 v9, s55, v14
v_add_u32 v10, s55, v15
s_waitcnt vmcnt(9) lgkmcnt(0)
s_barrier
ds_read_b128 a[192:195], v9 offset:0
ds_read_b128 a[196:199], v9 offset:1024
ds_read_b128 a[200:203], v10 offset:0
ds_read_b128 a[204:207], v10 offset:1024
s_waitcnt lgkmcnt(0)
v_mfma_f32_16x16x32_bf16 v[176:179], a[160:163], a[192:195], 0
s_add_u32 s35, s35, 0x12000
ds_read_b64_tr_b16 a[176:177], v7 offset:0
v_mfma_f32_16x16x32_bf16 v[180:183], a[160:163], a[196:199], 0
s_cmp_eq_u32 s35, s74
s_cselect_b32 s35, s70, s35
v_mfma_f32_16x16x32_bf16 v[184:187], a[160:163], a[200:203], 0
s_add_u32 m0, s61, 0x14000
ds_read_b64_tr_b16 a[178:179], v7 offset:2048
v_mfma_f32_16x16x32_bf16 v[188:191], a[160:163], a[204:207], 0
buffer_load_dwordx4 v30, s[40:43], s35 offen lds
v_mfma_f32_16x16x32_bf16 v[204:207], a[164:167], a[204:207], 0
s_add_u32 m0, s61, 0x15000
ds_read_b64_tr_b16 a[180:181], v8 offset:0
v_mfma_f32_16x16x32_bf16 v[200:203], a[164:167], a[200:203], 0
ds_read_b64_tr_b16 a[182:183], v8 offset:2048
ds_read_b128 a[208:211], v9 offset:4096
v_mfma_f32_16x16x32_bf16 v[196:199], a[164:167], a[196:199], 0
buffer_load_dwordx4 v31, s[40:43], s35 offen lds
v_mfma_f32_16x16x32_bf16 v[192:195], a[164:167], a[192:195], 0
ds_read_b128 a[212:215], v9 offset:5120
v_mfma_f32_16x16x32_bf16 v[208:211], a[168:171], a[192:195], 0
s_add_u32 m0, s53, 0x3000
s_waitcnt vmcnt(9)
ds_read_b64_tr_b16 a[184:185], v3 offset:4096
v_mfma_f32_16x16x32_bf16 v[212:215], a[168:171], a[196:199], 0
buffer_load_dwordx4 v71, s[40:43], s66 offen offset:3072 lds
v_mfma_f32_16x16x32_bf16 v[216:219], a[168:171], a[200:203], 0
s_add_u32 s66, s66, 0x12000
ds_read_b64_tr_b16 a[186:187], v3 offset:6144
v_mfma_f32_16x16x32_bf16 v[220:223], a[168:171], a[204:207], 0
s_cmp_eq_u32 s66, s74
s_cselect_b32 s66, s70, s66
v_mfma_f32_16x16x32_bf16 v[236:239], a[172:175], a[204:207], 0
s_add_u32 m0, s53, 0
ds_read_b64_tr_b16 a[188:189], v4 offset:4096
v_mfma_f32_16x16x32_bf16 v[232:235], a[172:175], a[200:203], 0
buffer_load_dwordx4 v64, s[40:43], s66 offen lds
v_mfma_f32_16x16x32_bf16 v[228:231], a[172:175], a[196:199], 0
ds_read_b64_tr_b16 a[190:191], v4 offset:6144
ds_read_b128 a[216:219], v10 offset:4096
v_mfma_f32_16x16x32_bf16 v[224:227], a[172:175], a[192:195], 0
ds_read_b128 a[220:223], v10 offset:5120
s_waitcnt lgkmcnt(8)
v_mfma_f32_16x16x32_bf16 v[240:243], a[176:179], a[192:195], 0
buffer_load_dwordx4 v65, s[40:43], s66 offen offset:1024 lds
v_mfma_f32_16x16x32_bf16 v[244:247], a[176:179], a[196:199], 0
ds_read_b64_tr_b16 a[160:161], v5 offset:4096
ds_read_b64_tr_b16 a[162:163], v5 offset:6144
v_mfma_f32_16x16x32_bf16 v[252:255], a[180:183], a[196:199], 0
ds_read_b64_tr_b16 a[164:165], v6 offset:4096
ds_read_b64_tr_b16 a[166:167], v6 offset:6144
v_mfma_f32_16x16x32_bf16 v[248:251], a[180:183], a[192:195], 0
s_waitcnt lgkmcnt(4)
v_mfma_f32_16x16x32_bf16 v[176:179], a[184:187], a[208:211], v[176:179]
buffer_load_dwordx4 v66, s[40:43], s66 offen offset:2048 lds
v_mfma_f32_16x16x32_bf16 v[180:183], a[184:187], a[212:215], v[180:183]
ds_read_b64_tr_b16 a[168:169], v7 offset:4096
s_add_u32 s55, s55, 0x2000
v_mfma_f32_16x16x32_bf16 v[184:187], a[184:187], a[216:219], v[184:187]
ds_read_b64_tr_b16 a[170:171], v7 offset:6144
v_mfma_f32_16x16x32_bf16 v[188:191], a[184:187], a[220:223], v[188:191]
buffer_load_dwordx4 v67, s[40:43], s66 offen offset:3072 lds
v_mfma_f32_16x16x32_bf16 v[204:207], a[188:191], a[220:223], v[204:207]
ds_read_b64_tr_b16 a[172:173], v8 offset:4096
ds_read_b64_tr_b16 a[174:175], v8 offset:6144
v_mfma_f32_16x16x32_bf16 v[200:203], a[188:191], a[216:219], v[200:203]
s_cmp_eq_u32 s55, 0x12000
s_cselect_b32 s55, 0, s55
v_mfma_f32_16x16x32_bf16 v[196:199], a[188:191], a[212:215], v[196:199]
v_add_u32 v9, s55, v14
v_add_u32 v10, s55, v15
v_mfma_f32_16x16x32_bf16 v[192:195], a[188:191], a[208:211], v[192:195]
ds_read_b128 a[192:195], v9 offset:0
s_waitcnt lgkmcnt(5)
v_mfma_f32_16x16x32_bf16 v[208:211], a[160:163], a[208:211], v[208:211]
s_add_u32 m0, s53, 0x1000
s_waitcnt vmcnt(10)
ds_read_b64_tr_b16 a[176:177], v3 offset:8192
v_mfma_f32_16x16x32_bf16 v[212:215], a[160:163], a[212:215], v[212:215]
buffer_load_dwordx4 v68, s[40:43], s66 offen lds
v_mfma_f32_16x16x32_bf16 v[216:219], a[160:163], a[216:219], v[216:219]
ds_read_b64_tr_b16 a[178:179], v3 offset:10240
ds_read_b128 a[196:199], v9 offset:1024
v_mfma_f32_16x16x32_bf16 v[220:223], a[160:163], a[220:223], v[220:223]
ds_read_b128 a[200:203], v10 offset:0
ds_read_b128 a[204:207], v10 offset:1024
v_mfma_f32_16x16x32_bf16 v[236:239], a[164:167], a[220:223], v[236:239]
buffer_load_dwordx4 v69, s[40:43], s66 offen offset:1024 lds
v_mfma_f32_16x16x32_bf16 v[232:235], a[164:167], a[216:219], v[232:235]
ds_read_b64_tr_b16 a[180:181], v4 offset:8192
ds_read_b64_tr_b16 a[182:183], v4 offset:10240
v_mfma_f32_16x16x32_bf16 v[228:231], a[164:167], a[212:215], v[228:231]
v_mfma_f32_16x16x32_bf16 v[224:227], a[164:167], a[208:211], v[224:227]
s_waitcnt lgkmcnt(8)
v_mfma_f32_16x16x32_bf16 v[240:243], a[168:171], a[208:211], v[240:243]
buffer_load_dwordx4 v70, s[40:43], s66 offen offset:2048 lds
v_mfma_f32_16x16x32_bf16 v[244:247], a[168:171], a[212:215], v[244:247]
ds_read_b64_tr_b16 a[184:185], v5 offset:8192
ds_read_b64_tr_b16 a[186:187], v5 offset:10240
v_mfma_f32_16x16x32_bf16 v[252:255], a[172:175], a[212:215], v[252:255]
ds_read_b64_tr_b16 a[188:189], v6 offset:8192
ds_read_b64_tr_b16 a[190:191], v6 offset:10240
v_mfma_f32_16x16x32_bf16 v[248:251], a[172:175], a[208:211], v[248:251]
s_waitcnt lgkmcnt(4)
s_mov_b32 s54, 2
.Lk2_D1:
s_waitcnt vmcnt(8)
s_barrier
v_mfma_f32_16x16x32_bf16 v[176:179], a[176:179], a[192:195], v[176:179]
s_add_u32 s35, s35, 0x12000
ds_read_b64_tr_b16 a[160:161], v7 offset:8192
v_mfma_f32_16x16x32_bf16 v[180:183], a[176:179], a[196:199], v[180:183]
s_cmp_eq_u32 s35, s74
s_cselect_b32 s35, s70, s35
v_mfma_f32_16x16x32_bf16 v[184:187], a[176:179], a[200:203], v[184:187]
s_add_u32 m0, s61, 0x12000
ds_read_b64_tr_b16 a[162:163], v7 offset:10240
v_mfma_f32_16x16x32_bf16 v[188:191], a[176:179], a[204:207], v[188:191]
buffer_load_dwordx4 v30, s[40:43], s35 offen lds
v_mfma_f32_16x16x32_bf16 v[204:207], a[180:183], a[204:207], v[204:207]
s_add_u32 m0, s61, 0x13000
ds_read_b64_tr_b16 a[164:165], v8 offset:8192
v_mfma_f32_16x16x32_bf16 v[200:203], a[180:183], a[200:203], v[200:203]
ds_read_b64_tr_b16 a[166:167], v8 offset:10240
ds_read_b128 a[208:211], v9 offset:4096
v_mfma_f32_16x16x32_bf16 v[196:199], a[180:183], a[196:199], v[196:199]
buffer_load_dwordx4 v31, s[40:43], s35 offen lds
v_mfma_f32_16x16x32_bf16 v[192:195], a[180:183], a[192:195], v[192:195]
ds_read_b128 a[212:215], v9 offset:5120
s_waitcnt lgkmcnt(6)
v_mfma_f32_16x16x32_bf16 v[208:211], a[184:187], a[192:195], v[208:211]
s_add_u32 m0, s53, 0x1000
s_waitcnt vmcnt(9)
ds_read_b64_tr_b16 a[168:169], v3 offset:12288
v_mfma_f32_16x16x32_bf16 v[212:215], a[184:187], a[196:199], v[212:215]
buffer_load_dwordx4 v71, s[40:43], s66 offen offset:3072 lds
v_mfma_f32_16x16x32_bf16 v[216:219], a[184:187], a[200:203], v[216:219]
s_add_u32 s66, s66, 0x12000
ds_read_b64_tr_b16 a[170:171], v3 offset:14336
v_mfma_f32_16x16x32_bf16 v[220:223], a[184:187], a[204:207], v[220:223]
s_cmp_eq_u32 s66, s74
s_cselect_b32 s66, s70, s66
v_mfma_f32_16x16x32_bf16 v[236:239], a[188:191], a[204:207], v[236:239]
s_add_u32 m0, s53, 0x2000
ds_read_b64_tr_b16 a[172:173], v4 offset:12288
v_mfma_f32_16x16x32_bf16 v[232:235], a[188:191], a[200:203], v[232:235]
buffer_load_dwordx4 v64, s[40:43], s66 offen lds
v_mfma_f32_16x16x32_bf16 v[228:231], a[188:191], a[196:199], v[228:231]
ds_read_b64_tr_b16 a[174:175], v4 offset:14336
ds_read_b128 a[216:219], v10 offset:4096
v_mfma_f32_16x16x32_bf16 v[224:227], a[188:191], a[192:195], v[224:227]
ds_read_b128 a[220:223], v10 offset:5120
s_waitcnt lgkmcnt(8)
v_mfma_f32_16x16x32_bf16 v[240:243], a[160:163], a[192:195], v[240:243]
buffer_load_dwordx4 v65, s[40:43], s66 offen offset:1024 lds
v_mfma_f32_16x16x32_bf16 v[244:247], a[160:163], a[196:199], v[244:247]
ds_read_b64_tr_b16 a[176:177], v5 offset:12288
ds_read_b64_tr_b16 a[178:179], v5 offset:14336
v_mfma_f32_16x16x32_bf16 v[252:255], a[164:167], a[196:199], v[252:255]
ds_read_b64_tr_b16 a[180:181], v6 offset:12288
ds_read_b64_tr_b16 a[182:183], v6 offset:14336
v_mfma_f32_16x16x32_bf16 v[248:251], a[164:167], a[192:195], v[248:251]
s_waitcnt lgkmcnt(4)
v_mfma_f32_16x16x32_bf16 v[176:179], a[168:171], a[208:211], v[176:179]
buffer_load_dwordx4 v66, s[40:43], s66 offen offset:2048 lds
v_mfma_f32_16x16x32_bf16 v[180:183], a[168:171], a[212:215], v[180:183]
ds_read_b64_tr_b16 a[184:185], v7 offset:12288
s_add_u32 s55, s55, 0x2000
v_mfma_f32_16x16x32_bf16 v[184:187], a[168:171], a[216:219], v[184:187]
ds_read_b64_tr_b16 a[186:187], v7 offset:14336
v_mfma_f32_16x16x32_bf16 v[188:191], a[168:171], a[220:223], v[188:191]
buffer_load_dwordx4 v67, s[40:43], s66 offen offset:3072 lds
v_mfma_f32_16x16x32_bf16 v[204:207], a[172:175], a[220:223], v[204:207]
ds_read_b64_tr_b16 a[188:189], v8 offset:12288
ds_read_b64_tr_b16 a[190:191], v8 offset:14336
v_mfma_f32_16x16x32_bf16 v[200:203], a[172:175], a[216:219], v[200:203]
s_cmp_eq_u32 s55, 0x12000
s_cselect_b32 s55, 0, s55
)ASM"
R"ASM(
v_mfma_f32_16x16x32_bf16 v[196:199], a[172:175], a[212:215], v[196:199]
v_add_u32 v9, s55, v14
v_add_u32 v10, s55, v15
v_mfma_f32_16x16x32_bf16 v[192:195], a[172:175], a[208:211], v[192:195]
ds_read_b128 a[192:195], v9 offset:0
s_waitcnt lgkmcnt(5)
v_mfma_f32_16x16x32_bf16 v[208:211], a[176:179], a[208:211], v[208:211]
s_add_u32 m0, s53, 0x3000
s_waitcnt vmcnt(10)
ds_read_b64_tr_b16 a[160:161], v3 offset:0
v_mfma_f32_16x16x32_bf16 v[212:215], a[176:179], a[212:215], v[212:215]
buffer_load_dwordx4 v68, s[40:43], s66 offen lds
v_mfma_f32_16x16x32_bf16 v[216:219], a[176:179], a[216:219], v[216:219]
ds_read_b64_tr_b16 a[162:163], v3 offset:2048
ds_read_b128 a[196:199], v9 offset:1024
v_mfma_f32_16x16x32_bf16 v[220:223], a[176:179], a[220:223], v[220:223]
ds_read_b128 a[200:203], v10 offset:0
ds_read_b128 a[204:207], v10 offset:1024
v_mfma_f32_16x16x32_bf16 v[236:239], a[180:183], a[220:223], v[236:239]
buffer_load_dwordx4 v69, s[40:43], s66 offen offset:1024 lds
v_mfma_f32_16x16x32_bf16 v[232:235], a[180:183], a[216:219], v[232:235]
ds_read_b64_tr_b16 a[164:165], v4 offset:0
ds_read_b64_tr_b16 a[166:167], v4 offset:2048
v_mfma_f32_16x16x32_bf16 v[228:231], a[180:183], a[212:215], v[228:231]
v_mfma_f32_16x16x32_bf16 v[224:227], a[180:183], a[208:211], v[224:227]
s_waitcnt lgkmcnt(8)
v_mfma_f32_16x16x32_bf16 v[240:243], a[184:187], a[208:211], v[240:243]
buffer_load_dwordx4 v70, s[40:43], s66 offen offset:2048 lds
v_mfma_f32_16x16x32_bf16 v[244:247], a[184:187], a[212:215], v[244:247]
ds_read_b64_tr_b16 a[168:169], v5 offset:0
ds_read_b64_tr_b16 a[170:171], v5 offset:2048
v_mfma_f32_16x16x32_bf16 v[252:255], a[188:191], a[212:215], v[252:255]
ds_read_b64_tr_b16 a[172:173], v6 offset:0
ds_read_b64_tr_b16 a[174:175], v6 offset:2048
v_mfma_f32_16x16x32_bf16 v[248:251], a[188:191], a[208:211], v[248:251]
s_waitcnt lgkmcnt(4)
s_waitcnt vmcnt(8)
s_barrier
v_mfma_f32_16x16x32_bf16 v[176:179], a[160:163], a[192:195], v[176:179]
s_add_u32 s35, s35, 0x12000
ds_read_b64_tr_b16 a[176:177], v7 offset:0
v_mfma_f32_16x16x32_bf16 v[180:183], a[160:163], a[196:199], v[180:183]
s_cmp_eq_u32 s35, s74
s_cselect_b32 s35, s70, s35
v_mfma_f32_16x16x32_bf16 v[184:187], a[160:163], a[200:203], v[184:187]
s_add_u32 m0, s61, 0x14000
ds_read_b64_tr_b16 a[178:179], v7 offset:2048
v_mfma_f32_16x16x32_bf16 v[188:191], a[160:163], a[204:207], v[188:191]
buffer_load_dwordx4 v30, s[40:43], s35 offen lds
v_mfma_f32_16x16x32_bf16 v[204:207], a[164:167], a[204:207], v[204:207]
s_add_u32 m0, s61, 0x15000
ds_read_b64_tr_b16 a[180:181], v8 offset:0
v_mfma_f32_16x16x32_bf16 v[200:203], a[164:167], a[200:203], v[200:203]
ds_read_b64_tr_b16 a[182:183], v8 offset:2048
ds_read_b128 a[208:211], v9 offset:4096
v_mfma_f32_16x16x32_bf16 v[196:199], a[164:167], a[196:199], v[196:199]
buffer_load_dwordx4 v31, s[40:43], s35 offen lds
v_mfma_f32_16x16x32_bf16 v[192:195], a[164:167], a[192:195], v[192:195]
ds_read_b128 a[212:215], v9 offset:5120
s_waitcnt lgkmcnt(6)
v_mfma_f32_16x16x32_bf16 v[208:211], a[168:171], a[192:195], v[208:211]
s_add_u32 m0, s53, 0x3000
s_waitcnt vmcnt(9)
ds_read_b64_tr_b16 a[184:185], v3 offset:4096
v_mfma_f32_16x16x32_bf16 v[212:215], a[168:171], a[196:199], v[212:215]
buffer_load_dwordx4 v71, s[40:43], s66 offen offset:3072 lds
v_mfma_f32_16x16x32_bf16 v[216:219], a[168:171], a[200:203], v[216:219]
s_add_u32 s66, s66, 0x12000
ds_read_b64_tr_b16 a[186:187], v3 offset:6144
v_mfma_f32_16x16x32_bf16 v[220:223], a[168:171], a[204:207], v[220:223]
s_cmp_eq_u32 s66, s74
s_cselect_b32 s66, s70, s66
v_mfma_f32_16x16x32_bf16 v[236:239], a[172:175], a[204:207], v[236:239]
s_add_u32 m0, s53, 0
ds_read_b64_tr_b16 a[188:189], v4 offset:4096
v_mfma_f32_16x16x32_bf16 v[232:235], a[172:175], a[200:203], v[232:235]
buffer_load_dwordx4 v64, s[40:43], s66 offen lds
v_mfma_f32_16x16x32_bf16 v[228:231], a[172:175], a[196:199], v[228:231]
ds_read_b64_tr_b16 a[190:191], v4 offset:6144
ds_read_b128 a[216:219], v10 offset:4096
v_mfma_f32_16x16x32_bf16 v[224:227], a[172:175], a[192:195], v[224:227]
ds_read_b128 a[220:223], v10 offset:5120
s_waitcnt lgkmcnt(8)
v_mfma_f32_16x16x32_bf16 v[240:243], a[176:179], a[192:195], v[240:243]
buffer_load_dwordx4 v65, s[40:43], s66 offen offset:1024 lds
v_mfma_f32_16x16x32_bf16 v[244:247], a[176:179], a[196:199], v[244:247]
ds_read_b64_tr_b16 a[160:161], v5 offset:4096
ds_read_b64_tr_b16 a[162:163], v5 offset:6144
v_mfma_f32_16x16x32_bf16 v[252:255], a[180:183], a[196:199], v[252:255]
ds_read_b64_tr_b16 a[164:165], v6 offset:4096
ds_read_b64_tr_b16 a[166:167], v6 offset:6144
v_mfma_f32_16x16x32_bf16 v[248:251], a[180:183], a[192:195], v[248:251]
s_waitcnt lgkmcnt(4)
v_mfma_f32_16x16x32_bf16 v[176:179], a[184:187], a[208:211], v[176:179]
buffer_load_dwordx4 v66, s[40:43], s66 offen offset:2048 lds
v_mfma_f32_16x16x32_bf16 v[180:183], a[184:187], a[212:215], v[180:183]
ds_read_b64_tr_b16 a[168:169], v7 offset:4096
s_add_u32 s55, s55, 0x2000
v_mfma_f32_16x16x32_bf16 v[184:187], a[184:187], a[216:219], v[184:187]
ds_read_b64_tr_b16 a[170:171], v7 offset:6144
v_mfma_f32_16x16x32_bf16 v[188:191], a[184:187], a[220:223], v[188:191]
buffer_load_dwordx4 v67, s[40:43], s66 offen offset:3072 lds
v_mfma_f32_16x16x32_bf16 v[204:207], a[188:191], a[220:223], v[204:207]
ds_read_b64_tr_b16 a[172:173], v8 offset:4096
ds_read_b64_tr_b16 a[174:175], v8 offset:6144
v_mfma_f32_16x16x32_bf16 v[200:203], a[188:191], a[216:219], v[200:203]
s_cmp_eq_u32 s55, 0x12000
s_cselect_b32 s55, 0, s55
v_mfma_f32_16x16x32_bf16 v[196:199], a[188:191], a[212:215], v[196:199]
v_add_u32 v9, s55, v14
v_add_u32 v10, s55, v15
v_mfma_f32_16x16x32_bf16 v[192:195], a[188:191], a[208:211], v[192:195]
ds_read_b128 a[192:195], v9 offset:0
s_waitcnt lgkmcnt(5)
v_mfma_f32_16x16x32_bf16 v[208:211], a[160:163], a[208:211], v[208:211]
s_add_u32 m0, s53, 0x1000
s_waitcnt vmcnt(10)
ds_read_b64_tr_b16 a[176:177], v3 offset:8192
v_mfma_f32_16x16x32_bf16 v[212:215], a[160:163], a[212:215], v[212:215]
buffer_load_dwordx4 v68, s[40:43], s66 offen lds
v_mfma_f32_16x16x32_bf16 v[216:219], a[160:163], a[216:219], v[216:219]
ds_read_b64_tr_b16 a[178:179], v3 offset:10240
ds_read_b128 a[196:199], v9 offset:1024
v_mfma_f32_16x16x32_bf16 v[220:223], a[160:163], a[220:223], v[220:223]
ds_read_b128 a[200:203], v10 offset:0
ds_read_b128 a[204:207], v10 offset:1024
v_mfma_f32_16x16x32_bf16 v[236:239], a[164:167], a[220:223], v[236:239]
buffer_load_dwordx4 v69, s[40:43], s66 offen offset:1024 lds
v_mfma_f32_16x16x32_bf16 v[232:235], a[164:167], a[216:219], v[232:235]
ds_read_b64_tr_b16 a[180:181], v4 offset:8192
ds_read_b64_tr_b16 a[182:183], v4 offset:10240
v_mfma_f32_16x16x32_bf16 v[228:231], a[164:167], a[212:215], v[228:231]
v_mfma_f32_16x16x32_bf16 v[224:227], a[164:167], a[208:211], v[224:227]
s_waitcnt lgkmcnt(8)
v_mfma_f32_16x16x32_bf16 v[240:243], a[168:171], a[208:211], v[240:243]
buffer_load_dwordx4 v70, s[40:43], s66 offen offset:2048 lds
v_mfma_f32_16x16x32_bf16 v[244:247], a[168:171], a[212:215], v[244:247]
ds_read_b64_tr_b16 a[184:185], v5 offset:8192
ds_read_b64_tr_b16 a[186:187], v5 offset:10240
v_mfma_f32_16x16x32_bf16 v[252:255], a[172:175], a[212:215], v[252:255]
ds_read_b64_tr_b16 a[188:189], v6 offset:8192
ds_read_b64_tr_b16 a[190:191], v6 offset:10240
v_mfma_f32_16x16x32_bf16 v[248:251], a[172:175], a[208:211], v[248:251]
s_waitcnt lgkmcnt(4)
s_add_u32 s54, s54, -1
s_cbranch_scc1 .Lk2_D1
s_waitcnt vmcnt(8)
s_barrier
v_mfma_f32_16x16x32_bf16 v[176:179], a[176:179], a[192:195], v[176:179]
s_add_u32 s35, s35, 0x12000
ds_read_b64_tr_b16 a[160:161], v7 offset:8192
v_mfma_f32_16x16x32_bf16 v[180:183], a[176:179], a[196:199], v[180:183]
s_cmp_eq_u32 s35, s74
s_cselect_b32 s35, s70, s35
v_mfma_f32_16x16x32_bf16 v[184:187], a[176:179], a[200:203], v[184:187]
s_add_u32 m0, s61, 0x12000
ds_read_b64_tr_b16 a[162:163], v7 offset:10240
v_mfma_f32_16x16x32_bf16 v[188:191], a[176:179], a[204:207], v[188:191]
buffer_load_dwordx4 v30, s[40:43], s35 offen lds
v_mfma_f32_16x16x32_bf16 v[204:207], a[180:183], a[204:207], v[204:207]
s_add_u32 m0, s61, 0x13000
ds_read_b64_tr_b16 a[164:165], v8 offset:8192
v_mfma_f32_16x16x32_bf16 v[200:203], a[180:183], a[200:203], v[200:203]
ds_read_b64_tr_b16 a[166:167], v8 offset:10240
ds_read_b128 a[208:211], v9 offset:4096
v_mfma_f32_16x16x32_bf16 v[196:199], a[180:183], a[196:199], v[196:199]
buffer_load_dwordx4 v31, s[40:43], s35 offen lds
v_mfma_f32_16x16x32_bf16 v[192:195], a[180:183], a[192:195], v[192:195]
ds_read_b128 a[212:215], v9 offset:5120
s_waitcnt lgkmcnt(6)
v_mfma_f32_16x16x32_bf16 v[208:211], a[184:187], a[192:195], v[208:211]
s_add_u32 m0, s53, 0x1000
s_waitcnt vmcnt(9)
ds_read_b64_tr_b16 a[168:169], v3 offset:12288
v_mfma_f32_16x16x32_bf16 v[212:215], a[184:187], a[196:199], v[212:215]
buffer_load_dwordx4 v71, s[40:43], s66 offen offset:3072 lds
v_mfma_f32_16x16x32_bf16 v[216:219], a[184:187], a[200:203], v[216:219]
s_add_u32 m0, s53, 0x2000
ds_read_b64_tr_b16 a[170:171], v3 offset:14336
v_mfma_f32_16x16x32_bf16 v[220:223], a[184:187], a[204:207], v[220:223]
buffer_load_dwordx4 v64, s[40:43], s67 offen lds
v_mfma_f32_16x16x32_bf16 v[236:239], a[188:191], a[204:207], v[236:239]
ds_read_b64_tr_b16 a[172:173], v4 offset:12288
ds_read_b128 a[216:219], v10 offset:4096
v_mfma_f32_16x16x32_bf16 v[232:235], a[188:191], a[200:203], v[232:235]
ds_read_b64_tr_b16 a[174:175], v4 offset:14336
ds_read_b128 a[220:223], v10 offset:5120
v_mfma_f32_16x16x32_bf16 v[228:231], a[188:191], a[196:199], v[228:231]
buffer_load_dwordx4 v65, s[40:43], s67 offen offset:1024 lds
v_mfma_f32_16x16x32_bf16 v[224:227], a[188:191], a[192:195], v[224:227]
s_waitcnt lgkmcnt(8)
v_mfma_f32_16x16x32_bf16 v[240:243], a[160:163], a[192:195], v[240:243]
ds_read_b64_tr_b16 a[176:177], v5 offset:12288
ds_read_b64_tr_b16 a[178:179], v5 offset:14336
v_mfma_f32_16x16x32_bf16 v[244:247], a[160:163], a[196:199], v[244:247]
buffer_load_dwordx4 v66, s[40:43], s67 offen offset:2048 lds
v_mfma_f32_16x16x32_bf16 v[252:255], a[164:167], a[196:199], v[252:255]
ds_read_b64_tr_b16 a[180:181], v6 offset:12288
ds_read_b64_tr_b16 a[182:183], v6 offset:14336
v_mfma_f32_16x16x32_bf16 v[248:251], a[164:167], a[192:195], v[248:251]
)ASM"
R"ASM(
s_waitcnt lgkmcnt(4)
v_mfma_f32_16x16x32_bf16 v[176:179], a[168:171], a[208:211], v[176:179]
buffer_load_dwordx4 v67, s[40:43], s67 offen offset:3072 lds
v_mfma_f32_16x16x32_bf16 v[180:183], a[168:171], a[212:215], v[180:183]
ds_read_b64_tr_b16 a[184:185], v7 offset:12288
s_add_u32 s55, s55, 0x2000
v_mfma_f32_16x16x32_bf16 v[184:187], a[168:171], a[216:219], v[184:187]
ds_read_b64_tr_b16 a[186:187], v7 offset:14336
v_mfma_f32_16x16x32_bf16 v[188:191], a[168:171], a[220:223], v[188:191]
s_cmp_eq_u32 s55, 0x12000
s_cselect_b32 s55, 0, s55
v_mfma_f32_16x16x32_bf16 v[204:207], a[172:175], a[220:223], v[204:207]
ds_read_b64_tr_b16 a[188:189], v8 offset:12288
v_add_u32 v9, s55, v14
v_mfma_f32_16x16x32_bf16 v[200:203], a[172:175], a[216:219], v[200:203]
ds_read_b64_tr_b16 a[190:191], v8 offset:14336
v_add_u32 v10, s55, v15
v_mfma_f32_16x16x32_bf16 v[196:199], a[172:175], a[212:215], v[196:199]
ds_read_b128 a[192:195], v9 offset:0
ds_read_b128 a[196:199], v9 offset:1024
v_mfma_f32_16x16x32_bf16 v[192:195], a[172:175], a[208:211], v[192:195]
ds_read_b128 a[200:203], v10 offset:0
s_waitcnt lgkmcnt(7)
v_mfma_f32_16x16x32_bf16 v[208:211], a[176:179], a[208:211], v[208:211]
s_add_u32 m0, s53, 0x3000
s_waitcnt vmcnt(10)
ds_read_b64_tr_b16 a[160:161], v3 offset:0
v_mfma_f32_16x16x32_bf16 v[212:215], a[176:179], a[212:215], v[212:215]
buffer_load_dwordx4 v68, s[40:43], s67 offen lds
v_mfma_f32_16x16x32_bf16 v[216:219], a[176:179], a[216:219], v[216:219]
ds_read_b64_tr_b16 a[162:163], v3 offset:2048
ds_read_b128 a[204:207], v10 offset:1024
v_mfma_f32_16x16x32_bf16 v[220:223], a[176:179], a[220:223], v[220:223]
v_mfma_f32_16x16x32_bf16 v[236:239], a[180:183], a[220:223], v[236:239]
buffer_load_dwordx4 v69, s[40:43], s67 offen offset:1024 lds
v_mfma_f32_16x16x32_bf16 v[232:235], a[180:183], a[216:219], v[232:235]
ds_read_b64_tr_b16 a[164:165], v4 offset:0
ds_read_b64_tr_b16 a[166:167], v4 offset:2048
v_mfma_f32_16x16x32_bf16 v[228:231], a[180:183], a[212:215], v[228:231]
v_mfma_f32_16x16x32_bf16 v[224:227], a[180:183], a[208:211], v[224:227]
s_waitcnt lgkmcnt(8)
v_mfma_f32_16x16x32_bf16 v[240:243], a[184:187], a[208:211], v[240:243]
buffer_load_dwordx4 v70, s[40:43], s67 offen offset:2048 lds
v_mfma_f32_16x16x32_bf16 v[244:247], a[184:187], a[212:215], v[244:247]
ds_read_b64_tr_b16 a[168:169], v5 offset:0
ds_read_b64_tr_b16 a[170:171], v5 offset:2048
v_mfma_f32_16x16x32_bf16 v[252:255], a[188:191], a[212:215], v[252:255]
ds_read_b64_tr_b16 a[172:173], v6 offset:0
ds_read_b64_tr_b16 a[174:175], v6 offset:2048
v_mfma_f32_16x16x32_bf16 v[248:251], a[188:191], a[208:211], v[248:251]
s_waitcnt lgkmcnt(4)
s_waitcnt vmcnt(8)
s_barrier
v_mfma_f32_16x16x32_bf16 v[176:179], a[160:163], a[192:195], v[176:179]
buffer_load_dwordx4 v71, s[40:43], s67 offen offset:3072 lds
v_mfma_f32_16x16x32_bf16 v[180:183], a[160:163], a[196:199], v[180:183]
ds_read_b64_tr_b16 a[176:177], v7 offset:0
ds_read_b128 a[208:211], v9 offset:4096
v_mfma_f32_16x16x32_bf16 v[184:187], a[160:163], a[200:203], v[184:187]
ds_read_b64_tr_b16 a[178:179], v7 offset:2048
ds_read_b128 a[212:215], v9 offset:5120
v_mfma_f32_16x16x32_bf16 v[188:191], a[160:163], a[204:207], v[188:191]
ds_read_b128 a[216:219], v10 offset:4096
ds_read_b128 a[220:223], v10 offset:5120
v_mfma_f32_16x16x32_bf16 v[204:207], a[164:167], a[204:207], v[204:207]
ds_read_b64_tr_b16 a[180:181], v8 offset:0
ds_read_b64_tr_b16 a[182:183], v8 offset:2048
v_mfma_f32_16x16x32_bf16 v[200:203], a[164:167], a[200:203], v[200:203]
v_mfma_f32_16x16x32_bf16 v[196:199], a[164:167], a[196:199], v[196:199]
v_mfma_f32_16x16x32_bf16 v[192:195], a[164:167], a[192:195], v[192:195]
s_waitcnt lgkmcnt(8)
v_mfma_f32_16x16x32_bf16 v[208:211], a[168:171], a[192:195], v[208:211]
s_add_u32 s67, s67, 0x12000
s_waitcnt vmcnt(8)
ds_read_b64_tr_b16 a[184:185], v3 offset:4096
v_mfma_f32_16x16x32_bf16 v[212:215], a[168:171], a[196:199], v[212:215]
s_cmp_eq_u32 s67, s75
s_cselect_b32 s67, s71, s67
v_mfma_f32_16x16x32_bf16 v[216:219], a[168:171], a[200:203], v[216:219]
s_add_u32 m0, s53, 0
ds_read_b64_tr_b16 a[186:187], v3 offset:6144
v_mfma_f32_16x16x32_bf16 v[220:223], a[168:171], a[204:207], v[220:223]
buffer_load_dwordx4 v64, s[40:43], s67 offen lds
v_mfma_f32_16x16x32_bf16 v[236:239], a[172:175], a[204:207], v[236:239]
ds_read_b64_tr_b16 a[188:189], v4 offset:4096
ds_read_b64_tr_b16 a[190:191], v4 offset:6144
v_mfma_f32_16x16x32_bf16 v[232:235], a[172:175], a[200:203], v[232:235]
v_mfma_f32_16x16x32_bf16 v[228:231], a[172:175], a[196:199], v[228:231]
buffer_load_dwordx4 v65, s[40:43], s67 offen offset:1024 lds
v_mfma_f32_16x16x32_bf16 v[224:227], a[172:175], a[192:195], v[224:227]
s_waitcnt lgkmcnt(4)
v_mfma_f32_16x16x32_bf16 v[240:243], a[176:179], a[192:195], v[240:243]
ds_read_b64_tr_b16 a[160:161], v5 offset:4096
ds_read_b64_tr_b16 a[162:163], v5 offset:6144
v_mfma_f32_16x16x32_bf16 v[244:247], a[176:179], a[196:199], v[244:247]
buffer_load_dwordx4 v66, s[40:43], s67 offen offset:2048 lds
v_mfma_f32_16x16x32_bf16 v[252:255], a[180:183], a[196:199], v[252:255]
ds_read_b64_tr_b16 a[164:165], v6 offset:4096
ds_read_b64_tr_b16 a[166:167], v6 offset:6144
v_mfma_f32_16x16x32_bf16 v[248:251], a[180:183], a[192:195], v[248:251]
s_waitcnt lgkmcnt(4)
v_mfma_f32_16x16x32_bf16 v[176:179], a[184:187], a[208:211], v[176:179]
buffer_load_dwordx4 v67, s[40:43], s67 offen offset:3072 lds
v_mfma_f32_16x16x32_bf16 v[180:183], a[184:187], a[212:215], v[180:183]
ds_read_b64_tr_b16 a[168:169], v7 offset:4096
s_add_u32 s55, s55, 0x2000
v_mfma_f32_16x16x32_bf16 v[184:187], a[184:187], a[216:219], v[184:187]
ds_read_b64_tr_b16 a[170:171], v7 offset:6144
v_mfma_f32_16x16x32_bf16 v[188:191], a[184:187], a[220:223], v[188:191]
s_cmp_eq_u32 s55, 0x12000
s_cselect_b32 s55, 0, s55
v_mfma_f32_16x16x32_bf16 v[204:207], a[188:191], a[220:223], v[204:207]
ds_read_b64_tr_b16 a[172:173], v8 offset:4096
v_add_u32 v9, s55, v14
v_mfma_f32_16x16x32_bf16 v[200:203], a[188:191], a[216:219], v[200:203]
ds_read_b64_tr_b16 a[174:175], v8 offset:6144
v_add_u32 v10, s55, v15
v_mfma_f32_16x16x32_bf16 v[196:199], a[188:191], a[212:215], v[196:199]
ds_read_b128 a[192:195], v9 offset:0
ds_read_b128 a[196:199], v9 offset:1024
v_mfma_f32_16x16x32_bf16 v[192:195], a[188:191], a[208:211], v[192:195]
ds_read_b128 a[200:203], v10 offset:0
s_waitcnt lgkmcnt(7)
v_mfma_f32_16x16x32_bf16 v[208:211], a[160:163], a[208:211], v[208:211]
s_add_u32 m0, s53, 0x1000
s_waitcnt vmcnt(8)
ds_read_b64_tr_b16 a[224:225], v3 offset:8192
v_mfma_f32_16x16x32_bf16 v[212:215], a[160:163], a[212:215], v[212:215]
buffer_load_dwordx4 v68, s[40:43], s67 offen lds
v_mfma_f32_16x16x32_bf16 v[216:219], a[160:163], a[216:219], v[216:219]
ds_read_b64_tr_b16 a[226:227], v3 offset:10240
ds_read_b128 a[204:207], v10 offset:1024
v_mfma_f32_16x16x32_bf16 v[220:223], a[160:163], a[220:223], v[220:223]
v_mfma_f32_16x16x32_bf16 v[236:239], a[164:167], a[220:223], v[236:239]
buffer_load_dwordx4 v69, s[40:43], s67 offen offset:1024 lds
v_mfma_f32_16x16x32_bf16 v[232:235], a[164:167], a[216:219], v[232:235]
ds_read_b64_tr_b16 a[228:229], v4 offset:8192
ds_read_b64_tr_b16 a[230:231], v4 offset:10240
v_mfma_f32_16x16x32_bf16 v[228:231], a[164:167], a[212:215], v[228:231]
v_mfma_f32_16x16x32_bf16 v[224:227], a[164:167], a[208:211], v[224:227]
s_waitcnt lgkmcnt(8)
v_mfma_f32_16x16x32_bf16 v[240:243], a[168:171], a[208:211], v[240:243]
buffer_load_dwordx4 v70, s[40:43], s67 offen offset:2048 lds
v_mfma_f32_16x16x32_bf16 v[244:247], a[168:171], a[212:215], v[244:247]
ds_read_b64_tr_b16 a[232:233], v5 offset:8192
ds_read_b64_tr_b16 a[234:235], v5 offset:10240
v_mfma_f32_16x16x32_bf16 v[252:255], a[172:175], a[212:215], v[252:255]
ds_read_b64_tr_b16 a[236:237], v6 offset:8192
ds_read_b64_tr_b16 a[238:239], v6 offset:10240
v_mfma_f32_16x16x32_bf16 v[248:251], a[172:175], a[208:211], v[248:251]
buffer_load_dwordx4 v71, s[40:43], s67 offen offset:3072 lds
s_waitcnt lgkmcnt(7)
v_mfma_f32_16x16x32_bf16 v[112:115], a[224:227], a[192:195], 0
s_waitcnt vmcnt(8)
ds_read_b64_tr_b16 a[240:241], v3 offset:12288
v_mfma_f32_16x16x32_bf16 v[116:119], a[224:227], a[196:199], 0
ds_read_b64_tr_b16 a[242:243], v3 offset:14336
ds_read_b128 a[208:211], v9 offset:4096
ds_read_b128 a[212:215], v9 offset:5120
v_mfma_f32_16x16x32_bf16 v[120:123], a[224:227], a[200:203], 0
ds_read_b128 a[216:219], v10 offset:4096
ds_read_b128 a[220:223], v10 offset:5120
s_waitcnt lgkmcnt(12)
v_mfma_f32_16x16x32_bf16 v[124:127], a[224:227], a[204:207], 0
s_waitcnt lgkmcnt(10)
v_mfma_f32_16x16x32_bf16 v[140:143], a[228:231], a[204:207], 0
ds_read_b64_tr_b16 a[244:245], v4 offset:12288
ds_read_b64_tr_b16 a[246:247], v4 offset:14336
v_mfma_f32_16x16x32_bf16 v[136:139], a[228:231], a[200:203], 0
v_cvt_pk_bf16_f32 v240, v240, v241
v_mfma_f32_16x16x32_bf16 v[132:135], a[228:231], a[196:199], 0
v_cvt_pk_bf16_f32 v241, v242, v243
v_mfma_f32_16x16x32_bf16 v[128:131], a[228:231], a[192:195], 0
s_waitcnt lgkmcnt(8)
v_mfma_f32_16x16x32_bf16 v[144:147], a[232:235], a[192:195], 0
s_add_u32 s67, s67, 0x12000
ds_read_b64_tr_b16 a[248:249], v5 offset:12288
v_mfma_f32_16x16x32_bf16 v[148:151], a[232:235], a[196:199], 0
s_cmp_eq_u32 s67, s75
s_cselect_b32 s67, s71, s67
ds_read_b64_tr_b16 a[250:251], v5 offset:14336
v_mfma_f32_16x16x32_bf16 v[152:155], a[232:235], a[200:203], 0
s_add_u32 m0, s53, 0x2000
v_cvt_pk_bf16_f32 v242, v248, v249
v_mfma_f32_16x16x32_bf16 v[156:159], a[232:235], a[204:207], 0
buffer_load_dwordx4 v64, s[40:43], s67 offen lds
v_cvt_pk_bf16_f32 v243, v250, v251
v_mfma_f32_16x16x32_bf16 v[172:175], a[236:239], a[204:207], 0
ds_read_b64_tr_b16 a[252:253], v6 offset:12288
ds_read_b64_tr_b16 a[254:255], v6 offset:14336
v_mfma_f32_16x16x32_bf16 v[168:171], a[236:239], a[200:203], 0
buffer_store_dwordx4 v[240:243], v14, s[48:51], s97 offen sc1
v_cvt_pk_bf16_f32 v244, v244, v245
v_mfma_f32_16x16x32_bf16 v[164:167], a[236:239], a[196:199], 0
buffer_load_dwordx4 v65, s[40:43], s67 offen offset:1024 lds
v_mfma_f32_16x16x32_bf16 v[160:163], a[236:239], a[192:195], 0
v_cvt_pk_bf16_f32 v245, v246, v247
s_waitcnt lgkmcnt(4)
v_mfma_f32_16x16x32_bf16 v[112:115], a[240:243], a[208:211], v[112:115]
s_waitcnt vmcnt(7)
)ASM"
R"ASM(
ds_read_b64_tr_b16 a[224:225], v3 offset:0
v_mfma_f32_16x16x32_bf16 v[116:119], a[240:243], a[212:215], v[116:119]
buffer_load_dwordx4 v66, s[40:43], s67 offen offset:2048 lds
s_add_u32 s55, s55, 0x2000
v_mfma_f32_16x16x32_bf16 v[120:123], a[240:243], a[216:219], v[120:123]
ds_read_b64_tr_b16 a[226:227], v3 offset:2048
v_mfma_f32_16x16x32_bf16 v[124:127], a[240:243], a[220:223], v[124:127]
s_cmp_eq_u32 s55, 0x12000
s_cselect_b32 s55, 0, s55
v_add_u32 v9, s55, v14
v_mfma_f32_16x16x32_bf16 v[140:143], a[244:247], a[220:223], v[140:143]
buffer_load_dwordx4 v67, s[40:43], s67 offen offset:3072 lds
v_mfma_f32_16x16x32_bf16 v[136:139], a[244:247], a[216:219], v[136:139]
ds_read_b64_tr_b16 a[228:229], v4 offset:0
v_add_u32 v10, s55, v15
ds_read_b128 a[192:195], v9 offset:0
v_mfma_f32_16x16x32_bf16 v[132:135], a[244:247], a[212:215], v[132:135]
ds_read_b64_tr_b16 a[230:231], v4 offset:2048
ds_read_b128 a[196:199], v9 offset:1024
v_mfma_f32_16x16x32_bf16 v[128:131], a[244:247], a[208:211], v[128:131]
ds_read_b128 a[200:203], v10 offset:0
ds_read_b128 a[204:207], v10 offset:1024
s_waitcnt lgkmcnt(8)
v_mfma_f32_16x16x32_bf16 v[144:147], a[248:251], a[208:211], v[144:147]
s_add_u32 m0, s53, 0x3000
ds_read_b64_tr_b16 a[232:233], v5 offset:0
v_mfma_f32_16x16x32_bf16 v[148:151], a[248:251], a[212:215], v[148:151]
buffer_load_dwordx4 v68, s[40:43], s67 offen lds
ds_read_b64_tr_b16 a[234:235], v5 offset:2048
v_mfma_f32_16x16x32_bf16 v[152:155], a[248:251], a[216:219], v[152:155]
v_cvt_pk_bf16_f32 v246, v252, v253
v_cvt_pk_bf16_f32 v247, v254, v255
v_mfma_f32_16x16x32_bf16 v[156:159], a[248:251], a[220:223], v[156:159]
buffer_store_dwordx4 v[244:247], v14, s[48:51], s97 offen offset:1024 sc1
v_cvt_pk_bf16_f32 v176, v176, v177
v_mfma_f32_16x16x32_bf16 v[172:175], a[252:255], a[220:223], v[172:175]
buffer_load_dwordx4 v69, s[40:43], s67 offen offset:1024 lds
v_mfma_f32_16x16x32_bf16 v[168:171], a[252:255], a[216:219], v[168:171]
ds_read_b64_tr_b16 a[236:237], v6 offset:0
ds_read_b64_tr_b16 a[238:239], v6 offset:2048
v_cvt_pk_bf16_f32 v177, v178, v179
v_mfma_f32_16x16x32_bf16 v[164:167], a[252:255], a[212:215], v[164:167]
v_cvt_pk_bf16_f32 v178, v192, v193
v_cvt_pk_bf16_f32 v179, v194, v195
v_mfma_f32_16x16x32_bf16 v[160:163], a[252:255], a[208:211], v[160:163]
buffer_store_dwordx4 v[176:179], v14, s[48:51], s76 offen sc1
s_waitcnt lgkmcnt(4)
v_mfma_f32_16x16x32_bf16 v[112:115], a[224:227], a[192:195], v[112:115]
buffer_load_dwordx4 v70, s[40:43], s67 offen offset:2048 lds
v_mfma_f32_16x16x32_bf16 v[116:119], a[224:227], a[196:199], v[116:119]
s_waitcnt vmcnt(10)
ds_read_b64_tr_b16 a[240:241], v3 offset:4096
ds_read_b128 a[208:211], v9 offset:4096
v_mfma_f32_16x16x32_bf16 v[120:123], a[224:227], a[200:203], v[120:123]
ds_read_b64_tr_b16 a[242:243], v3 offset:6144
ds_read_b128 a[212:215], v9 offset:5120
v_mfma_f32_16x16x32_bf16 v[124:127], a[224:227], a[204:207], v[124:127]
buffer_load_dwordx4 v71, s[40:43], s67 offen offset:3072 lds
ds_read_b128 a[216:219], v10 offset:4096
v_mfma_f32_16x16x32_bf16 v[140:143], a[228:231], a[204:207], v[140:143]
ds_read_b64_tr_b16 a[244:245], v4 offset:4096
ds_read_b128 a[220:223], v10 offset:5120
v_mfma_f32_16x16x32_bf16 v[136:139], a[228:231], a[200:203], v[136:139]
ds_read_b64_tr_b16 a[246:247], v4 offset:6144
v_cvt_pk_bf16_f32 v180, v180, v181
v_cvt_pk_bf16_f32 v181, v182, v183
v_mfma_f32_16x16x32_bf16 v[132:135], a[228:231], a[196:199], v[132:135]
v_cvt_pk_bf16_f32 v182, v196, v197
v_cvt_pk_bf16_f32 v183, v198, v199
v_mfma_f32_16x16x32_bf16 v[128:131], a[228:231], a[192:195], v[128:131]
buffer_store_dwordx4 v[180:183], v14, s[48:51], s76 offen offset:1024 sc1
s_waitcnt lgkmcnt(8)
v_mfma_f32_16x16x32_bf16 v[144:147], a[232:235], a[192:195], v[144:147]
s_add_u32 s67, s67, 0x12000
ds_read_b64_tr_b16 a[248:249], v5 offset:4096
v_mfma_f32_16x16x32_bf16 v[148:151], a[232:235], a[196:199], v[148:151]
s_cmp_eq_u32 s67, s75
s_cselect_b32 s67, s71, s67
ds_read_b64_tr_b16 a[250:251], v5 offset:6144
v_mfma_f32_16x16x32_bf16 v[152:155], a[232:235], a[200:203], v[152:155]
s_add_u32 m0, s53, 0
v_cvt_pk_bf16_f32 v184, v184, v185
v_mfma_f32_16x16x32_bf16 v[156:159], a[232:235], a[204:207], v[156:159]
buffer_load_dwordx4 v64, s[40:43], s67 offen lds
v_cvt_pk_bf16_f32 v185, v186, v187
v_mfma_f32_16x16x32_bf16 v[172:175], a[236:239], a[204:207], v[172:175]
ds_read_b64_tr_b16 a[252:253], v6 offset:4096
ds_read_b64_tr_b16 a[254:255], v6 offset:6144
v_mfma_f32_16x16x32_bf16 v[168:171], a[236:239], a[200:203], v[168:171]
v_cvt_pk_bf16_f32 v186, v200, v201
v_cvt_pk_bf16_f32 v187, v202, v203
v_mfma_f32_16x16x32_bf16 v[164:167], a[236:239], a[196:199], v[164:167]
buffer_load_dwordx4 v65, s[40:43], s67 offen offset:1024 lds
v_mfma_f32_16x16x32_bf16 v[160:163], a[236:239], a[192:195], v[160:163]
buffer_store_dwordx4 v[184:187], v15, s[48:51], s76 offen sc1
s_waitcnt lgkmcnt(4)
v_mfma_f32_16x16x32_bf16 v[112:115], a[240:243], a[208:211], v[112:115]
s_waitcnt vmcnt(10)
ds_read_b64_tr_b16 a[224:225], v3 offset:8192
v_mfma_f32_16x16x32_bf16 v[116:119], a[240:243], a[212:215], v[116:119]
buffer_load_dwordx4 v66, s[40:43], s67 offen offset:2048 lds
s_add_u32 s55, s55, 0x2000
v_mfma_f32_16x16x32_bf16 v[120:123], a[240:243], a[216:219], v[120:123]
ds_read_b64_tr_b16 a[226:227], v3 offset:10240
v_mfma_f32_16x16x32_bf16 v[124:127], a[240:243], a[220:223], v[124:127]
s_cmp_eq_u32 s55, 0x12000
s_cselect_b32 s55, 0, s55
v_add_u32 v9, s55, v14
v_mfma_f32_16x16x32_bf16 v[140:143], a[244:247], a[220:223], v[140:143]
buffer_load_dwordx4 v67, s[40:43], s67 offen offset:3072 lds
v_mfma_f32_16x16x32_bf16 v[136:139], a[244:247], a[216:219], v[136:139]
ds_read_b64_tr_b16 a[228:229], v4 offset:8192
v_add_u32 v10, s55, v15
ds_read_b128 a[192:195], v9 offset:0
v_mfma_f32_16x16x32_bf16 v[132:135], a[244:247], a[212:215], v[132:135]
ds_read_b64_tr_b16 a[230:231], v4 offset:10240
ds_read_b128 a[196:199], v9 offset:1024
v_mfma_f32_16x16x32_bf16 v[128:131], a[244:247], a[208:211], v[128:131]
ds_read_b128 a[200:203], v10 offset:0
ds_read_b128 a[204:207], v10 offset:1024
s_waitcnt lgkmcnt(8)
v_mfma_f32_16x16x32_bf16 v[144:147], a[248:251], a[208:211], v[144:147]
s_add_u32 m0, s53, 0x1000
ds_read_b64_tr_b16 a[232:233], v5 offset:8192
v_mfma_f32_16x16x32_bf16 v[148:151], a[248:251], a[212:215], v[148:151]
buffer_load_dwordx4 v68, s[40:43], s67 offen lds
ds_read_b64_tr_b16 a[234:235], v5 offset:10240
v_mfma_f32_16x16x32_bf16 v[152:155], a[248:251], a[216:219], v[152:155]
v_cvt_pk_bf16_f32 v188, v188, v189
v_cvt_pk_bf16_f32 v189, v190, v191
v_mfma_f32_16x16x32_bf16 v[156:159], a[248:251], a[220:223], v[156:159]
v_cvt_pk_bf16_f32 v190, v204, v205
v_cvt_pk_bf16_f32 v191, v206, v207
buffer_store_dwordx4 v[188:191], v15, s[48:51], s76 offen offset:1024 sc1
v_mfma_f32_16x16x32_bf16 v[172:175], a[252:255], a[220:223], v[172:175]
buffer_load_dwordx4 v69, s[40:43], s67 offen offset:1024 lds
v_mfma_f32_16x16x32_bf16 v[168:171], a[252:255], a[216:219], v[168:171]
ds_read_b64_tr_b16 a[236:237], v6 offset:8192
ds_read_b64_tr_b16 a[238:239], v6 offset:10240
v_cvt_pk_bf16_f32 v208, v208, v209
v_mfma_f32_16x16x32_bf16 v[164:167], a[252:255], a[212:215], v[164:167]
v_cvt_pk_bf16_f32 v209, v210, v211
v_cvt_pk_bf16_f32 v210, v224, v225
v_mfma_f32_16x16x32_bf16 v[160:163], a[252:255], a[208:211], v[160:163]
v_cvt_pk_bf16_f32 v211, v226, v227
s_waitcnt lgkmcnt(4)
v_mfma_f32_16x16x32_bf16 v[112:115], a[224:227], a[192:195], v[112:115]
buffer_load_dwordx4 v70, s[40:43], s67 offen offset:2048 lds
v_mfma_f32_16x16x32_bf16 v[116:119], a[224:227], a[196:199], v[116:119]
s_waitcnt vmcnt(10)
ds_read_b64_tr_b16 a[240:241], v3 offset:12288
ds_read_b128 a[208:211], v9 offset:4096
v_mfma_f32_16x16x32_bf16 v[120:123], a[224:227], a[200:203], v[120:123]
ds_read_b64_tr_b16 a[242:243], v3 offset:14336
ds_read_b128 a[212:215], v9 offset:5120
v_mfma_f32_16x16x32_bf16 v[124:127], a[224:227], a[204:207], v[124:127]
buffer_load_dwordx4 v71, s[40:43], s67 offen offset:3072 lds
ds_read_b128 a[216:219], v10 offset:4096
v_mfma_f32_16x16x32_bf16 v[140:143], a[228:231], a[204:207], v[140:143]
ds_read_b64_tr_b16 a[244:245], v4 offset:12288
ds_read_b128 a[220:223], v10 offset:5120
v_mfma_f32_16x16x32_bf16 v[136:139], a[228:231], a[200:203], v[136:139]
ds_read_b64_tr_b16 a[246:247], v4 offset:14336
buffer_store_dwordx4 v[208:211], v14, s[48:51], s77 offen sc1
v_mfma_f32_16x16x32_bf16 v[132:135], a[228:231], a[196:199], v[132:135]
v_cvt_pk_bf16_f32 v212, v212, v213
v_cvt_pk_bf16_f32 v213, v214, v215
v_mfma_f32_16x16x32_bf16 v[128:131], a[228:231], a[192:195], v[128:131]
v_cvt_pk_bf16_f32 v214, v228, v229
v_cvt_pk_bf16_f32 v215, v230, v231
s_waitcnt lgkmcnt(8)
v_mfma_f32_16x16x32_bf16 v[144:147], a[232:235], a[192:195], v[144:147]
s_add_u32 s67, s67, 0x12000
ds_read_b64_tr_b16 a[248:249], v5 offset:12288
v_mfma_f32_16x16x32_bf16 v[148:151], a[232:235], a[196:199], v[148:151]
s_cmp_eq_u32 s67, s75
s_cselect_b32 s67, s71, s67
ds_read_b64_tr_b16 a[250:251], v5 offset:14336
v_mfma_f32_16x16x32_bf16 v[152:155], a[232:235], a[200:203], v[152:155]
s_add_u32 m0, s53, 0x2000
buffer_store_dwordx4 v[212:215], v14, s[48:51], s77 offen offset:1024 sc1
v_mfma_f32_16x16x32_bf16 v[156:159], a[232:235], a[204:207], v[156:159]
buffer_load_dwordx4 v64, s[40:43], s67 offen lds
v_cvt_pk_bf16_f32 v216, v216, v217
v_mfma_f32_16x16x32_bf16 v[172:175], a[236:239], a[204:207], v[172:175]
ds_read_b64_tr_b16 a[252:253], v6 offset:12288
ds_read_b64_tr_b16 a[254:255], v6 offset:14336
v_mfma_f32_16x16x32_bf16 v[168:171], a[236:239], a[200:203], v[168:171]
v_cvt_pk_bf16_f32 v217, v218, v219
v_mfma_f32_16x16x32_bf16 v[164:167], a[236:239], a[196:199], v[164:167]
buffer_load_dwordx4 v65, s[40:43], s67 offen offset:1024 lds
v_mfma_f32_16x16x32_bf16 v[160:163], a[236:239], a[192:195], v[160:163]
v_cvt_pk_bf16_f32 v218, v232, v233
s_waitcnt lgkmcnt(4)
v_mfma_f32_16x16x32_bf16 v[112:115], a[240:243], a[208:211], v[112:115]
s_waitcnt vmcnt(9)
ds_read_b64_tr_b16 a[224:225], v3 offset:0
v_mfma_f32_16x16x32_bf16 v[116:119], a[240:243], a[212:215], v[116:119]
buffer_load_dwordx4 v66, s[40:43], s67 offen offset:2048 lds
s_add_u32 s55, s55, 0x2000
)ASM"
R"ASM(
v_mfma_f32_16x16x32_bf16 v[120:123], a[240:243], a[216:219], v[120:123]
ds_read_b64_tr_b16 a[226:227], v3 offset:2048
v_mfma_f32_16x16x32_bf16 v[124:127], a[240:243], a[220:223], v[124:127]
s_cmp_eq_u32 s55, 0x12000
s_cselect_b32 s55, 0, s55
v_add_u32 v9, s55, v14
v_mfma_f32_16x16x32_bf16 v[140:143], a[244:247], a[220:223], v[140:143]
buffer_load_dwordx4 v67, s[40:43], s67 offen offset:3072 lds
v_mfma_f32_16x16x32_bf16 v[136:139], a[244:247], a[216:219], v[136:139]
ds_read_b64_tr_b16 a[228:229], v4 offset:0
v_add_u32 v10, s55, v15
ds_read_b128 a[192:195], v9 offset:0
v_mfma_f32_16x16x32_bf16 v[132:135], a[244:247], a[212:215], v[132:135]
ds_read_b64_tr_b16 a[230:231], v4 offset:2048
ds_read_b128 a[196:199], v9 offset:1024
v_mfma_f32_16x16x32_bf16 v[128:131], a[244:247], a[208:211], v[128:131]
ds_read_b128 a[200:203], v10 offset:0
ds_read_b128 a[204:207], v10 offset:1024
s_waitcnt lgkmcnt(8)
v_mfma_f32_16x16x32_bf16 v[144:147], a[248:251], a[208:211], v[144:147]
s_add_u32 m0, s53, 0x3000
ds_read_b64_tr_b16 a[232:233], v5 offset:0
v_mfma_f32_16x16x32_bf16 v[148:151], a[248:251], a[212:215], v[148:151]
buffer_load_dwordx4 v68, s[40:43], s67 offen lds
ds_read_b64_tr_b16 a[234:235], v5 offset:2048
v_mfma_f32_16x16x32_bf16 v[152:155], a[248:251], a[216:219], v[152:155]
v_cvt_pk_bf16_f32 v219, v234, v235
buffer_store_dwordx4 v[216:219], v15, s[48:51], s77 offen sc1
v_mfma_f32_16x16x32_bf16 v[156:159], a[248:251], a[220:223], v[156:159]
v_cvt_pk_bf16_f32 v220, v220, v221
v_cvt_pk_bf16_f32 v221, v222, v223
v_cvt_pk_bf16_f32 v222, v236, v237
v_mfma_f32_16x16x32_bf16 v[172:175], a[252:255], a[220:223], v[172:175]
buffer_load_dwordx4 v69, s[40:43], s67 offen offset:1024 lds
v_mfma_f32_16x16x32_bf16 v[168:171], a[252:255], a[216:219], v[168:171]
ds_read_b64_tr_b16 a[236:237], v6 offset:0
ds_read_b64_tr_b16 a[238:239], v6 offset:2048
v_cvt_pk_bf16_f32 v223, v238, v239
v_mfma_f32_16x16x32_bf16 v[164:167], a[252:255], a[212:215], v[164:167]
buffer_store_dwordx4 v[220:223], v15, s[48:51], s77 offen offset:1024 sc1
v_mfma_f32_16x16x32_bf16 v[160:163], a[252:255], a[208:211], v[160:163]
s_waitcnt lgkmcnt(4)
s_mov_b32 s54, 1
.Lk2_D2:
v_mfma_f32_16x16x32_bf16 v[112:115], a[224:227], a[192:195], v[112:115]
buffer_load_dwordx4 v70, s[40:43], s67 offen offset:2048 lds
v_mfma_f32_16x16x32_bf16 v[116:119], a[224:227], a[196:199], v[116:119]
s_waitcnt vmcnt(7)
ds_read_b64_tr_b16 a[240:241], v3 offset:4096
ds_read_b128 a[208:211], v9 offset:4096
v_mfma_f32_16x16x32_bf16 v[120:123], a[224:227], a[200:203], v[120:123]
ds_read_b64_tr_b16 a[242:243], v3 offset:6144
ds_read_b128 a[212:215], v9 offset:5120
v_mfma_f32_16x16x32_bf16 v[124:127], a[224:227], a[204:207], v[124:127]
buffer_load_dwordx4 v71, s[40:43], s67 offen offset:3072 lds
ds_read_b128 a[216:219], v10 offset:4096
v_mfma_f32_16x16x32_bf16 v[140:143], a[228:231], a[204:207], v[140:143]
ds_read_b64_tr_b16 a[244:245], v4 offset:4096
ds_read_b128 a[220:223], v10 offset:5120
v_mfma_f32_16x16x32_bf16 v[136:139], a[228:231], a[200:203], v[136:139]
ds_read_b64_tr_b16 a[246:247], v4 offset:6144
v_mfma_f32_16x16x32_bf16 v[132:135], a[228:231], a[196:199], v[132:135]
v_mfma_f32_16x16x32_bf16 v[128:131], a[228:231], a[192:195], v[128:131]
s_waitcnt lgkmcnt(8)
v_mfma_f32_16x16x32_bf16 v[144:147], a[232:235], a[192:195], v[144:147]
s_add_u32 s67, s67, 0x12000
ds_read_b64_tr_b16 a[248:249], v5 offset:4096
v_mfma_f32_16x16x32_bf16 v[148:151], a[232:235], a[196:199], v[148:151]
s_cmp_eq_u32 s67, s75
s_cselect_b32 s67, s71, s67
ds_read_b64_tr_b16 a[250:251], v5 offset:6144
v_mfma_f32_16x16x32_bf16 v[152:155], a[232:235], a[200:203], v[152:155]
s_add_u32 m0, s53, 0
v_mfma_f32_16x16x32_bf16 v[156:159], a[232:235], a[204:207], v[156:159]
buffer_load_dwordx4 v64, s[40:43], s67 offen lds
v_mfma_f32_16x16x32_bf16 v[172:175], a[236:239], a[204:207], v[172:175]
ds_read_b64_tr_b16 a[252:253], v6 offset:4096
ds_read_b64_tr_b16 a[254:255], v6 offset:6144
v_mfma_f32_16x16x32_bf16 v[168:171], a[236:239], a[200:203], v[168:171]
v_mfma_f32_16x16x32_bf16 v[164:167], a[236:239], a[196:199], v[164:167]
buffer_load_dwordx4 v65, s[40:43], s67 offen offset:1024 lds
v_mfma_f32_16x16x32_bf16 v[160:163], a[236:239], a[192:195], v[160:163]
s_waitcnt lgkmcnt(4)
v_mfma_f32_16x16x32_bf16 v[112:115], a[240:243], a[208:211], v[112:115]
s_waitcnt vmcnt(6)
ds_read_b64_tr_b16 a[224:225], v3 offset:8192
v_mfma_f32_16x16x32_bf16 v[116:119], a[240:243], a[212:215], v[116:119]
buffer_load_dwordx4 v66, s[40:43], s67 offen offset:2048 lds
s_add_u32 s55, s55, 0x2000
v_mfma_f32_16x16x32_bf16 v[120:123], a[240:243], a[216:219], v[120:123]
ds_read_b64_tr_b16 a[226:227], v3 offset:10240
v_mfma_f32_16x16x32_bf16 v[124:127], a[240:243], a[220:223], v[124:127]
s_cmp_eq_u32 s55, 0x12000
s_cselect_b32 s55, 0, s55
v_add_u32 v9, s55, v14
v_mfma_f32_16x16x32_bf16 v[140:143], a[244:247], a[220:223], v[140:143]
buffer_load_dwordx4 v67, s[40:43], s67 offen offset:3072 lds
v_mfma_f32_16x16x32_bf16 v[136:139], a[244:247], a[216:219], v[136:139]
ds_read_b64_tr_b16 a[228:229], v4 offset:8192
v_add_u32 v10, s55, v15
ds_read_b128 a[192:195], v9 offset:0
v_mfma_f32_16x16x32_bf16 v[132:135], a[244:247], a[212:215], v[132:135]
ds_read_b64_tr_b16 a[230:231], v4 offset:10240
ds_read_b128 a[196:199], v9 offset:1024
v_mfma_f32_16x16x32_bf16 v[128:131], a[244:247], a[208:211], v[128:131]
ds_read_b128 a[200:203], v10 offset:0
ds_read_b128 a[204:207], v10 offset:1024
s_waitcnt lgkmcnt(8)
v_mfma_f32_16x16x32_bf16 v[144:147], a[248:251], a[208:211], v[144:147]
s_add_u32 m0, s53, 0x1000
ds_read_b64_tr_b16 a[232:233], v5 offset:8192
v_mfma_f32_16x16x32_bf16 v[148:151], a[248:251], a[212:215], v[148:151]
buffer_load_dwordx4 v68, s[40:43], s67 offen lds
ds_read_b64_tr_b16 a[234:235], v5 offset:10240
v_mfma_f32_16x16x32_bf16 v[152:155], a[248:251], a[216:219], v[152:155]
v_mfma_f32_16x16x32_bf16 v[156:159], a[248:251], a[220:223], v[156:159]
v_mfma_f32_16x16x32_bf16 v[172:175], a[252:255], a[220:223], v[172:175]
buffer_load_dwordx4 v69, s[40:43], s67 offen offset:1024 lds
v_mfma_f32_16x16x32_bf16 v[168:171], a[252:255], a[216:219], v[168:171]
ds_read_b64_tr_b16 a[236:237], v6 offset:8192
ds_read_b64_tr_b16 a[238:239], v6 offset:10240
v_mfma_f32_16x16x32_bf16 v[164:167], a[252:255], a[212:215], v[164:167]
v_mfma_f32_16x16x32_bf16 v[160:163], a[252:255], a[208:211], v[160:163]
s_waitcnt lgkmcnt(4)
v_mfma_f32_16x16x32_bf16 v[112:115], a[224:227], a[192:195], v[112:115]
buffer_load_dwordx4 v70, s[40:43], s67 offen offset:2048 lds
v_mfma_f32_16x16x32_bf16 v[116:119], a[224:227], a[196:199], v[116:119]
s_waitcnt vmcnt(7)
ds_read_b64_tr_b16 a[240:241], v3 offset:12288
ds_read_b128 a[208:211], v9 offset:4096
v_mfma_f32_16x16x32_bf16 v[120:123], a[224:227], a[200:203], v[120:123]
ds_read_b64_tr_b16 a[242:243], v3 offset:14336
ds_read_b128 a[212:215], v9 offset:5120
v_mfma_f32_16x16x32_bf16 v[124:127], a[224:227], a[204:207], v[124:127]
buffer_load_dwordx4 v71, s[40:43], s67 offen offset:3072 lds
ds_read_b128 a[216:219], v10 offset:4096
v_mfma_f32_16x16x32_bf16 v[140:143], a[228:231], a[204:207], v[140:143]
ds_read_b64_tr_b16 a[244:245], v4 offset:12288
ds_read_b128 a[220:223], v10 offset:5120
v_mfma_f32_16x16x32_bf16 v[136:139], a[228:231], a[200:203], v[136:139]
ds_read_b64_tr_b16 a[246:247], v4 offset:14336
v_mfma_f32_16x16x32_bf16 v[132:135], a[228:231], a[196:199], v[132:135]
v_mfma_f32_16x16x32_bf16 v[128:131], a[228:231], a[192:195], v[128:131]
s_waitcnt lgkmcnt(8)
v_mfma_f32_16x16x32_bf16 v[144:147], a[232:235], a[192:195], v[144:147]
s_add_u32 s67, s67, 0x12000
ds_read_b64_tr_b16 a[248:249], v5 offset:12288
v_mfma_f32_16x16x32_bf16 v[148:151], a[232:235], a[196:199], v[148:151]
s_cmp_eq_u32 s67, s75
s_cselect_b32 s67, s71, s67
ds_read_b64_tr_b16 a[250:251], v5 offset:14336
v_mfma_f32_16x16x32_bf16 v[152:155], a[232:235], a[200:203], v[152:155]
s_add_u32 m0, s53, 0x2000
v_mfma_f32_16x16x32_bf16 v[156:159], a[232:235], a[204:207], v[156:159]
buffer_load_dwordx4 v64, s[40:43], s67 offen lds
v_mfma_f32_16x16x32_bf16 v[172:175], a[236:239], a[204:207], v[172:175]
ds_read_b64_tr_b16 a[252:253], v6 offset:12288
ds_read_b64_tr_b16 a[254:255], v6 offset:14336
v_mfma_f32_16x16x32_bf16 v[168:171], a[236:239], a[200:203], v[168:171]
v_mfma_f32_16x16x32_bf16 v[164:167], a[236:239], a[196:199], v[164:167]
buffer_load_dwordx4 v65, s[40:43], s67 offen offset:1024 lds
v_mfma_f32_16x16x32_bf16 v[160:163], a[236:239], a[192:195], v[160:163]
s_waitcnt lgkmcnt(4)
v_mfma_f32_16x16x32_bf16 v[112:115], a[240:243], a[208:211], v[112:115]
s_waitcnt vmcnt(6)
ds_read_b64_tr_b16 a[224:225], v3 offset:0
v_mfma_f32_16x16x32_bf16 v[116:119], a[240:243], a[212:215], v[116:119]
buffer_load_dwordx4 v66, s[40:43], s67 offen offset:2048 lds
s_add_u32 s55, s55, 0x2000
v_mfma_f32_16x16x32_bf16 v[120:123], a[240:243], a[216:219], v[120:123]
ds_read_b64_tr_b16 a[226:227], v3 offset:2048
v_mfma_f32_16x16x32_bf16 v[124:127], a[240:243], a[220:223], v[124:127]
s_cmp_eq_u32 s55, 0x12000
s_cselect_b32 s55, 0, s55
v_add_u32 v9, s55, v14
v_mfma_f32_16x16x32_bf16 v[140:143], a[244:247], a[220:223], v[140:143]
buffer_load_dwordx4 v67, s[40:43], s67 offen offset:3072 lds
v_mfma_f32_16x16x32_bf16 v[136:139], a[244:247], a[216:219], v[136:139]
ds_read_b64_tr_b16 a[228:229], v4 offset:0
v_add_u32 v10, s55, v15
ds_read_b128 a[192:195], v9 offset:0
v_mfma_f32_16x16x32_bf16 v[132:135], a[244:247], a[212:215], v[132:135]
ds_read_b64_tr_b16 a[230:231], v4 offset:2048
ds_read_b128 a[196:199], v9 offset:1024
v_mfma_f32_16x16x32_bf16 v[128:131], a[244:247], a[208:211], v[128:131]
ds_read_b128 a[200:203], v10 offset:0
ds_read_b128 a[204:207], v10 offset:1024
s_waitcnt lgkmcnt(8)
v_mfma_f32_16x16x32_bf16 v[144:147], a[248:251], a[208:211], v[144:147]
s_add_u32 m0, s53, 0x3000
ds_read_b64_tr_b16 a[232:233], v5 offset:0
v_mfma_f32_16x16x32_bf16 v[148:151], a[248:251], a[212:215], v[148:151]
buffer_load_dwordx4 v68, s[40:43], s67 offen lds
ds_read_b64_tr_b16 a[234:235], v5 offset:2048
v_mfma_f32_16x16x32_bf16 v[152:155], a[248:251], a[216:219], v[152:155]
v_mfma_f32_16x16x32_bf16 v[156:159], a[248:251], a[220:223], v[156:159]
v_mfma_f32_16x16x32_bf16 v[172:175], a[252:255], a[220:223], v[172:175]
buffer_load_dwordx4 v69, s[40:43], s67 offen offset:1024 lds
v_mfma_f32_16x16x32_bf16 v[168:171], a[252:255], a[216:219], v[168:171]
ds_read_b64_tr_b16 a[236:237], v6 offset:0
)ASM"
R"ASM(
ds_read_b64_tr_b16 a[238:239], v6 offset:2048
v_mfma_f32_16x16x32_bf16 v[164:167], a[252:255], a[212:215], v[164:167]
v_mfma_f32_16x16x32_bf16 v[160:163], a[252:255], a[208:211], v[160:163]
s_waitcnt lgkmcnt(4)
s_add_u32 s54, s54, -1
s_cbranch_scc1 .Lk2_D2
v_mfma_f32_16x16x32_bf16 v[112:115], a[224:227], a[192:195], v[112:115]
buffer_load_dwordx4 v70, s[40:43], s67 offen offset:2048 lds
v_mfma_f32_16x16x32_bf16 v[116:119], a[224:227], a[196:199], v[116:119]
s_waitcnt vmcnt(7)
ds_read_b64_tr_b16 a[240:241], v3 offset:4096
ds_read_b128 a[208:211], v9 offset:4096
v_mfma_f32_16x16x32_bf16 v[120:123], a[224:227], a[200:203], v[120:123]
ds_read_b64_tr_b16 a[242:243], v3 offset:6144
ds_read_b128 a[212:215], v9 offset:5120
v_mfma_f32_16x16x32_bf16 v[124:127], a[224:227], a[204:207], v[124:127]
buffer_load_dwordx4 v71, s[40:43], s67 offen offset:3072 lds
ds_read_b128 a[216:219], v10 offset:4096
v_mfma_f32_16x16x32_bf16 v[140:143], a[228:231], a[204:207], v[140:143]
ds_read_b64_tr_b16 a[244:245], v4 offset:4096
ds_read_b128 a[220:223], v10 offset:5120
v_mfma_f32_16x16x32_bf16 v[136:139], a[228:231], a[200:203], v[136:139]
ds_read_b64_tr_b16 a[246:247], v4 offset:6144
v_mfma_f32_16x16x32_bf16 v[132:135], a[228:231], a[196:199], v[132:135]
v_mfma_f32_16x16x32_bf16 v[128:131], a[228:231], a[192:195], v[128:131]
s_waitcnt lgkmcnt(8)
v_mfma_f32_16x16x32_bf16 v[144:147], a[232:235], a[192:195], v[144:147]
ds_read_b64_tr_b16 a[248:249], v5 offset:4096
ds_read_b64_tr_b16 a[250:251], v5 offset:6144
v_mfma_f32_16x16x32_bf16 v[148:151], a[232:235], a[196:199], v[148:151]
v_mfma_f32_16x16x32_bf16 v[152:155], a[232:235], a[200:203], v[152:155]
v_mfma_f32_16x16x32_bf16 v[156:159], a[232:235], a[204:207], v[156:159]
v_mfma_f32_16x16x32_bf16 v[172:175], a[236:239], a[204:207], v[172:175]
ds_read_b64_tr_b16 a[252:253], v6 offset:4096
ds_read_b64_tr_b16 a[254:255], v6 offset:6144
v_mfma_f32_16x16x32_bf16 v[168:171], a[236:239], a[200:203], v[168:171]
v_mfma_f32_16x16x32_bf16 v[164:167], a[236:239], a[196:199], v[164:167]
v_mfma_f32_16x16x32_bf16 v[160:163], a[236:239], a[192:195], v[160:163]
s_waitcnt lgkmcnt(4)
v_mfma_f32_16x16x32_bf16 v[112:115], a[240:243], a[208:211], v[112:115]
s_waitcnt vmcnt(4)
ds_read_b64_tr_b16 a[224:225], v3 offset:8192
v_mfma_f32_16x16x32_bf16 v[116:119], a[240:243], a[212:215], v[116:119]
ds_read_b64_tr_b16 a[226:227], v3 offset:10240
s_add_u32 s55, s55, 0x2000
v_mfma_f32_16x16x32_bf16 v[120:123], a[240:243], a[216:219], v[120:123]
s_cmp_eq_u32 s55, 0x12000
s_cselect_b32 s55, 0, s55
v_mfma_f32_16x16x32_bf16 v[124:127], a[240:243], a[220:223], v[124:127]
v_add_u32 v9, s55, v14
v_add_u32 v10, s55, v15
ds_read_b128 a[192:195], v9 offset:0
v_mfma_f32_16x16x32_bf16 v[140:143], a[244:247], a[220:223], v[140:143]
ds_read_b64_tr_b16 a[228:229], v4 offset:8192
ds_read_b128 a[196:199], v9 offset:1024
v_mfma_f32_16x16x32_bf16 v[136:139], a[244:247], a[216:219], v[136:139]
ds_read_b64_tr_b16 a[230:231], v4 offset:10240
ds_read_b128 a[200:203], v10 offset:0
ds_read_b128 a[204:207], v10 offset:1024
v_mfma_f32_16x16x32_bf16 v[132:135], a[244:247], a[212:215], v[132:135]
v_mfma_f32_16x16x32_bf16 v[128:131], a[244:247], a[208:211], v[128:131]
s_waitcnt lgkmcnt(8)
v_mfma_f32_16x16x32_bf16 v[144:147], a[248:251], a[208:211], v[144:147]
ds_read_b64_tr_b16 a[232:233], v5 offset:8192
ds_read_b64_tr_b16 a[234:235], v5 offset:10240
v_mfma_f32_16x16x32_bf16 v[148:151], a[248:251], a[212:215], v[148:151]
v_mfma_f32_16x16x32_bf16 v[152:155], a[248:251], a[216:219], v[152:155]
s_waitcnt vmcnt(0)
buffer_atomic_add v72, v73, s[12:15], 0 offen offset:768 sc1
v_mfma_f32_16x16x32_bf16 v[156:159], a[248:251], a[220:223], v[156:159]
v_mfma_f32_16x16x32_bf16 v[172:175], a[252:255], a[220:223], v[172:175]
ds_read_b64_tr_b16 a[236:237], v6 offset:8192
ds_read_b64_tr_b16 a[238:239], v6 offset:10240
v_mfma_f32_16x16x32_bf16 v[168:171], a[252:255], a[216:219], v[168:171]
v_mfma_f32_16x16x32_bf16 v[164:167], a[252:255], a[212:215], v[164:167]
v_mfma_f32_16x16x32_bf16 v[160:163], a[252:255], a[208:211], v[160:163]
s_waitcnt lgkmcnt(4)
v_mfma_f32_16x16x32_bf16 v[112:115], a[224:227], a[192:195], v[112:115]
ds_read_b64_tr_b16 a[240:241], v3 offset:12288
v_mfma_f32_16x16x32_bf16 v[116:119], a[224:227], a[196:199], v[116:119]
ds_read_b64_tr_b16 a[242:243], v3 offset:14336
ds_read_b128 a[208:211], v9 offset:4096
ds_read_b128 a[212:215], v9 offset:5120
v_mfma_f32_16x16x32_bf16 v[120:123], a[224:227], a[200:203], v[120:123]
ds_read_b128 a[216:219], v10 offset:4096
ds_read_b128 a[220:223], v10 offset:5120
v_mfma_f32_16x16x32_bf16 v[124:127], a[224:227], a[204:207], v[124:127]
v_mfma_f32_16x16x32_bf16 v[140:143], a[228:231], a[204:207], v[140:143]
ds_read_b64_tr_b16 a[244:245], v4 offset:12288
ds_read_b64_tr_b16 a[246:247], v4 offset:14336
v_mfma_f32_16x16x32_bf16 v[136:139], a[228:231], a[200:203], v[136:139]
v_mfma_f32_16x16x32_bf16 v[132:135], a[228:231], a[196:199], v[132:135]
v_mfma_f32_16x16x32_bf16 v[128:131], a[228:231], a[192:195], v[128:131]
s_waitcnt lgkmcnt(8)
v_mfma_f32_16x16x32_bf16 v[144:147], a[232:235], a[192:195], v[144:147]
ds_read_b64_tr_b16 a[248:249], v5 offset:12288
ds_read_b64_tr_b16 a[250:251], v5 offset:14336
v_mfma_f32_16x16x32_bf16 v[148:151], a[232:235], a[196:199], v[148:151]
v_mfma_f32_16x16x32_bf16 v[152:155], a[232:235], a[200:203], v[152:155]
v_mfma_f32_16x16x32_bf16 v[156:159], a[232:235], a[204:207], v[156:159]
v_mfma_f32_16x16x32_bf16 v[172:175], a[236:239], a[204:207], v[172:175]
ds_read_b64_tr_b16 a[252:253], v6 offset:12288
ds_read_b64_tr_b16 a[254:255], v6 offset:14336
v_mfma_f32_16x16x32_bf16 v[168:171], a[236:239], a[200:203], v[168:171]
v_mfma_f32_16x16x32_bf16 v[164:167], a[236:239], a[196:199], v[164:167]
v_mfma_f32_16x16x32_bf16 v[160:163], a[236:239], a[192:195], v[160:163]
s_waitcnt lgkmcnt(4)
v_mfma_f32_16x16x32_bf16 v[112:115], a[240:243], a[208:211], v[112:115]
v_mfma_f32_16x16x32_bf16 v[116:119], a[240:243], a[212:215], v[116:119]
v_mfma_f32_16x16x32_bf16 v[120:123], a[240:243], a[216:219], v[120:123]
v_mfma_f32_16x16x32_bf16 v[124:127], a[240:243], a[220:223], v[124:127]
v_mfma_f32_16x16x32_bf16 v[140:143], a[244:247], a[220:223], v[140:143]
v_mfma_f32_16x16x32_bf16 v[136:139], a[244:247], a[216:219], v[136:139]
v_mfma_f32_16x16x32_bf16 v[132:135], a[244:247], a[212:215], v[132:135]
v_mfma_f32_16x16x32_bf16 v[128:131], a[244:247], a[208:211], v[128:131]
s_waitcnt lgkmcnt(0)
v_mfma_f32_16x16x32_bf16 v[144:147], a[248:251], a[208:211], v[144:147]
v_mfma_f32_16x16x32_bf16 v[148:151], a[248:251], a[212:215], v[148:151]
v_mfma_f32_16x16x32_bf16 v[152:155], a[248:251], a[216:219], v[152:155]
v_mfma_f32_16x16x32_bf16 v[156:159], a[248:251], a[220:223], v[156:159]
v_mfma_f32_16x16x32_bf16 v[172:175], a[252:255], a[220:223], v[172:175]
v_mfma_f32_16x16x32_bf16 v[168:171], a[252:255], a[216:219], v[168:171]
v_mfma_f32_16x16x32_bf16 v[164:167], a[252:255], a[212:215], v[164:167]
v_mfma_f32_16x16x32_bf16 v[160:163], a[252:255], a[208:211], v[160:163]
v_cvt_pk_bf16_f32 v112, v112, v113
v_cvt_pk_bf16_f32 v113, v114, v115
v_cvt_pk_bf16_f32 v114, v128, v129
v_cvt_pk_bf16_f32 v115, v130, v131
buffer_store_dwordx4 v[112:115], v14, s[48:51], s78 offen sc1
v_cvt_pk_bf16_f32 v116, v116, v117
v_cvt_pk_bf16_f32 v117, v118, v119
v_cvt_pk_bf16_f32 v118, v132, v133
v_cvt_pk_bf16_f32 v119, v134, v135
buffer_store_dwordx4 v[116:119], v14, s[48:51], s78 offen offset:1024 sc1
v_cvt_pk_bf16_f32 v120, v120, v121
v_cvt_pk_bf16_f32 v121, v122, v123
v_cvt_pk_bf16_f32 v122, v136, v137
v_cvt_pk_bf16_f32 v123, v138, v139
buffer_store_dwordx4 v[120:123], v15, s[48:51], s78 offen sc1
v_cvt_pk_bf16_f32 v124, v124, v125
v_cvt_pk_bf16_f32 v125, v126, v127
v_cvt_pk_bf16_f32 v126, v140, v141
v_cvt_pk_bf16_f32 v127, v142, v143
buffer_store_dwordx4 v[124:127], v15, s[48:51], s78 offen offset:1024 sc1
v_cvt_pk_bf16_f32 v144, v144, v145
v_cvt_pk_bf16_f32 v145, v146, v147
v_cvt_pk_bf16_f32 v146, v160, v161
v_cvt_pk_bf16_f32 v147, v162, v163
buffer_store_dwordx4 v[144:147], v14, s[48:51], s96 offen sc1
v_cvt_pk_bf16_f32 v148, v148, v149
v_cvt_pk_bf16_f32 v149, v150, v151
v_cvt_pk_bf16_f32 v150, v164, v165
v_cvt_pk_bf16_f32 v151, v166, v167
buffer_store_dwordx4 v[148:151], v14, s[48:51], s96 offen offset:1024 sc1
v_cvt_pk_bf16_f32 v152, v152, v153
v_cvt_pk_bf16_f32 v153, v154, v155
v_cvt_pk_bf16_f32 v154, v168, v169
v_cvt_pk_bf16_f32 v155, v170, v171
buffer_store_dwordx4 v[152:155], v15, s[48:51], s96 offen sc1
v_cvt_pk_bf16_f32 v156, v156, v157
v_cvt_pk_bf16_f32 v157, v158, v159
v_cvt_pk_bf16_f32 v158, v172, v173
v_cvt_pk_bf16_f32 v159, v174, v175
buffer_store_dwordx4 v[156:159], v15, s[48:51], s96 offen offset:1024 sc1
.Ls2_entry:
s_lshl_b32 s29, s52, 1
s_cmp_ge_u32 s52, 2
s_addc_u32 s29, s29, 0
v_bfe_u32 v13, v1, 3, 2
v_and_b32 v38, 7, v1
v_lshrrev_b32 v39, 5, v1
v_lshlrev_b32 v19, 4, v13
v_lshl_add_u32 v19, v39, 7, v19
s_mov_b32 s58, 0x480
v_lshrrev_b32 v53, 1, v19
v_mad_u32_u24 v53, v38, s58, v53
v_lshlrev_b32 v50, 4, v38
v_lshl_add_u32 v50, v13, 8, v50
v_lshl_add_u32 v50, v39, 12, v50
s_lshl_b32 s58, s29, 8
v_add_u32 v54, s58, v19
v_add_u32 v55, 0x100, v54
v_add_u32 v19, 0x400, v19
s_lshl_b32 s58, s29, 7
v_add_u32 v51, s58, v53
v_add_u32 v52, 0x80, v51
s_cmp_eq_u32 s52, 0
s_cselect_b32 s58, 0, 0x40000000
s_add_u32 s58, s58, 0x200
v_add_u32 v53, s58, v53
s_lshl_b32 s58, s29, 13
v_add_u32 v48, s58, v50
v_add_u32 v49, 0x2000, v48
s_cmp_eq_u32 s52, 0
s_cselect_b32 s58, 0, 0x40000000
s_add_u32 s58, s58, 0x8000
v_add_u32 v50, s58, v50
s_mov_b32 s58, 0
s_add_u32 s16, s16, s58
s_addc_u32 s17, s17, 0
)ASM"
R"ASM(
s_and_b32 s17, s17, 0xffff
s_sub_u32 s18, 0x3600, s58
s_mov_b32 s19, 0x20000
s_lshl_b32 s58, s57, 3
s_add_u32 s58, s58, s56
s_mul_i32 s58, s58, 0x2400
s_add_u32 s24, s24, s58
s_addc_u32 s25, s25, 0
s_and_b32 s25, s25, 0xffff
s_sub_u32 s26, 0x240000, s58
s_mov_b32 s27, 0x20000
s_mul_i32 s58, s57, 0x12000
s_lshr_b32 s59, s56, 1
s_lshl_b32 s59, s59, 10
s_add_u32 s58, s58, s59
s_and_b32 s59, s56, 1
s_lshl_b32 s59, s59, 7
s_add_u32 s58, s58, s59
s_add_u32 s20, s20, s58
s_addc_u32 s21, s21, 0
s_and_b32 s21, s21, 0xffff
s_sub_u32 s22, 0x1200000, s58
s_mov_b32 s23, 0x20000
v_mov_b32 v255, 0
s_mov_b32 s28, 0x2d00
buffer_load_dwordx4 v[176:179], v54, s[16:19], s28 offen sc1
buffer_load_dwordx4 v[180:183], v54, s[16:19], s28 offen offset:64 sc1
buffer_load_dwordx4 v[184:187], v55, s[16:19], s28 offen sc1
buffer_load_dwordx4 v[188:191], v55, s[16:19], s28 offen offset:64 sc1
buffer_load_dwordx4 v[192:195], v19, s[16:19], s28 offen sc1
buffer_load_dwordx4 v[196:199], v19, s[16:19], s28 offen offset:64 sc1
s_mov_b32 s54, 0x20000
.Ls2_a_p:
buffer_load_dword v13, v255, s[12:15], 0 offen offset:768 sc1
s_waitcnt vmcnt(0)
v_readfirstlane_b32 s58, v13
s_nop 3
s_sub_u32 s58, s58, s7
s_cmp_ge_u32 s58, 32
s_cbranch_scc1 .Ls2_a_d
s_sleep 1
s_add_u32 s54, s54, -1
s_cbranch_scc1 .Ls2_a_p
.Ls2_a_d:
buffer_atomic_add v72, v73, s[12:15], 0 offen offset:1536 sc1
s_mov_b32 s28, 0
buffer_load_dwordx4 v[200:203], v48, s[20:23], s28 offen sc1
s_add_u32 s28, s28, 0x240000
buffer_load_dwordx4 v[204:207], v48, s[20:23], s28 offen sc1
s_add_u32 s28, s28, 0x240000
buffer_load_dwordx4 v[208:211], v48, s[20:23], s28 offen sc1
s_add_u32 s28, s28, 0x240000
buffer_load_dwordx4 v[212:215], v48, s[20:23], s28 offen sc1
s_add_u32 s28, s28, 0x240000
buffer_load_dwordx4 v[216:219], v48, s[20:23], s28 offen sc1
s_add_u32 s28, s28, 0x240000
buffer_load_dwordx4 v[220:223], v48, s[20:23], s28 offen sc1
s_add_u32 s28, s28, 0x240000
buffer_load_dwordx4 v[224:227], v48, s[20:23], s28 offen sc1
s_add_u32 s28, s28, 0x240000
buffer_load_dwordx4 v[228:231], v48, s[20:23], s28 offen sc1
s_mov_b32 s28, 0
buffer_load_dwordx4 v[232:235], v50, s[20:23], s28 offen sc1
s_add_u32 s28, s28, 0x240000
buffer_load_dwordx4 v[236:239], v50, s[20:23], s28 offen sc1
s_add_u32 s28, s28, 0x240000
buffer_load_dwordx4 v[88:91], v50, s[20:23], s28 offen sc1
s_add_u32 s28, s28, 0x240000
buffer_load_dwordx4 v[92:95], v50, s[20:23], s28 offen sc1
s_add_u32 s28, s28, 0x240000
buffer_load_dwordx4 v[96:99], v50, s[20:23], s28 offen sc1
s_add_u32 s28, s28, 0x240000
buffer_load_dwordx4 v[100:103], v50, s[20:23], s28 offen sc1
s_add_u32 s28, s28, 0x240000
buffer_load_dwordx4 v[40:43], v50, s[20:23], s28 offen sc1
s_add_u32 s28, s28, 0x240000
buffer_load_dwordx4 v[44:47], v50, s[20:23], s28 offen sc1
.Ls2_join:
s_waitcnt vmcnt(7)
v_lshlrev_b32 v104, 16, v232
v_and_b32 v105, 0xffff0000, v232
v_lshlrev_b32 v106, 16, v233
v_and_b32 v107, 0xffff0000, v233
v_lshlrev_b32 v108, 16, v234
v_and_b32 v109, 0xffff0000, v234
v_lshlrev_b32 v110, 16, v235
v_and_b32 v111, 0xffff0000, v235
s_waitcnt vmcnt(6)
v_lshlrev_b32 v39, 16, v236
v_and_b32 v248, 0xffff0000, v236
v_add_f32 v104, v104, v39
v_add_f32 v105, v105, v248
v_lshlrev_b32 v13, 16, v237
v_and_b32 v38, 0xffff0000, v237
v_add_f32 v106, v106, v13
v_add_f32 v107, v107, v38
v_lshlrev_b32 v39, 16, v238
v_and_b32 v248, 0xffff0000, v238
v_add_f32 v108, v108, v39
v_add_f32 v109, v109, v248
v_lshlrev_b32 v13, 16, v239
v_and_b32 v38, 0xffff0000, v239
v_add_f32 v110, v110, v13
v_add_f32 v111, v111, v38
s_waitcnt vmcnt(5)
v_lshlrev_b32 v39, 16, v88
v_and_b32 v248, 0xffff0000, v88
v_add_f32 v104, v104, v39
v_add_f32 v105, v105, v248
v_lshlrev_b32 v13, 16, v89
v_and_b32 v38, 0xffff0000, v89
v_add_f32 v106, v106, v13
v_add_f32 v107, v107, v38
v_lshlrev_b32 v39, 16, v90
v_and_b32 v248, 0xffff0000, v90
v_add_f32 v108, v108, v39
v_add_f32 v109, v109, v248
v_lshlrev_b32 v13, 16, v91
v_and_b32 v38, 0xffff0000, v91
v_add_f32 v110, v110, v13
v_add_f32 v111, v111, v38
s_waitcnt vmcnt(4)
v_lshlrev_b32 v39, 16, v92
v_and_b32 v248, 0xffff0000, v92
v_add_f32 v104, v104, v39
v_add_f32 v105, v105, v248
v_lshlrev_b32 v13, 16, v93
v_and_b32 v38, 0xffff0000, v93
v_add_f32 v106, v106, v13
v_add_f32 v107, v107, v38
v_lshlrev_b32 v39, 16, v94
v_and_b32 v248, 0xffff0000, v94
v_add_f32 v108, v108, v39
v_add_f32 v109, v109, v248
v_lshlrev_b32 v13, 16, v95
v_and_b32 v38, 0xffff0000, v95
v_add_f32 v110, v110, v13
v_add_f32 v111, v111, v38
s_waitcnt vmcnt(3)
v_lshlrev_b32 v39, 16, v96
v_and_b32 v248, 0xffff0000, v96
v_add_f32 v104, v104, v39
v_add_f32 v105, v105, v248
v_lshlrev_b32 v13, 16, v97
v_and_b32 v38, 0xffff0000, v97
v_add_f32 v106, v106, v13
v_add_f32 v107, v107, v38
v_lshlrev_b32 v39, 16, v98
v_and_b32 v248, 0xffff0000, v98
v_add_f32 v108, v108, v39
v_add_f32 v109, v109, v248
v_lshlrev_b32 v13, 16, v99
v_and_b32 v38, 0xffff0000, v99
v_add_f32 v110, v110, v13
v_add_f32 v111, v111, v38
s_waitcnt vmcnt(2)
v_lshlrev_b32 v39, 16, v100
v_and_b32 v248, 0xffff0000, v100
v_add_f32 v104, v104, v39
v_add_f32 v105, v105, v248
v_lshlrev_b32 v13, 16, v101
v_and_b32 v38, 0xffff0000, v101
v_add_f32 v106, v106, v13
v_add_f32 v107, v107, v38
v_lshlrev_b32 v39, 16, v102
v_and_b32 v248, 0xffff0000, v102
v_add_f32 v108, v108, v39
v_add_f32 v109, v109, v248
v_lshlrev_b32 v13, 16, v103
v_and_b32 v38, 0xffff0000, v103
v_add_f32 v110, v110, v13
v_add_f32 v111, v111, v38
s_waitcnt vmcnt(1)
v_lshlrev_b32 v39, 16, v40
v_and_b32 v248, 0xffff0000, v40
v_add_f32 v104, v104, v39
v_add_f32 v105, v105, v248
v_lshlrev_b32 v13, 16, v41
v_and_b32 v38, 0xffff0000, v41
v_add_f32 v106, v106, v13
v_add_f32 v107, v107, v38
v_lshlrev_b32 v39, 16, v42
v_and_b32 v248, 0xffff0000, v42
v_add_f32 v108, v108, v39
v_add_f32 v109, v109, v248
v_lshlrev_b32 v13, 16, v43
v_and_b32 v38, 0xffff0000, v43
v_add_f32 v110, v110, v13
v_add_f32 v111, v111, v38
s_waitcnt vmcnt(0)
v_lshlrev_b32 v39, 16, v44
v_and_b32 v248, 0xffff0000, v44
v_add_f32 v104, v104, v39
v_add_f32 v105, v105, v248
v_lshlrev_b32 v13, 16, v45
v_and_b32 v38, 0xffff0000, v45
v_add_f32 v106, v106, v13
v_add_f32 v107, v107, v38
v_lshlrev_b32 v39, 16, v46
v_and_b32 v248, 0xffff0000, v46
)ASM"
R"ASM(
v_add_f32 v108, v108, v39
v_add_f32 v109, v109, v248
v_lshlrev_b32 v13, 16, v47
v_and_b32 v38, 0xffff0000, v47
v_add_f32 v110, v110, v13
v_add_f32 v111, v111, v38
v_lshlrev_b32 v88, 16, v200
v_and_b32 v89, 0xffff0000, v200
v_lshlrev_b32 v90, 16, v201
v_and_b32 v91, 0xffff0000, v201
v_lshlrev_b32 v92, 16, v202
v_and_b32 v93, 0xffff0000, v202
v_lshlrev_b32 v94, 16, v203
v_and_b32 v95, 0xffff0000, v203
v_lshlrev_b32 v39, 16, v204
v_and_b32 v248, 0xffff0000, v204
v_add_f32 v88, v88, v39
v_add_f32 v89, v89, v248
v_lshlrev_b32 v13, 16, v205
v_and_b32 v38, 0xffff0000, v205
v_add_f32 v90, v90, v13
v_add_f32 v91, v91, v38
v_lshlrev_b32 v39, 16, v206
v_and_b32 v248, 0xffff0000, v206
v_add_f32 v92, v92, v39
v_add_f32 v93, v93, v248
v_lshlrev_b32 v13, 16, v207
v_and_b32 v38, 0xffff0000, v207
v_add_f32 v94, v94, v13
v_add_f32 v95, v95, v38
v_lshlrev_b32 v39, 16, v208
v_and_b32 v248, 0xffff0000, v208
v_add_f32 v88, v88, v39
v_add_f32 v89, v89, v248
v_lshlrev_b32 v13, 16, v209
v_and_b32 v38, 0xffff0000, v209
v_add_f32 v90, v90, v13
v_add_f32 v91, v91, v38
v_lshlrev_b32 v39, 16, v210
v_and_b32 v248, 0xffff0000, v210
v_add_f32 v92, v92, v39
v_add_f32 v93, v93, v248
v_lshlrev_b32 v13, 16, v211
v_and_b32 v38, 0xffff0000, v211
v_add_f32 v94, v94, v13
v_add_f32 v95, v95, v38
v_lshlrev_b32 v39, 16, v212
v_and_b32 v248, 0xffff0000, v212
v_add_f32 v88, v88, v39
v_add_f32 v89, v89, v248
v_lshlrev_b32 v13, 16, v213
v_and_b32 v38, 0xffff0000, v213
v_add_f32 v90, v90, v13
v_add_f32 v91, v91, v38
v_lshlrev_b32 v39, 16, v214
v_and_b32 v248, 0xffff0000, v214
v_add_f32 v92, v92, v39
v_add_f32 v93, v93, v248
v_lshlrev_b32 v13, 16, v215
v_and_b32 v38, 0xffff0000, v215
v_add_f32 v94, v94, v13
v_add_f32 v95, v95, v38
v_lshlrev_b32 v39, 16, v216
v_and_b32 v248, 0xffff0000, v216
v_add_f32 v88, v88, v39
v_add_f32 v89, v89, v248
v_lshlrev_b32 v13, 16, v217
v_and_b32 v38, 0xffff0000, v217
v_add_f32 v90, v90, v13
v_add_f32 v91, v91, v38
v_lshlrev_b32 v39, 16, v218
v_and_b32 v248, 0xffff0000, v218
v_add_f32 v92, v92, v39
v_add_f32 v93, v93, v248
v_lshlrev_b32 v13, 16, v219
v_and_b32 v38, 0xffff0000, v219
v_add_f32 v94, v94, v13
v_add_f32 v95, v95, v38
v_lshlrev_b32 v39, 16, v220
v_and_b32 v248, 0xffff0000, v220
v_add_f32 v88, v88, v39
v_add_f32 v89, v89, v248
v_lshlrev_b32 v13, 16, v221
v_and_b32 v38, 0xffff0000, v221
v_add_f32 v90, v90, v13
v_add_f32 v91, v91, v38
v_lshlrev_b32 v39, 16, v222
v_and_b32 v248, 0xffff0000, v222
v_add_f32 v92, v92, v39
v_add_f32 v93, v93, v248
v_lshlrev_b32 v13, 16, v223
v_and_b32 v38, 0xffff0000, v223
v_add_f32 v94, v94, v13
v_add_f32 v95, v95, v38
v_lshlrev_b32 v39, 16, v224
v_and_b32 v248, 0xffff0000, v224
v_add_f32 v88, v88, v39
v_add_f32 v89, v89, v248
v_lshlrev_b32 v13, 16, v225
v_and_b32 v38, 0xffff0000, v225
v_add_f32 v90, v90, v13
v_add_f32 v91, v91, v38
v_lshlrev_b32 v39, 16, v226
v_and_b32 v248, 0xffff0000, v226
v_add_f32 v92, v92, v39
v_add_f32 v93, v93, v248
v_lshlrev_b32 v13, 16, v227
v_and_b32 v38, 0xffff0000, v227
v_add_f32 v94, v94, v13
v_add_f32 v95, v95, v38
v_lshlrev_b32 v39, 16, v228
v_and_b32 v248, 0xffff0000, v228
v_add_f32 v88, v88, v39
v_add_f32 v89, v89, v248
v_lshlrev_b32 v13, 16, v229
v_and_b32 v38, 0xffff0000, v229
v_add_f32 v90, v90, v13
v_add_f32 v91, v91, v38
v_lshlrev_b32 v39, 16, v230
v_and_b32 v248, 0xffff0000, v230
v_add_f32 v92, v92, v39
v_add_f32 v93, v93, v248
v_lshlrev_b32 v13, 16, v231
v_and_b32 v38, 0xffff0000, v231
v_add_f32 v94, v94, v13
v_add_f32 v95, v95, v38
s_mov_b32 s54, 0x20000
.Ls2_b_p:
buffer_load_dword v13, v255, s[12:15], 0 offen offset:1536 sc1
s_waitcnt vmcnt(0)
v_readfirstlane_b32 s58, v13
s_nop 3
s_sub_u32 s58, s58, s10
s_cmp_ge_u32 s58, 32
s_cbranch_scc1 .Ls2_b_d
s_sleep 1
s_add_u32 s54, s54, -1
s_cbranch_scc1 .Ls2_b_p
.Ls2_b_d:
s_mov_b32 s28, 0
buffer_load_dwordx4 v[200:203], v49, s[20:23], s28 offen sc1
s_add_u32 s28, s28, 0x240000
buffer_load_dwordx4 v[204:207], v49, s[20:23], s28 offen sc1
s_add_u32 s28, s28, 0x240000
buffer_load_dwordx4 v[208:211], v49, s[20:23], s28 offen sc1
s_add_u32 s28, s28, 0x240000
buffer_load_dwordx4 v[212:215], v49, s[20:23], s28 offen sc1
s_add_u32 s28, s28, 0x240000
buffer_load_dwordx4 v[216:219], v49, s[20:23], s28 offen sc1
s_add_u32 s28, s28, 0x240000
buffer_load_dwordx4 v[220:223], v49, s[20:23], s28 offen sc1
s_add_u32 s28, s28, 0x240000
buffer_load_dwordx4 v[224:227], v49, s[20:23], s28 offen sc1
s_add_u32 s28, s28, 0x240000
buffer_load_dwordx4 v[228:231], v49, s[20:23], s28 offen sc1
v_lshlrev_b32 v39, 16, v56
v_and_b32 v248, 0xffff0000, v56
v_fmac_f32 v39, v176, v88
v_fmac_f32 v248, v177, v89
v_cvt_pk_bf16_f32 v76, v39, v248
v_lshlrev_b32 v13, 16, v57
v_and_b32 v38, 0xffff0000, v57
v_fmac_f32 v13, v178, v90
v_fmac_f32 v38, v179, v91
v_cvt_pk_bf16_f32 v77, v13, v38
v_lshlrev_b32 v39, 16, v58
v_and_b32 v248, 0xffff0000, v58
v_fmac_f32 v39, v180, v92
v_fmac_f32 v248, v181, v93
v_cvt_pk_bf16_f32 v78, v39, v248
v_lshlrev_b32 v13, 16, v59
v_and_b32 v38, 0xffff0000, v59
v_fmac_f32 v13, v182, v94
v_fmac_f32 v38, v183, v95
v_cvt_pk_bf16_f32 v79, v13, v38
buffer_store_dwordx2 v[76:77], v51, s[24:27], 0 offen
buffer_store_dwordx2 v[78:79], v51, s[24:27], 0 offen offset:32
v_lshlrev_b32 v39, 16, v34
v_and_b32 v248, 0xffff0000, v34
v_fmac_f32 v39, v192, v104
v_fmac_f32 v248, v193, v105
v_cvt_pk_bf16_f32 v84, v39, v248
v_lshlrev_b32 v13, 16, v35
v_and_b32 v38, 0xffff0000, v35
v_fmac_f32 v13, v194, v106
v_fmac_f32 v38, v195, v107
v_cvt_pk_bf16_f32 v85, v13, v38
v_lshlrev_b32 v39, 16, v36
v_and_b32 v248, 0xffff0000, v36
v_fmac_f32 v39, v196, v108
v_fmac_f32 v248, v197, v109
v_cvt_pk_bf16_f32 v86, v39, v248
v_lshlrev_b32 v13, 16, v37
v_and_b32 v38, 0xffff0000, v37
v_fmac_f32 v13, v198, v110
v_fmac_f32 v38, v199, v111
v_cvt_pk_bf16_f32 v87, v13, v38
buffer_store_dwordx2 v[84:85], v53, s[24:27], 0 offen
buffer_store_dwordx2 v[86:87], v53, s[24:27], 0 offen offset:32
s_waitcnt vmcnt(11)
)ASM"
R"ASM(
v_lshlrev_b32 v96, 16, v200
v_and_b32 v97, 0xffff0000, v200
v_lshlrev_b32 v98, 16, v201
v_and_b32 v99, 0xffff0000, v201
v_lshlrev_b32 v100, 16, v202
v_and_b32 v101, 0xffff0000, v202
v_lshlrev_b32 v102, 16, v203
v_and_b32 v103, 0xffff0000, v203
s_waitcnt vmcnt(10)
v_lshlrev_b32 v39, 16, v204
v_and_b32 v248, 0xffff0000, v204
v_add_f32 v96, v96, v39
v_add_f32 v97, v97, v248
v_lshlrev_b32 v13, 16, v205
v_and_b32 v38, 0xffff0000, v205
v_add_f32 v98, v98, v13
v_add_f32 v99, v99, v38
v_lshlrev_b32 v39, 16, v206
v_and_b32 v248, 0xffff0000, v206
v_add_f32 v100, v100, v39
v_add_f32 v101, v101, v248
v_lshlrev_b32 v13, 16, v207
v_and_b32 v38, 0xffff0000, v207
v_add_f32 v102, v102, v13
v_add_f32 v103, v103, v38
s_waitcnt vmcnt(9)
v_lshlrev_b32 v39, 16, v208
v_and_b32 v248, 0xffff0000, v208
v_add_f32 v96, v96, v39
v_add_f32 v97, v97, v248
v_lshlrev_b32 v13, 16, v209
v_and_b32 v38, 0xffff0000, v209
v_add_f32 v98, v98, v13
v_add_f32 v99, v99, v38
v_lshlrev_b32 v39, 16, v210
v_and_b32 v248, 0xffff0000, v210
v_add_f32 v100, v100, v39
v_add_f32 v101, v101, v248
v_lshlrev_b32 v13, 16, v211
v_and_b32 v38, 0xffff0000, v211
v_add_f32 v102, v102, v13
v_add_f32 v103, v103, v38
s_waitcnt vmcnt(8)
v_lshlrev_b32 v39, 16, v212
v_and_b32 v248, 0xffff0000, v212
v_add_f32 v96, v96, v39
v_add_f32 v97, v97, v248
v_lshlrev_b32 v13, 16, v213
v_and_b32 v38, 0xffff0000, v213
v_add_f32 v98, v98, v13
v_add_f32 v99, v99, v38
v_lshlrev_b32 v39, 16, v214
v_and_b32 v248, 0xffff0000, v214
v_add_f32 v100, v100, v39
v_add_f32 v101, v101, v248
v_lshlrev_b32 v13, 16, v215
v_and_b32 v38, 0xffff0000, v215
v_add_f32 v102, v102, v13
v_add_f32 v103, v103, v38
s_waitcnt vmcnt(7)
v_lshlrev_b32 v39, 16, v216
v_and_b32 v248, 0xffff0000, v216
v_add_f32 v96, v96, v39
v_add_f32 v97, v97, v248
v_lshlrev_b32 v13, 16, v217
v_and_b32 v38, 0xffff0000, v217
v_add_f32 v98, v98, v13
v_add_f32 v99, v99, v38
v_lshlrev_b32 v39, 16, v218
v_and_b32 v248, 0xffff0000, v218
v_add_f32 v100, v100, v39
v_add_f32 v101, v101, v248
v_lshlrev_b32 v13, 16, v219
v_and_b32 v38, 0xffff0000, v219
v_add_f32 v102, v102, v13
v_add_f32 v103, v103, v38
s_waitcnt vmcnt(6)
v_lshlrev_b32 v39, 16, v220
v_and_b32 v248, 0xffff0000, v220
v_add_f32 v96, v96, v39
v_add_f32 v97, v97, v248
v_lshlrev_b32 v13, 16, v221
v_and_b32 v38, 0xffff0000, v221
v_add_f32 v98, v98, v13
v_add_f32 v99, v99, v38
v_lshlrev_b32 v39, 16, v222
v_and_b32 v248, 0xffff0000, v222
v_add_f32 v100, v100, v39
v_add_f32 v101, v101, v248
v_lshlrev_b32 v13, 16, v223
v_and_b32 v38, 0xffff0000, v223
v_add_f32 v102, v102, v13
v_add_f32 v103, v103, v38
s_waitcnt vmcnt(5)
v_lshlrev_b32 v39, 16, v224
v_and_b32 v248, 0xffff0000, v224
v_add_f32 v96, v96, v39
v_add_f32 v97, v97, v248
v_lshlrev_b32 v13, 16, v225
v_and_b32 v38, 0xffff0000, v225
v_add_f32 v98, v98, v13
v_add_f32 v99, v99, v38
v_lshlrev_b32 v39, 16, v226
v_and_b32 v248, 0xffff0000, v226
v_add_f32 v100, v100, v39
v_add_f32 v101, v101, v248
v_lshlrev_b32 v13, 16, v227
v_and_b32 v38, 0xffff0000, v227
v_add_f32 v102, v102, v13
v_add_f32 v103, v103, v38
s_waitcnt vmcnt(4)
v_lshlrev_b32 v39, 16, v228
v_and_b32 v248, 0xffff0000, v228
v_add_f32 v96, v96, v39
v_add_f32 v97, v97, v248
v_lshlrev_b32 v13, 16, v229
v_and_b32 v38, 0xffff0000, v229
v_add_f32 v98, v98, v13
v_add_f32 v99, v99, v38
v_lshlrev_b32 v39, 16, v230
v_and_b32 v248, 0xffff0000, v230
v_add_f32 v100, v100, v39
v_add_f32 v101, v101, v248
v_lshlrev_b32 v13, 16, v231
v_and_b32 v38, 0xffff0000, v231
v_add_f32 v102, v102, v13
v_add_f32 v103, v103, v38
v_lshlrev_b32 v39, 16, v60
v_and_b32 v248, 0xffff0000, v60
v_fmac_f32 v39, v184, v96
v_fmac_f32 v248, v185, v97
v_cvt_pk_bf16_f32 v80, v39, v248
v_lshlrev_b32 v13, 16, v61
v_and_b32 v38, 0xffff0000, v61
v_fmac_f32 v13, v186, v98
v_fmac_f32 v38, v187, v99
v_cvt_pk_bf16_f32 v81, v13, v38
v_lshlrev_b32 v39, 16, v62
v_and_b32 v248, 0xffff0000, v62
v_fmac_f32 v39, v188, v100
v_fmac_f32 v248, v189, v101
v_cvt_pk_bf16_f32 v82, v39, v248
v_lshlrev_b32 v13, 16, v63
v_and_b32 v38, 0xffff0000, v63
v_fmac_f32 v13, v190, v102
v_fmac_f32 v38, v191, v103
v_cvt_pk_bf16_f32 v83, v13, v38
buffer_store_dwordx2 v[80:81], v52, s[24:27], 0 offen
buffer_store_dwordx2 v[82:83], v52, s[24:27], 0 offen offset:32
.Lend:
)ASM"
  :: "s"(__builtin_amdgcn_kernarg_segment_ptr()), "v"(threadIdx.x), "s"(blockIdx.x), "s"((unsigned)(size_t)lds) : ASM_CLOB);
}

constexpr size_t WS_MOD = 0, WS_XH = 16384, WS_FL = WS_XH + S_ * D_ * 2, WS_H2 = WS_FL + S_ * D_ * 2, WS_P1 = WS_H2 + S_ * D_ * 2,
                 WS_P2 = WS_P1 + (size_t)8 * S_ * D_ * 2, WS_END = WS_P2 + (size_t)8 * S_ * D_ * 2;

extern "C" void kernel_launch(void* const* d_in, const int* in_sizes, int n_in, void* d_out, int out_size, void* d_ws, size_t ws_size,
                              hipStream_t stream) {
  char* ws = (char*)d_ws;
  kmain<<<256, 256, 0, stream>>>(ws + WS_XH, ws + WS_MOD, d_in[3], d_in[1], d_in[2], d_in[4], ws + WS_P1, ws + WS_END, d_in[6], d_in[7], d_in[8],
                                 d_in[0], ws + WS_H2, ws + WS_P2, d_out, ws + WS_FL, d_in[9], d_in[10], ws + WS_FL + 262144);
}
```
